```python
import torch

def _softmax(x, axis=-1):
    return torch.softmax(x, dim=axis)
def _gelu(x):
    return torch.nn.functional.gelu(x, approximate="tanh")
def _silu(x):
    return torch.nn.functional.silu(x)
def _sigmoid(x):
    return torch.sigmoid(x)
def _layernorm(x, axis=-1, eps=1e-5):
    m = x.mean(dim=axis, keepdim=True)
    v = x.var(dim=axis, keepdim=True, unbiased=False)
    return (x - m) / torch.sqrt(v + eps)
def _scatter_add(tgt, idx, src, axis=0):
    return tgt.clone().index_add_(axis, idx, src)
def _segment_sum(src, seg, n):
    out = torch.zeros((n,) + src.shape[1:], dtype=src.dtype)
    return out.index_add_(0, seg, src)
def _pad(x, pw):
    flat = tuple(v for pair in reversed(pw) for v in pair)
    return torch.nn.functional.pad(x, flat)

kernel_name = 'recurrent_scan__ssm_conv_s5__dense_activation_silu__prompt'

def setup_inputs():

    _w = (lambda a: (lambda t: t.to(torch.bfloat16) if t.is_floating_point() else t)(torch.as_tensor(a)))
    def _A_setup():
        B, S, D = 1, 2048, 512
        P = D
        Lambda_re = -0.5 * (lambda _x: torch.exp(torch.as_tensor(_x)))((lambda *s: torch.randn(*s, dtype=torch.bfloat16) * 0.8)(P))
        Lambda_im = (lambda *s: torch.randn(*s, dtype=torch.bfloat16) * 0.8)(P) * 1.0
        B_re = (lambda *s: torch.randn(*s, dtype=torch.bfloat16) * 0.8)(P, D) * (D ** -0.5)
        B_im = (lambda *s: torch.randn(*s, dtype=torch.bfloat16) * 0.8)(P, D) * (D ** -0.5)
        C_re = (lambda *s: torch.randn(*s, dtype=torch.bfloat16) * 0.8)(D, P) * (P ** -0.5)
        C_im = (lambda *s: torch.randn(*s, dtype=torch.bfloat16) * 0.8)(D, P) * (P ** -0.5)
        log_dt = (lambda *s: torch.randn(*s, dtype=torch.bfloat16) * 0.8)(P) * 0.3 - 2.0
        Dskip = (lambda *s: torch.randn(*s, dtype=torch.bfloat16) * 0.8)(D)
        return {
            "x": _w((lambda *s: torch.randn(*s, dtype=torch.bfloat16) * 0.05)(B, S, D)),
            "Lambda_re": _w(Lambda_re),
            "Lambda_im": _w(Lambda_im),
            "B_re": _w(B_re),
            "B_im": _w(B_im),
            "C_re": _w(C_re),
            "C_im": _w(C_im),
            "log_dt": _w(log_dt),
            "Dskip": _w(Dskip),
        }
    _A_in = _A_setup()

    def _B_setup():
        B, S, D, F = 1, 2048, 512, 1792
        return {
            "x": _w((lambda *s: torch.randn(*s, dtype=torch.bfloat16) * 0.05)(B, S, D)),
            "w1": _w((lambda *s: torch.randn(*s, dtype=torch.bfloat16) * 0.8)(D, F) * (D ** -0.5)),
            "w2": _w((lambda *s: torch.randn(*s, dtype=torch.bfloat16) * 0.2)(F, D) * (F ** -0.5)),
        }
    _B_in = _B_setup()

    return {
        "A_x": _A_in["x"],
        "A_Lambda_re": _A_in["Lambda_re"],
        "A_Lambda_im": _A_in["Lambda_im"],
        "A_B_re": _A_in["B_re"],
        "A_B_im": _A_in["B_im"],
        "A_C_re": _A_in["C_re"],
        "A_C_im": _A_in["C_im"],
        "A_log_dt": _A_in["log_dt"],
        "A_Dskip": _A_in["Dskip"],
        "B_x": _B_in["x"],
        "B_w1": _B_in["w1"],
        "B_w2": _B_in["w2"],
    }

def reference(A_x, A_Lambda_re, A_Lambda_im, A_B_re, A_B_im, A_C_re, A_C_im, A_log_dt, A_Dskip, B_x, B_w1, B_w2):
    def _A_ref(x, Lambda_re, Lambda_im, B_re, B_im, C_re, C_im, log_dt, Dskip):
        Bsz, L, H = x.shape
        Lam = Lambda_re + 1j * Lambda_im
        Bmat = B_re + 1j * B_im
        Cmat = C_re + 1j * C_im
        dt = torch.exp(log_dt)
        Lam_bar = torch.exp(dt * Lam)
        B_bar = ((Lam_bar - 1.0) / Lam)[:, None] * Bmat
        Bu = torch.einsum("bld,pd->blp", x + 0j, B_bar)
        h = torch.zeros_like(Bu[:, 0, :])
        hs = []
        for t in range(L):
            h = Lam_bar[None, :] * h + Bu[:, t, :]
            hs.append(h)
        hseq = torch.stack(hs, 1)
        y = 2.0 * torch.einsum("blp,dp->bld", hseq, Cmat).real + x * Dskip[None, None, :]
        return y

    def _B_ref(x, w1, w2):
        h = torch.einsum("bsd,df->bsf", x, w1)
        h = _silu(h)
        out = torch.einsum("bsf,fd->bsd", h, w2)
        return out

    x = A_x
    xn = _layernorm(x, axis=-1)
    s = _A_ref(xn, A_Lambda_re, A_Lambda_im, A_B_re, A_B_im, A_C_re, A_C_im, A_log_dt, A_Dskip)
    g = _silu(_B_ref(xn, B_w1, B_w2))
    return x + s * g
```

```cpp
#include <hip/hip_runtime.h>
#include <hip/hip_bf16.h>
typedef __hip_bfloat16 bf16;
static constexpr int L=2048, D=512, P=512, F=1792;
__device__ inline float b2f(bf16 v){ return __bfloat162float(v); }
__device__ inline float rbf(float v){ return __bfloat162float(__float2bfloat16(v)); }

__global__ void k_ln(const bf16* x, float* xn){
  int t=blockIdx.x; int tid=threadIdx.x;
  float v[8]; float s=0;
  for(int i=0;i<8;i++){ v[i]=b2f(x[t*D+tid*8+i]); s+=v[i]; }
  for(int o=32;o;o>>=1) s+=__shfl_xor(s,o);
  float m=s/D; float q=0;
  for(int i=0;i<8;i++){ float d=v[i]-m; q+=d*d; }
  for(int o=32;o;o>>=1) q+=__shfl_xor(q,o);
  float r=rsqrtf(q/D+1e-5f);
  for(int i=0;i<8;i++) xn[t*D+tid*8+i]=(v[i]-m)*r;
}
__global__ void k_u(const float* xn,const bf16* Bre,const bf16* Bim,float2* U){
  int t=blockIdx.x; __shared__ float xs[D];
  for(int i=threadIdx.x;i<D;i+=blockDim.x) xs[i]=xn[t*D+i];
  __syncthreads();
  for(int p=threadIdx.x;p<P;p+=blockDim.x){ float a=0,b=0;
    for(int d=0;d<D;d++){ a+=xs[d]*b2f(Bre[p*D+d]); b+=xs[d]*b2f(Bim[p*D+d]); }
    U[t*P+p]=make_float2(a,b); }
}
__global__ void k_scan(const bf16* Lre,const bf16* Lim,const bf16* logdt,float2* U){
  int p=blockIdx.x*blockDim.x+threadIdx.x; if(p>=P) return;
  float dt=rbf(expf(b2f(logdt[p]))); float lr=b2f(Lre[p]), li=b2f(Lim[p]);
  float e=expf(dt*lr); float ar=e*cosf(dt*li), ai=e*sinf(dt*li);
  float nr=ar-1.f, ni=ai; float den=lr*lr+li*li;
  float cr=(nr*lr+ni*li)/den, ci=(ni*lr-nr*li)/den;
  float hr=0,hi=0;
  for(int t=0;t<L;t++){ float2 u=U[t*P+p]; float ur=cr*u.x-ci*u.y, ui=cr*u.y+ci*u.x;
    float n_r=ar*hr-ai*hi+ur, n_i=ar*hi+ai*hr+ui; hr=n_r; hi=n_i; U[t*P+p]=make_float2(hr,hi); }
}
__global__ void k_h1(const float* xn,const bf16* w1,float* H1){
  int t=blockIdx.x; __shared__ float xs[D];
  for(int i=threadIdx.x;i<D;i+=blockDim.x) xs[i]=xn[t*D+i];
  __syncthreads();
  for(int f=threadIdx.x;f<F;f+=blockDim.x){ float a=0; for(int d=0;d<D;d++) a+=xs[d]*b2f(w1[d*F+f]);
    H1[t*F+f]=a/(1.f+expf(-a)); }
}
__global__ void k_out(const bf16* x,const float* xn,const float2* H,const float* H1,const bf16* Cre,const bf16* Cim,const bf16* w2,const bf16* Dsk,float* out){
  int t=blockIdx.x; __shared__ float hs[F]; __shared__ float2 hh[P];
  for(int i=threadIdx.x;i<F;i+=blockDim.x) hs[i]=H1[t*F+i];
  for(int i=threadIdx.x;i<P;i+=blockDim.x) hh[i]=H[t*P+i];
  __syncthreads();
  for(int d=threadIdx.x;d<D;d+=blockDim.x){ float y=0,f=0;
    for(int p=0;p<P;p++) y+=hh[p].x*b2f(Cre[d*P+p])-hh[p].y*b2f(Cim[d*P+p]);
    for(int k=0;k<F;k++) f+=hs[k]*b2f(w2[k*D+d]);
    float s=2.f*y+xn[t*D+d]*b2f(Dsk[d]); float g=f/(1.f+expf(-f));
    out[t*D+d]=b2f(x[t*D+d])+s*g; }
}
extern "C" void kernel_launch(void* const* d_in, const int* in_sizes, int n_in, void* d_out, int out_size, void* d_ws, size_t ws_size, hipStream_t stream){
  const bf16* x=(const bf16*)d_in[0]; const bf16* Lre=(const bf16*)d_in[1]; const bf16* Lim=(const bf16*)d_in[2];
  const bf16* Bre=(const bf16*)d_in[3]; const bf16* Bim=(const bf16*)d_in[4]; const bf16* Cre=(const bf16*)d_in[5]; const bf16* Cim=(const bf16*)d_in[6];
  const bf16* logdt=(const bf16*)d_in[7]; const bf16* Dsk=(const bf16*)d_in[8]; const bf16* w1=(const bf16*)d_in[10]; const bf16* w2=(const bf16*)d_in[11];
  float* xn=(float*)d_ws; float2* U=(float2*)(xn+L*D); float* H1=(float*)(U+L*P);
  k_ln<<<L,64,0,stream>>>(x,xn);
  k_u<<<L,256,0,stream>>>(xn,Bre,Bim,U);
  k_scan<<<P/64,64,0,stream>>>(Lre,Lim,logdt,U);
  k_h1<<<L,256,0,stream>>>(xn,w1,H1);
  k_out<<<L,256,0,stream>>>(x,xn,U,H1,Cre,Cim,w2,Dsk,(float*)d_out);
}
```

```cpp
#include <hip/hip_runtime.h>
#include <hip/hip_bf16.h>
#include <type_traits>

#define FUSED 0

typedef unsigned v4u __attribute__((ext_vector_type(4)));
typedef unsigned v2u __attribute__((ext_vector_type(2)));
typedef float f32x4 __attribute__((ext_vector_type(4)));
typedef float f32x2 __attribute__((ext_vector_type(2)));
typedef short bf16x8 __attribute__((ext_vector_type(8)));
typedef short s16x4 __attribute__((ext_vector_type(4)));
typedef __bf16 bf16x2_t __attribute__((ext_vector_type(2)));
typedef unsigned long long u64;
#define LAS __attribute__((address_space(3)))
#define GAS __attribute__((address_space(1)))
#define DI __device__ __forceinline__
#define RLX_AGENT __ATOMIC_RELAXED, __HIP_MEMORY_SCOPE_AGENT

static constexpr int L_ = 2048, D_ = 512, P_ = 512, F_ = 1792;
static constexpr int NKS = 88;
static constexpr size_t WS_Z = 0;
static constexpr size_t WS_Z_BYTES = (size_t)32 * NKS * 4096;
static constexpr size_t WS_STAT = WS_Z + WS_Z_BYTES;
static constexpr size_t WS_GRAN = WS_STAT + 2048 * 8;
static constexpr size_t WS_FLAG = WS_GRAN + 16 * 16 * 512;
static constexpr size_t WS_EPOCH = WS_FLAG + 16 * 16 * 8;
static constexpr size_t WS_END = WS_EPOCH + 16 * 128;

static constexpr int LDS_BYTES = 160 * 1024;
static constexpr int L1_XS = 0;
static constexpr int L1_WU = 16 * 1024;
static constexpr int L1_W1 = 80 * 1024;
static constexpr int L1_STAT = 0;
static constexpr int L1_HT = 512;
static constexpr int L1_SU = 3072;
static constexpr int L1_SH = 3072 + 256;
static constexpr int L1_E = 4096 + 3072;

DI f32x4 mfma16(v4u a, v4u b, f32x4 c) {
  return __builtin_amdgcn_mfma_f32_16x16x32_bf16(__builtin_bit_cast(bf16x8, a), __builtin_bit_cast(bf16x8, b), c, 0, 0, 0);
}
DI unsigned pk_bf16(float lo, float hi) {
  f32x2 v = {lo, hi};
  return __builtin_bit_cast(unsigned, __builtin_convertvector(v, bf16x2_t));
}
DI float bf2f(unsigned short u) { return __builtin_bit_cast(float, (unsigned)u << 16); }

template <int IMM> DI void gld16(v4u& d, unsigned voff, const void* sb) {
  asm volatile("global_load_dwordx4 %0, %1, %2 offset:%3" : "=v"(d) : "v"(voff), "s"(sb), "i"(IMM) : "memory");
}
template <int IMM> DI void gld16_sc1(v4u& d, unsigned voff, const void* sb) {
  asm volatile("global_load_dwordx4 %0, %1, %2 offset:%3 sc1" : "=v"(d) : "v"(voff), "s"(sb), "i"(IMM) : "memory");
}
DI void gld_u16(unsigned& d, unsigned voff, const void* sb) {
  asm volatile("global_load_ushort %0, %1, %2" : "=v"(d) : "v"(voff), "s"(sb) : "memory");
}
DI void gld8_sc1(v2u& d, unsigned voff, const void* sb) {
  asm volatile("global_load_dwordx2 %0, %1, %2 sc1" : "=v"(d) : "v"(voff), "s"(sb) : "memory");
}
DI void dma4(unsigned lds, const void* sb, unsigned v0, unsigned v1, unsigned v2, unsigned v3) {
  asm volatile(
      "s_mov_b32 m0, %0\n\ts_nop 0\n\t"
      "global_load_lds_dwordx4 %2, %1\n\t"
      "global_load_lds_dwordx4 %3, %1 offset:1024\n\t"
      "global_load_lds_dwordx4 %4, %1 offset:2048\n\t"
      "global_load_lds_dwordx4 %5, %1 offset:3072"
      :: "s"(lds), "s"(sb), "v"(v0), "v"(v1), "v"(v2), "v"(v3) : "memory");
}
template <int N> DI void wait_vm() { asm volatile("s_waitcnt vmcnt(%0)" :: "i"(N) : "memory"); }
template <int N> DI void wait_vm4(v4u& a, v4u& b, v4u& c, v4u& d) {
  asm volatile("s_waitcnt vmcnt(%4)" : "+v"(a), "+v"(b), "+v"(c), "+v"(d) : "i"(N) : "memory");
}
DI void bar() { asm volatile("s_waitcnt lgkmcnt(0)" ::: "memory"); __builtin_amdgcn_s_barrier(); asm volatile("" ::: "memory"); }

DI v4u lds_rd16(LAS char* p) { return *(LAS v4u*)p; }
DI v4u lds_tr(LAS char* p0, LAS char* p1) {
  s16x4 a = __builtin_amdgcn_ds_read_tr16_b64_v4i16((LAS s16x4*)p0);
  s16x4 b = __builtin_amdgcn_ds_read_tr16_b64_v4i16((LAS s16x4*)p1);
  bf16x8 f = __builtin_shufflevector(a, b, 0, 1, 2, 3, 4, 5, 6, 7);
  return __builtin_bit_cast(v4u, f);
}
struct cpx { float r, i; };
DI cpx cmul(cpx a, cpx b) { return {a.r * b.r - a.i * b.i, a.r * b.i + a.i * b.r}; }
DI cpx cfma(cpx a, cpx h, cpx u) {
  return {fmaf(a.r, h.r, fmaf(-a.i, h.i, u.r)), fmaf(a.r, h.i, fmaf(a.i, h.r, u.i))};
}
DI cpx csel(bool c, cpx a, cpx b) { return {c ? a.r : b.r, c ? a.i : b.i}; }

struct Params {
  const char *x, *lre, *lim, *bre, *bim, *cre, *cim, *logdt, *dskip, *w1, *w2;
  float* out;
  char* ws;
};

template <bool ONE> DI void st16(char* ws, __amdgpu_buffer_rsrc_t rs, unsigned off, v4u v) {
  __builtin_amdgcn_raw_buffer_store_b128(v, rs, (int)off, 0, ONE ? 16 : 0);
}
template <bool ONE> DI void st8(char* ws, __amdgpu_buffer_rsrc_t rs, unsigned off, v2u v) {
  __builtin_amdgcn_raw_buffer_store_b64(v, rs, (int)off, 0, ONE ? 16 : 0);
}

template <bool ONE>
DI void phase1(const Params& p, LAS char* lds, u64& epoch_out) {
  const int tid = threadIdx.x, l = tid & 63;
  const int w = __builtin_amdgcn_readfirstlane(tid >> 6);
  const int bid = blockIdx.x, xcd = bid & 7, r = bid >> 3;
  const int j = 4 * (xcd >> 1) + (r >> 3);
  const int s = 8 * (xcd & 1) + (r & 7);
  const int odd = s & 1;
  const int i16 = l & 15, q = l >> 4;
  const unsigned ldsb = (unsigned)(size_t)lds;
  const __amdgpu_buffer_rsrc_t rs = __builtin_amdgcn_make_buffer_rsrc((void*)p.ws, 0, (int)WS_END, 0x00020000);

  unsigned plr[2], pli[2], pdt[2];
  v2u ep;
  {
    const unsigned co = (unsigned)(32 * s + i16) * 2;
    gld_u16(plr[0], co, p.lre); gld_u16(plr[1], co + 32, p.lre);
    gld_u16(pli[0], co, p.lim); gld_u16(pli[1], co + 32, p.lim);
    gld_u16(pdt[0], co, p.logdt); gld_u16(pdt[1], co + 32, p.logdt);
    gld8_sc1(ep, 0u, p.ws + WS_EPOCH + s * 128);
  }
  v4u xl[4][2][4];
  const unsigned xvoff = (unsigned)(q * 1024 + i16 * 16);
  const char* xb = p.x + (size_t)(128 * j + 32 * w) * 1024;
  unsigned wuv[4];
  {
#pragma unroll
    for (int n = 0; n < 4; n++) {
      const int ch = 16 * (w & 1) + 4 * n + q;
      const int c16 = i16 ^ ((4 * n + q) & 15);
      wuv[n] = (unsigned)(ch * 1024 + c16 * 16 - 1024 * n);
    }
  }
  const char* wub = ((w >> 1) ? p.bim : p.bre) + (size_t)(32 * s) * 1024;
#define XQ(QK)                                                                            \
  {                                                                                       \
    _Pragma("unroll") for (int b = 0; b < 2; b++) _Pragma("unroll") for (int m = 0; m < 4; m++) \
        gld16<QK * 256>(xl[QK][b][m], xvoff, xb + (8 * m + 4 * b) * 1024);                \
    dma4(ldsb + L1_WU + QK * 16384 + w * 4096, wub + QK * 256, wuv[0], wuv[1], wuv[2], wuv[3]); \
  }
  XQ(0) XQ(1) XQ(2) XQ(3)
#undef XQ
  unsigned w1v[4];
  {
    const int winoff = 448 * (s >> 1) + (odd ? 192 : 0);
#pragma unroll
    for (int n = 0; n < 4; n++) {
      const int row = 16 * w + 4 * n + q;
      const int f = q | (((n >> 1) & 1) << 2);
      const int c16 = (((i16 >> 1) ^ f) << 1) | (i16 & 1);
      w1v[n] = (unsigned)(row * 3584 + winoff + c16 * 16 - 1024 * n);
    }
  }
#pragma unroll
  for (int c = 0; c < 5; c++)
    dma4(ldsb + L1_W1 + c * 16384 + w * 4096, p.w1 + (size_t)c * 64 * 3584, w1v[0], w1v[1], w1v[2], w1v[3]);

  asm volatile("s_waitcnt vmcnt(63)" : "+v"(plr[0]), "+v"(plr[1]), "+v"(pli[0]), "+v"(pli[1]), "+v"(pdt[0]), "+v"(pdt[1]), "+v"(ep) :: "memory");
  const u64 epoch = (((u64)ep.y << 32) | ep.x) + 1ull;
  epoch_out = epoch;
  cpx a1[2], cc[2], a8[2], pw[2], a128[2];
#pragma unroll
  for (int nb = 0; nb < 2; nb++) {
    const float lr = bf2f((unsigned short)plr[nb]), li = bf2f((unsigned short)pli[nb]);
    const float dtf = expf(bf2f((unsigned short)pdt[nb]));
    const float dt = bf2f((unsigned short)(pk_bf16(dtf, 0.f) & 0xffffu));
    const float e = expf(dt * lr);
    float sn, cs;
    sincosf(dt * li, &sn, &cs);
    cpx a = {e * cs, e * sn};
    a1[nb] = a;
    const float nr = a.r - 1.f, ni = a.i, den = lr * lr + li * li;
    cc[nb] = {(nr * lr + ni * li) / den, (ni * lr - nr * li) / den};
    cpx t = cmul(a, a); t = cmul(t, t); t = cmul(t, t);
    a8[nb] = t;
    cpx a16 = cmul(t, t), a32 = cmul(a16, a16), a64 = cmul(a32, a32);
    a128[nb] = cmul(a64, a64);
    cpx one = {1.f, 0.f};
    cpx pq = cmul(csel(q & 1, t, one), csel(q & 2, a16, one));
    cpx pwv = cmul(csel(w & 1, a32, one), csel(w & 2, a64, one));
    pw[nb] = cmul(pq, pwv);
  }

  v4u xf[2][16];
  f32x4 au[2][2][2], asum[2], agr[2], asu;
#pragma unroll
  for (int b = 0; b < 2; b++) {
    asum[b] = f32x4{0, 0, 0, 0}; agr[b] = f32x4{0, 0, 0, 0};
#pragma unroll
    for (int nb = 0; nb < 2; nb++) { au[b][nb][0] = f32x4{0, 0, 0, 0}; au[b][nb][1] = f32x4{0, 0, 0, 0}; }
  }
  asu = f32x4{0, 0, 0, 0};
  const v4u ones = {0x3F803F80u, 0x3F803F80u, 0x3F803F80u, 0x3F803F80u};
  LAS char* xs = lds + L1_XS + w * 4096;
  LAS char* xs_w = xs + q * 256;
  LAS char* xs_r = xs + i16 * 256;
  LAS char* wu_r = lds + L1_WU + i16 * 256;
#define UQ(QK, NW)                                                                                   \
  {                                                                                                  \
    wait_vm4<NW + 4>(xl[QK][0][0], xl[QK][0][1], xl[QK][0][2], xl[QK][0][3]);                        \
    wait_vm4<NW + 4>(xl[QK][1][0], xl[QK][1][1], xl[QK][1][2], xl[QK][1][3]);                        \
    _Pragma("unroll") for (int b = 0; b < 2; b++) {                                                  \
      _Pragma("unroll") for (int m = 0; m < 4; m++)                                                  \
          *(LAS v4u*)(xs_w + m * 1024 + ((i16 ^ (4 * m + q)) & 15) * 16) = xl[QK][b][m];            \
      _Pragma("unroll") for (int t = 0; t < 4; t++)                                                  \
          xf[b][QK * 4 + t] = lds_rd16(xs_r + (((4 * t + q) ^ i16) & 15) * 16);                      \
    }                                                                                                \
    wait_vm<NW>();                                                                                   \
    bar();                                                                                           \
    _Pragma("unroll") for (int t = 0; t < 4; t++) {                                                  \
      v4u wf[2][2];                                                                                  \
      _Pragma("unroll") for (int part = 0; part < 2; part++) _Pragma("unroll") for (int nb = 0; nb < 2; nb++) \
          wf[nb][part] = lds_rd16(wu_r + QK * 16384 + (part * 32 + nb * 16) * 256 + (((4 * t + q) ^ i16) & 15) * 16); \
      _Pragma("unroll") for (int b = 0; b < 2; b++) {                                                \
        asum[b] = mfma16(xf[b][QK * 4 + t], ones, asum[b]);                                          \
        agr[b] = mfma16(xf[b][QK * 4 + t], xf[b][QK * 4 + t], agr[b]);                               \
      }                                                                                              \
      _Pragma("unroll") for (int b = 0; b < 2; b++) _Pragma("unroll") for (int nb = 0; nb < 2; nb++) \
          _Pragma("unroll") for (int part = 0; part < 2; part++)                                     \
              au[b][nb][part] = mfma16(xf[b][QK * 4 + t], wf[nb][part], au[b][nb][part]);            \
      v4u wsel = (w & 2) ? ((w & 1) ? wf[1][1] : wf[1][0]) : ((w & 1) ? wf[0][1] : wf[0][0]);         \
      asu = mfma16(ones, wsel, asu);                                                                 \
    }                                                                                                \
  }
  UQ(0, 56) UQ(1, 44) UQ(2, 32) UQ(3, 20)
#undef UQ

  {
    LAS f32x2* st = (LAS f32x2*)(xs + L1_STAT);
#pragma unroll
    for (int b = 0; b < 2; b++) {
      const int rg = i16 & 3;
      const float sx = rg == 0 ? asum[b][0] : rg == 1 ? asum[b][1] : rg == 2 ? asum[b][2] : asum[b][3];
      const float sq = rg == 0 ? agr[b][0] : rg == 1 ? agr[b][1] : rg == 2 ? agr[b][2] : agr[b][3];
      const float mean = sx * (1.f / 512.f);
      float var = fmaf(-mean, mean, sq * (1.f / 512.f));
      var = fmaxf(var, 0.f);
      const float rstd = rsqrtf(var + 1e-5f);
      if ((i16 >> 2) == q) st[b * 16 + i16] = f32x2{mean, rstd};
    }
    if (q == 0) ((LAS float*)(lds + L1_SU))[w * 16 + i16] = asu[0];
  }
  bar();
#pragma unroll
  for (int c = 5; c < 8; c++)
    dma4(ldsb + L1_WU + (c - 5) * 16384 + w * 4096, p.w1 + (size_t)c * 64 * 3584, w1v[0], w1v[1], w1v[2], w1v[3]);

  cpx u[2][8];
  LAS char* eb = lds + L1_WU + 3 * 16384;
  {
    f32x2 stt[2][4];
#pragma unroll
    for (int b = 0; b < 2; b++) {
      f32x4 s0 = *(LAS f32x4*)(xs + L1_STAT + (b * 16 + 4 * q) * 8);
      f32x4 s1 = *(LAS f32x4*)(xs + L1_STAT + (b * 16 + 4 * q) * 8 + 16);
      stt[b][0] = f32x2{s0[0], s0[1]}; stt[b][1] = f32x2{s0[2], s0[3]};
      stt[b][2] = f32x2{s1[0], s1[1]}; stt[b][3] = f32x2{s1[2], s1[3]};
    }
#pragma unroll
    for (int nb = 0; nb < 2; nb++) {
      const float sre = ((LAS float*)(lds + L1_SU))[(nb * 2 + 0) * 16 + i16];
      const float sim = ((LAS float*)(lds + L1_SU))[(nb * 2 + 1) * 16 + i16];
      cpx h = {0.f, 0.f};
#pragma unroll
      for (int k = 0; k < 8; k++) {
        const int b = k >> 2, rg = k & 3;
        const float m = stt[b][rg][0], rr = stt[b][rg][1];
        u[nb][k].r = fmaf(-m, sre, au[b][nb][0][rg]) * rr;
        u[nb][k].i = fmaf(-m, sim, au[b][nb][1][rg]) * rr;
        h = cfma(a1[nb], h, u[nb][k]);
      }
      *(LAS f32x2*)(eb + ((nb * 16 + i16) * 16 + 4 * w + q) * 8) = f32x2{h.r, h.i};
    }
  }
  bar();
  cpx cl[2];
  {
    cpx tot[2];
#pragma unroll
    for (int nb = 0; nb < 2; nb++) {
      LAS f32x4* ep4 = (LAS f32x4*)(eb + (nb * 16 + i16) * 128);
      cpx c = {0.f, 0.f};
      for (int ww = 0; ww < w; ww++) {
        f32x4 e0 = ep4[2 * ww], e1 = ep4[2 * ww + 1];
        c = cfma(a8[nb], c, cpx{e0[0], e0[1]});
        c = cfma(a8[nb], c, cpx{e0[2], e0[3]});
        c = cfma(a8[nb], c, cpx{e1[0], e1[1]});
        c = cfma(a8[nb], c, cpx{e1[2], e1[3]});
      }
      f32x4 e0 = ep4[2 * w], e1 = ep4[2 * w + 1];
      cpx c1 = cfma(a8[nb], c, cpx{e0[0], e0[1]});
      cpx c2 = cfma(a8[nb], c1, cpx{e0[2], e0[3]});
      cpx c3 = cfma(a8[nb], c2, cpx{e1[0], e1[1]});
      tot[nb] = cfma(a8[nb], c3, cpx{e1[2], e1[3]});
      cl[nb] = csel(q & 2, csel(q & 1, c3, c2), csel(q & 1, c1, c));
    }
    if (w == 3 && j < 15) {
      const cpx t = (q & 2) ? tot[1] : tot[0];
      const float v = (q & 1) ? t.i : t.r;
      unsigned vb = __builtin_bit_cast(unsigned, v);
      vb = (vb + 0x80u) >> 8;
      const u64 g = (epoch << 24) | (u64)(vb & 0xffffffu);
      GAS u64* gp = (GAS u64*)(p.ws + WS_GRAN + (size_t)(j * 16 + s) * 512) + (((q >> 1) * 16 + i16) * 2 + (q & 1));
      __hip_atomic_store(gp, g, RLX_AGENT);
    }
  }

  f32x4 ah[7][2], ash[2];
#pragma unroll
  for (int mb = 0; mb < 7; mb++) { ah[mb][0] = f32x4{0, 0, 0, 0}; ah[mb][1] = f32x4{0, 0, 0, 0}; }
  ash[0] = f32x4{0, 0, 0, 0}; ash[1] = f32x4{0, 0, 0, 0};
  {
    const int ta = i16 >> 2, tp = i16 & 3;
    const int key = ta | ((q & 1) << 2);
    unsigned tro[7];
#pragma unroll
    for (int mb = 0; mb < 7; mb++) {
      const int wb = mb < 6 ? mb + 2 * odd : (odd ? 1 : 6);
      tro[mb] = (unsigned)((8 * q + ta) * 256 + ((wb ^ key) * 32) + 8 * tp);
    }
#define HK(T)                                                                                      \
  {                                                                                                \
    constexpr int c_ = (T) >> 1;                                                                   \
    constexpr int cb_ = (c_ < 5 ? L1_W1 + c_ * 16384 : L1_WU + (c_ - 5) * 16384) + ((T) & 1) * 8192; \
    v4u wf[7];                                                                                     \
    _Pragma("unroll") for (int mb = 0; mb < 7; mb++)                                               \
        wf[mb] = lds_tr(lds + cb_ + tro[mb], lds + cb_ + 1024 + tro[mb]);                          \
    _Pragma("unroll") for (int mb = 0; mb < 7; mb++) {                                             \
      ah[mb][0] = mfma16(wf[mb], xf[0][T], ah[mb][0]);                                             \
      ah[mb][1] = mfma16(wf[mb], xf[1][T], ah[mb][1]);                                             \
    }                                                                                              \
    v4u s0 = (w & 2) ? ((w & 1) ? wf[3] : wf[2]) : ((w & 1) ? wf[1] : wf[0]);                      \
    v4u s1 = (w & 2) ? wf[6] : ((w & 1) ? wf[5] : wf[4]);                                          \
    ash[0] = mfma16(s0, ones, ash[0]);                                                             \
    ash[1] = mfma16(s1, ones, ash[1]);                                                             \
  }
    if (w == 3 && j < 15) wait_vm<13>(); else wait_vm<12>();
    bar();
    HK(0) HK(1) HK(2) HK(3) HK(4) HK(5) HK(6) HK(7) HK(8) HK(9)
    wait_vm<0>();
    bar();
    HK(10) HK(11) HK(12) HK(13) HK(14) HK(15)
#undef HK
  }
  v4u gr[2][4];
  const unsigned gvoff = (unsigned)(q * 16 * 512 + i16 * 16);
  const char* gb = p.ws + WS_GRAN + (size_t)s * 512;
#pragma unroll
  for (int nb = 0; nb < 2; nb++)
#pragma unroll
    for (int m = 0; m < 4; m++) {
      if (nb == 0) gld16_sc1<0>(gr[nb][m], gvoff, gb + (size_t)m * 4 * 16 * 512);
      else gld16_sc1<256>(gr[nb][m], gvoff, gb + (size_t)m * 4 * 16 * 512);
    }
  if (i16 == 0) {
    *(LAS f32x4*)(lds + L1_SH + (w * 16 + 4 * q) * 4) = ash[0];
    if (w < 3) *(LAS f32x4*)(lds + L1_SH + ((4 + w) * 16 + 4 * q) * 4) = ash[1];
  }
  bar();
  const int hp = 2 * j + (w >> 1);
  const unsigned zrow = (unsigned)(hp * NKS) * 4096u + (unsigned)(2 * (w & 1)) * 1024u + (unsigned)l * 16u;
  {
    f32x2 stc[2];
    stc[0] = *(LAS f32x2*)(xs + L1_STAT + i16 * 8);
    stc[1] = *(LAS f32x2*)(xs + L1_STAT + (16 + i16) * 8);
    unsigned pkd[7][2][2];
#pragma unroll
    for (int mb = 0; mb < 7; mb++) {
      const f32x4 sh = *(LAS f32x4*)(lds + L1_SH + (mb * 16 + 4 * q) * 4);
#pragma unroll
      for (int b = 0; b < 2; b++) {
        float y[4];
#pragma unroll
        for (int e = 0; e < 4; e++) {
          const float v = fmaf(-stc[b][0], sh[e], ah[mb][b][e]) * stc[b][1];
          y[e] = v * __builtin_amdgcn_rcpf(1.f + __builtin_amdgcn_exp2f(v * -1.44269504088896f));
        }
        pkd[mb][b][0] = pk_bf16(y[0], y[1]);
        pkd[mb][b][1] = pk_bf16(y[2], y[3]);
      }
    }
    const int ks0 = 7 * (s >> 1);
#pragma unroll
    for (int k = 0; k < 3; k++)
#pragma unroll
      for (int b = 0; b < 2; b++) {
        v4u v = {pkd[2 * k][b][0], pkd[2 * k][b][1], pkd[2 * k + 1][b][0], pkd[2 * k + 1][b][1]};
        st16<ONE>(p.ws, rs, (unsigned)WS_Z + zrow + (unsigned)(ks0 + k + 4 * odd) * 4096u + b * 1024u, v);
      }
#pragma unroll
    for (int b = 0; b < 2; b++) {
      v2u v = {pkd[6][b][0], pkd[6][b][1]};
      st8<ONE>(p.ws, rs, (unsigned)WS_Z + zrow + (unsigned)(ks0 + 3) * 4096u + b * 1024u + odd * 8u, v);
    }
    if (s == 0 && q < 2) {
      const int tok = 128 * j + 32 * w + 8 * (i16 >> 2) + 4 * q + (i16 & 3);
      const f32x2 sv = q ? stc[1] : stc[0];
      st8<ONE>(p.ws, rs, (unsigned)WS_STAT + (unsigned)tok * 8u, __builtin_bit_cast(v2u, sv));
    }
  }
  {
    asm volatile("s_waitcnt vmcnt(8)" : "+v"(gr[0][0]), "+v"(gr[0][1]), "+v"(gr[0][2]), "+v"(gr[0][3]),
                 "+v"(gr[1][0]), "+v"(gr[1][1]), "+v"(gr[1][2]), "+v"(gr[1][3]) :: "memory");
    const unsigned tlo = (unsigned)(epoch & 0xffu), thi = (unsigned)(epoch >> 8);
    unsigned spins = 0;
    for (;;) {
      bool ok = true;
#pragma unroll
      for (int nb = 0; nb < 2; nb++)
#pragma unroll
        for (int m = 0; m < 4; m++) {
          const bool need = (4 * m + q) < j;
          const bool good = (gr[nb][m].y == thi) && ((gr[nb][m].x >> 24) == tlo) && (gr[nb][m].w == thi) && ((gr[nb][m].z >> 24) == tlo);
          ok = ok && (good || !need);
        }
      if (__all(ok)) break;
      if (++spins > (1u << 22)) break;
      __builtin_amdgcn_s_sleep(1);
#pragma unroll
      for (int nb = 0; nb < 2; nb++)
#pragma unroll
        for (int m = 0; m < 4; m++) {
          if (nb == 0) gld16_sc1<0>(gr[nb][m], gvoff, gb + (size_t)m * 4 * 16 * 512);
          else gld16_sc1<256>(gr[nb][m], gvoff, gb + (size_t)m * 4 * 16 * 512);
        }
      asm volatile("s_waitcnt vmcnt(0)" : "+v"(gr[0][0]), "+v"(gr[0][1]), "+v"(gr[0][2]), "+v"(gr[0][3]),
                   "+v"(gr[1][0]), "+v"(gr[1][1]), "+v"(gr[1][2]), "+v"(gr[1][3]) :: "memory");
    }
  }
  if (j == 15) {
    bar();
    if (tid == 0) __hip_atomic_store((GAS u64*)(p.ws + WS_EPOCH + s * 128), epoch, RLX_AGENT);
  }
  {
    LAS char* ht = xs + L1_HT;
    unsigned hpk[2][2][2][2];
#pragma unroll
    for (int nb = 0; nb < 2; nb++) {
      const cpx a256 = cmul(a128[nb], a128[nb]);
      const cpx a512 = cmul(a256, a256);
      cpx acc = {0.f, 0.f};
#pragma unroll
      for (int m = 0; m < 4; m++) {
        const bool need = (4 * m + q) < j;
        cpx e = {__builtin_bit_cast(float, gr[nb][m].x << 8), __builtin_bit_cast(float, gr[nb][m].z << 8)};
        acc = csel(need, cfma(a512, acc, e), acc);
      }
      const int dd = (j - 1 - q) & 3;
      cpx one = {1.f, 0.f};
      cpx fz = cmul(csel(dd & 1, a128[nb], one), csel(dd & 2, a256, one));
      acc = cmul(acc, fz);
      acc.r += __shfl_xor(acc.r, 16); acc.i += __shfl_xor(acc.i, 16);
      acc.r += __shfl_xor(acc.r, 32); acc.i += __shfl_xor(acc.i, 32);
      cpx h = cfma(pw[nb], acc, cl[nb]);
      const cpx c2 = {2.f * cc[nb].r, 2.f * cc[nb].i};
      float orr[8], oi[8];
#pragma unroll
      for (int k = 0; k < 8; k++) {
        h = cfma(a1[nb], h, u[nb][k]);
        orr[k] = c2.r * h.r - c2.i * h.i;
        oi[k] = -(c2.r * h.i + c2.i * h.r);
      }
#pragma unroll
      for (int b = 0; b < 2; b++) {
        hpk[nb][0][b][0] = pk_bf16(orr[4 * b], orr[4 * b + 1]); hpk[nb][0][b][1] = pk_bf16(orr[4 * b + 2], orr[4 * b + 3]);
        hpk[nb][1][b][0] = pk_bf16(oi[4 * b], oi[4 * b + 1]);   hpk[nb][1][b][1] = pk_bf16(oi[4 * b + 2], oi[4 * b + 3]);
      }
    }
    const int ta = i16 >> 2, tp = i16 & 3;
    const int ksy = 56 + 4 * (s >> 1) + (s & 1);
#pragma unroll
    for (int b = 0; b < 2; b++) {
#pragma unroll
      for (int nb = 0; nb < 2; nb++)
#pragma unroll
        for (int part = 0; part < 2; part++)
          *(LAS v2u*)(ht + (part * 32 + nb * 16 + i16) * 32 + q * 8) = v2u{hpk[nb][part][b][0], hpk[nb][part][b][1]};
#pragma unroll
      for (int part = 0; part < 2; part++) {
        LAS char* rp = ht + (part * 32 + 8 * q + ta) * 32 + tp * 8;
        v4u v = lds_tr(rp, rp + 4 * 32);
        st16<ONE>(p.ws, rs, (unsigned)WS_Z + zrow + (unsigned)(ksy + 2 * part) * 4096u + b * 1024u, v);
      }
    }
  }
}

static constexpr int DEPTH = 7;
template <bool ONE, int KS0, int NF>
DI void p2_wave(const Params& p, LAS char* lds, int w, int hp, int ct, LAS char* red) {
  const int l = threadIdx.x & 63, i16 = l & 15, q = l >> 4;
  const unsigned ring = (unsigned)(size_t)lds + w * (DEPTH * 4096);
  LAS char* ringp = lds + w * (DEPTH * 4096);
  unsigned fv[4], yv[4];
#pragma unroll
  for (int n = 0; n < 4; n++) {
    {
      const int row = 8 * n + (l >> 3), c16p = l & 7;
      const int c16 = ((((c16p >> 1) ^ ((row >> 1) & 3)) << 1) | (c16p & 1));
      fv[n] = (unsigned)(row * 1024 + ct * 128 + c16 * 16 - 1024 * n);
    }
    {
      const int row = 16 * n + (l >> 2), cp = l & 3;
      const int c16 = cp ^ ((-((row & 15) >> 2)) & 3);
      yv[n] = (unsigned)((64 * ct + row) * 1024 + c16 * 16 - 1024 * n);
    }
  }
  const unsigned avoff = (unsigned)l * 16;
  const char* ab = p.ws + WS_Z + (size_t)(hp * NKS) * 4096;
  v4u af[DEPTH][4];
  auto issue = [&](auto KSC) {
    constexpr int ks = decltype(KSC)::value;
    constexpr int sl = (ks - KS0) % DEPTH;
    const char* a = ab + (size_t)ks * 4096;
    if (ONE) {
      gld16_sc1<0>(af[sl][0], avoff, a); gld16_sc1<1024>(af[sl][1], avoff, a);
      gld16_sc1<2048>(af[sl][2], avoff, a); gld16_sc1<3072>(af[sl][3], avoff, a);
    } else {
      gld16<0>(af[sl][0], avoff, a); gld16<1024>(af[sl][1], avoff, a);
      gld16<2048>(af[sl][2], avoff, a); gld16<3072>(af[sl][3], avoff, a);
    }
    if (ks < 56) {
      dma4(ring + sl * 4096, p.w2 + (size_t)ks * 32 * 1024, fv[0], fv[1], fv[2], fv[3]);
    } else {
      constexpr int ky = ks - 56;
      constexpr int sidx = 2 * (ky >> 2) + (ky & 1), part = (ky >> 1) & 1;
      dma4(ring + sl * 4096, (part ? p.cim : p.cre) + sidx * 64, yv[0], yv[1], yv[2], yv[3]);
    }
  };
  f32x4 acc[4][4];
#pragma unroll
  for (int a = 0; a < 4; a++)
#pragma unroll
    for (int b = 0; b < 4; b++) acc[a][b] = f32x4{0, 0, 0, 0};
  const int ta = i16 >> 2, tp = i16 & 3;
  unsigned ftr[4], yrd;
#pragma unroll
  for (int nb = 0; nb < 4; nb++) {
    const int row = 4 * q + ta;
    ftr[nb] = (unsigned)(row * 128 + ((nb ^ ((row >> 1) & 3)) * 32) + tp * 8);
  }
  yrd = (unsigned)(i16 * 64 + ((q ^ ((-(i16 >> 2)) & 3)) * 16));
  auto dump = [&](LAS char* dst) {
#pragma unroll
    for (int tb = 0; tb < 4; tb++) {
      const int tk = 32 * (tb >> 1) + 8 * (i16 >> 2) + 4 * (tb & 1) + (i16 & 3);
#pragma unroll
      for (int nb = 0; nb < 4; nb++)
        *(LAS f32x4*)(dst + tk * 256 + (((4 * nb + q) ^ i16) & 15) * 16) = acc[nb][tb];
    }
  };
  auto step = [&](auto IC) {
    constexpr int it = decltype(IC)::value;
    constexpr int ks = KS0 + it;
    constexpr int sl = it % DEPTH;
    constexpr int rem = (21 - it) < (DEPTH - 1) ? (21 - it) : (DEPTH - 1);
    wait_vm4<8 * rem>(af[sl][0], af[sl][1], af[sl][2], af[sl][3]);
    v4u wf[4];
    if (ks < 56) {
#pragma unroll
      for (int nb = 0; nb < 4; nb++) wf[nb] = lds_tr(ringp + sl * 4096 + ftr[nb], ringp + sl * 4096 + 2048 + ftr[nb]);
    } else {
#pragma unroll
      for (int nb = 0; nb < 4; nb++) wf[nb] = lds_rd16(ringp + sl * 4096 + nb * 1024 + yrd);
    }
    if (NF > 0 && NF < 22 && it == NF) {
      dump(red);
#pragma unroll
      for (int a = 0; a < 4; a++)
#pragma unroll
        for (int b = 0; b < 4; b++) acc[a][b] = f32x4{0, 0, 0, 0};
    }
#pragma unroll
    for (int nb = 0; nb < 4; nb++)
#pragma unroll
      for (int tb = 0; tb < 4; tb++) acc[nb][tb] = mfma16(wf[nb], af[sl][tb], acc[nb][tb]);
    if constexpr (it + DEPTH < 22) {
      asm volatile("s_waitcnt lgkmcnt(0)" ::: "memory");
      issue(std::integral_constant<int, ks + DEPTH>{});
    }
  };
  issue(std::integral_constant<int, KS0 + 0>{}); issue(std::integral_constant<int, KS0 + 1>{});
  issue(std::integral_constant<int, KS0 + 2>{}); issue(std::integral_constant<int, KS0 + 3>{});
  issue(std::integral_constant<int, KS0 + 4>{}); issue(std::integral_constant<int, KS0 + 5>{});
  issue(std::integral_constant<int, KS0 + 6>{});
#define ST(n) step(std::integral_constant<int, n>{});
  ST(0) ST(1) ST(2) ST(3) ST(4) ST(5) ST(6) ST(7) ST(8) ST(9) ST(10) ST(11) ST(12) ST(13) ST(14) ST(15) ST(16) ST(17)
  ST(18) ST(19) ST(20) ST(21)
#undef ST
  dump(ringp);
}

template <bool ONE>
DI void phase2(const Params& p, LAS char* lds) {
  const int tid = threadIdx.x;
  const int w = __builtin_amdgcn_readfirstlane(tid >> 6);
  const int bid = blockIdx.x, xcd = bid & 7, r = bid >> 3;
  const int hp = 8 * (xcd >> 1) + (r >> 2);
  const int ct = 4 * (xcd & 1) + (r & 3);
  LAS char* mid = lds + 4 * DEPTH * 4096;
  const int ec = tid & 15, et = tid >> 4;
  v2u xe[4]; f32x2 se[4];
  const v2u dk = *(const GAS v2u*)(p.dskip + (64 * ct + 4 * ec) * 2);
#pragma unroll
  for (int n = 0; n < 4; n++) {
    const int tok = 64 * hp + et + 16 * n;
    xe[n] = *(const GAS v2u*)(p.x + (size_t)tok * 1024 + (64 * ct + 4 * ec) * 2);
    if (!ONE) se[n] = *(const GAS f32x2*)(p.ws + WS_STAT + (size_t)tok * 8);
  }
  if (w == 0) p2_wave<ONE, 0, 22>(p, lds, w, hp, ct, mid);
  else if (w == 1) p2_wave<ONE, 22, 22>(p, lds, w, hp, ct, mid);
  else if (w == 2) p2_wave<ONE, 44, 12>(p, lds, w, hp, ct, mid);
  else p2_wave<ONE, 66, 0>(p, lds, w, hp, ct, mid);
  bar();
  const float dkf[4] = {bf2f(dk.x & 0xffff), bf2f(dk.x >> 16), bf2f(dk.y & 0xffff), bf2f(dk.y >> 16)};
#pragma unroll
  for (int n = 0; n < 4; n++) {
    const int tk = et + 16 * n;
    const int key = (tk & 3) | (((tk >> 3) & 3) << 2);
    const int o = tk * 256 + ((ec ^ key) & 15) * 16;
    const f32x4 f = *(LAS f32x4*)(lds + o) + *(LAS f32x4*)(lds + DEPTH * 4096 + o) + *(LAS f32x4*)(mid + o);
    const f32x4 y = *(LAS f32x4*)(lds + 2 * DEPTH * 4096 + o) + *(LAS f32x4*)(lds + 3 * DEPTH * 4096 + o);
    const float xv[4] = {bf2f(xe[n].x & 0xffff), bf2f(xe[n].x >> 16), bf2f(xe[n].y & 0xffff), bf2f(xe[n].y >> 16)};
    f32x4 o4;
#pragma unroll
    for (int e = 0; e < 4; e++) {
      const float xn = (xv[e] - se[n][0]) * se[n][1];
      const float sv = fmaf(xn, dkf[e], y[e]);
      const float g = f[e] * __builtin_amdgcn_rcpf(1.f + __builtin_amdgcn_exp2f(f[e] * -1.44269504088896f));
      o4[e] = fmaf(sv, g, xv[e]);
    }
    *(GAS f32x4*)(p.out + (size_t)(64 * hp + tk) * 512 + 64 * ct + 4 * ec) = o4;
  }
}

__global__ __launch_bounds__(256, 1) void k_phase1(Params p) {
  extern __shared__ __attribute__((aligned(16))) char smem[];
  u64 e;
  phase1<false>(p, (LAS char*)smem, e);
}
__global__ __launch_bounds__(256, 1) void k_phase2(Params p) {
  extern __shared__ __attribute__((aligned(16))) char smem[];
  phase2<false>(p, (LAS char*)smem);
}

extern "C" void kernel_launch(void* const* d_in, const int* in_sizes, int n_in, void* d_out, int out_size, void* d_ws,
                              size_t ws_size, hipStream_t stream) {
  Params p;
  p.x = (const char*)d_in[0]; p.lre = (const char*)d_in[1]; p.lim = (const char*)d_in[2];
  p.bre = (const char*)d_in[3]; p.bim = (const char*)d_in[4]; p.cre = (const char*)d_in[5]; p.cim = (const char*)d_in[6];
  p.logdt = (const char*)d_in[7]; p.dskip = (const char*)d_in[8]; p.w1 = (const char*)d_in[10]; p.w2 = (const char*)d_in[11];
  p.out = (float*)d_out; p.ws = (char*)d_ws;
  hipFuncSetAttribute((const void*)k_phase1, hipFuncAttributeMaxDynamicSharedMemorySize, LDS_BYTES);
  hipFuncSetAttribute((const void*)k_phase2, hipFuncAttributeMaxDynamicSharedMemorySize, LDS_BYTES);
  k_phase1<<<256, 256, LDS_BYTES, stream>>>(p);
  k_phase2<<<256, 256, LDS_BYTES, stream>>>(p);
}
```

```cpp
#include <hip/hip_runtime.h>
#include <hip/hip_bf16.h>
#include <type_traits>

typedef unsigned v4u __attribute__((ext_vector_type(4)));
typedef unsigned v2u __attribute__((ext_vector_type(2)));
typedef float f32x4 __attribute__((ext_vector_type(4)));
typedef float f32x2 __attribute__((ext_vector_type(2)));
typedef short bf16x8 __attribute__((ext_vector_type(8)));
typedef short s16x4 __attribute__((ext_vector_type(4)));
typedef __bf16 bf16x2_t __attribute__((ext_vector_type(2)));
typedef unsigned long long u64;
#define LAS __attribute__((address_space(3)))
#define GAS __attribute__((address_space(1)))
#define DI __device__ __forceinline__
#define RLX_AGENT __ATOMIC_RELAXED, __HIP_MEMORY_SCOPE_AGENT
#define RLX_WG __ATOMIC_RELAXED, __HIP_MEMORY_SCOPE_WORKGROUP

static constexpr int NKS = 88;
static constexpr size_t WS_Z = 0;
static constexpr size_t WS_Z_BYTES = (size_t)32 * NKS * 4096;
static constexpr size_t WS_GRAN = WS_Z + WS_Z_BYTES;
static constexpr size_t WS_FLAG = WS_GRAN + 16 * 16 * 512;
static constexpr size_t WS_EPOCH = WS_FLAG + 2 * 16 * 16 * 8;
static constexpr size_t WS_END = WS_EPOCH + 128;

static constexpr int LDS_BYTES = 160 * 1024;
static constexpr int L_XS = 0;
static constexpr int L_WU = 32 * 1024;
static constexpr int L_W1 = 96 * 1024;
static constexpr int L_CST = 0;
static constexpr int L_HT = 16 * 1024;
static constexpr int L_E = 24 * 1024;
static constexpr int L_KEEP = 28 * 1024;
static constexpr int L_YB0 = 0;
static constexpr int L_YB1 = 32 * 1024;
static constexpr int L_RING = 48 * 1024;
static constexpr int L_STAT = L_KEEP;
static constexpr int L_SU = L_KEEP + 1024;
static constexpr int L_SH = L_KEEP + 1280;
static constexpr int L_CNT = L_KEEP + 1792;
enum { C_E = 0, C_W = 1, C_SH = 2, C_B2 = 3, C_HS = 4, C_FS = 5, C_Y0 = 6, C_Y1 = 7 };

DI f32x4 mfma16(v4u a, v4u b, f32x4 c) {
  return __builtin_amdgcn_mfma_f32_16x16x32_bf16(__builtin_bit_cast(bf16x8, a), __builtin_bit_cast(bf16x8, b), c, 0, 0, 0);
}
DI unsigned pk_bf16(float lo, float hi) {
  f32x2 v = {lo, hi};
  return __builtin_bit_cast(unsigned, __builtin_convertvector(v, bf16x2_t));
}
DI unsigned pk_bf16v(f32x2 v) { return __builtin_bit_cast(unsigned, __builtin_convertvector(v, bf16x2_t)); }
DI float bf2f(unsigned u) { return __builtin_bit_cast(float, u << 16); }
DI unsigned opaque(unsigned v) { asm volatile("" : "+v"(v)); return v; }
DI LAS char* lp(unsigned a) { return (LAS char*)(size_t)a; }

template <int IMM> DI void gld16(v4u& d, unsigned voff, const void* sb) {
  asm volatile("global_load_dwordx4 %0, %1, %2 offset:%3" : "=v"(d) : "v"(voff), "s"(sb), "i"(IMM) : "memory");
}
template <int IMM> DI void gld16_sc1(v4u& d, unsigned voff, const void* sb) {
  asm volatile("global_load_dwordx4 %0, %1, %2 offset:%3 sc1" : "=v"(d) : "v"(voff), "s"(sb), "i"(IMM) : "memory");
}
template <int IMM> DI void gld_u16(unsigned& d, unsigned voff, const void* sb) {
  asm volatile("global_load_ushort %0, %1, %2 offset:%3" : "=v"(d) : "v"(voff), "s"(sb), "i"(IMM) : "memory");
}
DI void gld8_sc1(v2u& d, unsigned voff, const void* sb) {
  asm volatile("global_load_dwordx2 %0, %1, %2 sc1" : "=v"(d) : "v"(voff), "s"(sb) : "memory");
}
DI void dma4(unsigned lds, const void* sb, unsigned v0, unsigned v1, unsigned v2, unsigned v3) {
  asm volatile(
      "s_mov_b32 m0, %0\n\ts_nop 0\n\t"
      "global_load_lds_dwordx4 %2, %1\n\t"
      "global_load_lds_dwordx4 %3, %1 offset:1024\n\t"
      "global_load_lds_dwordx4 %4, %1 offset:2048\n\t"
      "global_load_lds_dwordx4 %5, %1 offset:3072"
      :: "s"(lds), "s"(sb), "v"(v0), "v"(v1), "v"(v2), "v"(v3) : "memory");
}
template <int N> DI void wait_vm() { asm volatile("s_waitcnt vmcnt(%0)" :: "i"(N) : "memory"); }
template <int N> DI void wait_vm4(v4u& a, v4u& b, v4u& c, v4u& d) {
  asm volatile("s_waitcnt vmcnt(%4)" : "+v"(a), "+v"(b), "+v"(c), "+v"(d) : "i"(N) : "memory");
}
DI void wait_lgkm() { asm volatile("s_waitcnt lgkmcnt(0)" ::: "memory"); }
DI void bar() { wait_lgkm(); __builtin_amdgcn_s_barrier(); asm volatile("" ::: "memory"); }

DI v4u lds_rd16(LAS char* p) { return *(LAS v4u*)p; }
DI v4u lds_tr(LAS char* p0, LAS char* p1) {
  s16x4 a = __builtin_amdgcn_ds_read_tr16_b64_v4i16((LAS s16x4*)p0);
  s16x4 b = __builtin_amdgcn_ds_read_tr16_b64_v4i16((LAS s16x4*)p1);
  bf16x8 f = __builtin_shufflevector(a, b, 0, 1, 2, 3, 4, 5, 6, 7);
  return __builtin_bit_cast(v4u, f);
}
DI unsigned cnt_arrive(LAS char* lds, int c) {
  wait_lgkm();
  unsigned old = 0;
  if ((threadIdx.x & 63) == 0) old = __hip_atomic_fetch_add((LAS unsigned*)(lds + L_CNT) + c, 1u, RLX_WG);
  return __builtin_amdgcn_readfirstlane(old);
}
DI void cnt_wait(LAS char* lds, int c, unsigned target) {
  unsigned spins = 0;
  while ((unsigned)__builtin_amdgcn_readfirstlane(__hip_atomic_load((LAS unsigned*)(lds + L_CNT) + c, RLX_WG)) < target) {
    if (++spins > (1u << 24)) break;
    __builtin_amdgcn_s_sleep(1);
  }
  asm volatile("" ::: "memory");
}
struct cpx { float r, i; };
DI cpx cmul(cpx a, cpx b) { return {a.r * b.r - a.i * b.i, a.r * b.i + a.i * b.r}; }
DI cpx cfma(cpx a, cpx h, cpx u) {
  return {fmaf(a.r, h.r, fmaf(-a.i, h.i, u.r)), fmaf(a.r, h.i, fmaf(a.i, h.r, u.i))};
}
DI cpx csel(bool c, cpx a, cpx b) { return {c ? a.r : b.r, c ? a.i : b.i}; }

#define STAMP(i)
#define STAMP_DECL
#define STAMP_OUT()

struct Params {
  const char *x, *lre, *lim, *bre, *bim, *cre, *cim, *logdt, *dskip, *w1, *w2;
  float* out;
  char* ws;
};

DI void st16(__amdgpu_buffer_rsrc_t rs, unsigned off, v4u v) { __builtin_amdgcn_raw_buffer_store_b128(v, rs, (int)off, 0, 16); }
DI void st8(__amdgpu_buffer_rsrc_t rs, unsigned off, v2u v) { __builtin_amdgcn_raw_buffer_store_b64(v, rs, (int)off, 0, 16); }

DI void poll_flags(const char* fl, int l, u64 epoch) {
  GAS u64* fp = (GAS u64*)fl + (l & 3);
  unsigned spins = 0;
  while (!__all(__hip_atomic_load(fp, RLX_AGENT) == epoch)) {
    if (++spins > (1u << 22)) break;
    __builtin_amdgcn_s_sleep(1);
  }
  asm volatile("" ::: "memory");
}

constexpr int f_wait(int it) {
  int n = 0; bool seen = false;
  for (int a = 0; a < 7; a++) { if (seen) n += 4; if (a == it) seen = true; }
  for (int k = 0; k < it; k++) {
    if (k + 7 < 14) { if (seen) n += 8; if (k + 7 == it) seen = true; }
  }
  return n;
}
constexpr int y_wait(int it) {
  int n = 0; bool seen = false;
  for (int a = 0; a < 3; a++) { if (seen) n += 4; if (a == it) seen = true; }
  for (int k = 0; k < it; k++)
    for (int a = 3 + 2 * k; a < 5 + 2 * k; a++)
      if (a < 8) { if (seen) n += 4; if (a == it) seen = true; }
  return n;
}

__global__ __launch_bounds__(512, 2) void k_fused(Params p) {
  extern __shared__ __attribute__((aligned(16))) char smem[];
  LAS char* lds = (LAS char*)smem;
  STAMP_DECL
  STAMP(0);
  const int tid = threadIdx.x, l = tid & 63;
  const int w8 = __builtin_amdgcn_readfirstlane(tid >> 6);
  const int role = w8 >> 2, w = w8 & 3;
  const int bid = blockIdx.x, xcd = bid & 7, r = bid >> 3;
  const int j = 4 * (xcd >> 1) + (r >> 3);
  const int s = 8 * (xcd & 1) + (r & 7);
  const int hp2 = 8 * (xcd >> 1) + (r >> 2);
  const int ct = 4 * (xcd & 1) + (r & 3);
  const int odd = s & 1;
  const int i16 = l & 15, q = l >> 4;
  const unsigned ldsb = (unsigned)(size_t)lds;
  const __amdgpu_buffer_rsrc_t rs = __builtin_amdgcn_make_buffer_rsrc((void*)p.ws, 0, (int)WS_END, 0x00020000);
  const v4u ones = {0x3F803F80u, 0x3F803F80u, 0x3F803F80u, 0x3F803F80u};
  const int ta = i16 >> 2, tp = i16 & 3;

  v4u xl[4][4];
  const unsigned xvoff = (unsigned)(q * 1024 + i16 * 16);
  const char* xb = p.x + (size_t)(128 * j + 32 * w + 4 * role) * 1024;
  const unsigned xw_base = opaque(ldsb + L_XS + (w * 2 + role) * 4096 + q * 256);
  unsigned xwa[4], sw4[4];
#pragma unroll
  for (int m = 0; m < 4; m++) {
    xwa[m] = opaque(xw_base + m * 1024 + ((i16 ^ (4 * m + q)) & 15) * 16);
    sw4[m] = (unsigned)(i16 * 256 + (((4 * m + q) ^ i16) & 15) * 16);
  }
  const unsigned zrow = (unsigned)((2 * j + (w >> 1)) * NKS) * 4096u + (unsigned)(2 * (w & 1)) * 1024u + (unsigned)l * 16u;
  const char* ab = p.ws + WS_Z + (size_t)(hp2 * NKS) * 4096;
  const unsigned avoff = (unsigned)l * 16;
  const int trkey = ta | ((q & 1) << 2);
  const unsigned tr_row = (unsigned)((8 * q + ta) * 256 + 8 * tp);
  v2u ep;
  u64 epoch;

  if (role == 0) {
    unsigned plr[2], pli[2], pdt[2];
    {
      const unsigned co = (unsigned)(32 * s + i16) * 2;
      gld_u16<0>(plr[0], co, p.lre); gld_u16<32>(plr[1], co, p.lre);
      gld_u16<0>(pli[0], co, p.lim); gld_u16<32>(pli[1], co, p.lim);
      gld_u16<0>(pdt[0], co, p.logdt); gld_u16<32>(pdt[1], co, p.logdt);
      gld8_sc1(ep, 0u, p.ws + WS_EPOCH);
    }
    unsigned wuv[4];
#pragma unroll
    for (int n = 0; n < 4; n++) {
      const int ch = 16 * (w & 1) + 4 * n + q;
      const int c16 = i16 ^ ((4 * n + q) & 15);
      wuv[n] = (unsigned)(ch * 1024 + c16 * 16 - 1024 * n);
    }
    const char* wub = ((w >> 1) ? p.bim : p.bre) + (size_t)(32 * s) * 1024;
#define XQ(QK)                                                                                        \
  {                                                                                                   \
    _Pragma("unroll") for (int m = 0; m < 4; m++) gld16<QK * 256>(xl[QK][m], xvoff, xb + (8 * m) * 1024); \
    dma4(ldsb + L_WU + QK * 16384 + w * 4096, wub + QK * 256, wuv[0], wuv[1], wuv[2], wuv[3]);         \
  }
    XQ(0) XQ(1) XQ(2) XQ(3)
#undef XQ
    asm volatile("s_waitcnt vmcnt(32)" : "+v"(plr[0]), "+v"(plr[1]), "+v"(pli[0]), "+v"(pli[1]), "+v"(pdt[0]), "+v"(pdt[1]), "+v"(ep) :: "memory");
    epoch = (((u64)ep.y << 32) | ep.x) + 1ull;
    cpx a1[2], cc[2], a8[2], pw[2], a128[2];
#pragma unroll
    for (int nb = 0; nb < 2; nb++) {
      const float lr = bf2f(plr[nb]), li = bf2f(pli[nb]);
      const float dtf = expf(bf2f(pdt[nb]));
      const float dt = bf2f(pk_bf16(dtf, 0.f) & 0xffffu);
      const float e = expf(dt * lr);
      float sn, cs;
      sincosf(dt * li, &sn, &cs);
      cpx a = {e * cs, e * sn};
      a1[nb] = a;
      const float nr = a.r - 1.f, ni = a.i, den = lr * lr + li * li;
      cc[nb] = {2.f * (nr * lr + ni * li) / den, 2.f * (ni * lr - nr * li) / den};
      cpx t = cmul(a, a); t = cmul(t, t); t = cmul(t, t);
      a8[nb] = t;
      cpx a16 = cmul(t, t), a32 = cmul(a16, a16), a64 = cmul(a32, a32);
      a128[nb] = cmul(a64, a64);
      cpx one = {1.f, 0.f};
      cpx pq = cmul(csel(q & 1, t, one), csel(q & 2, a16, one));
      cpx pwv = cmul(csel(w & 1, a32, one), csel(w & 2, a64, one));
      pw[nb] = cmul(pq, pwv);
    }
    STAMP(1);
    f32x4 au[2][2][2], asu;
#pragma unroll
    for (int b = 0; b < 2; b++)
#pragma unroll
      for (int nb = 0; nb < 2; nb++) { au[b][nb][0] = f32x4{0, 0, 0, 0}; au[b][nb][1] = f32x4{0, 0, 0, 0}; }
    asu = f32x4{0, 0, 0, 0};
    unsigned xra[4], wra[4], wsa[4];
#pragma unroll
    for (int t = 0; t < 4; t++) {
      xra[t] = opaque(ldsb + L_XS + w * 8192 + sw4[t]);
      wra[t] = opaque(ldsb + L_WU + sw4[t]);
      wsa[t] = opaque(ldsb + L_WU + sw4[t] + ((w & 1) * 32 + (w >> 1) * 16) * 256);
    }
#define UQ(QK, NW)                                                                                   \
  {                                                                                                  \
    wait_vm4<NW>(xl[QK][0], xl[QK][1], xl[QK][2], xl[QK][3]);                                        \
    _Pragma("unroll") for (int m = 0; m < 4; m++) *(LAS v4u*)lp(xwa[m]) = xl[QK][m];                 \
    bar();                                                                                           \
    v4u xf[2][4];                                                                                    \
    _Pragma("unroll") for (int t = 0; t < 4; t++) {                                                  \
      xf[0][t] = lds_rd16(lp(xra[t])); xf[1][t] = lds_rd16(lp(xra[t]) + 4096);                       \
    }                                                                                                \
    bar();                                                                                           \
    _Pragma("unroll") for (int t = 0; t < 4; t++) {                                                  \
      v4u wf[2][2];                                                                                  \
      _Pragma("unroll") for (int part = 0; part < 2; part++) _Pragma("unroll") for (int nb = 0; nb < 2; nb++) \
          wf[nb][part] = lds_rd16(lp(wra[t]) + QK * 16384 + (part * 32 + nb * 16) * 256);            \
      v4u wsel = lds_rd16(lp(wsa[t]) + QK * 16384);                                                  \
      _Pragma("unroll") for (int b = 0; b < 2; b++) _Pragma("unroll") for (int nb = 0; nb < 2; nb++) \
          _Pragma("unroll") for (int part = 0; part < 2; part++)                                     \
              au[b][nb][part] = mfma16(xf[b][t], wf[nb][part], au[b][nb][part]);                     \
      asu = mfma16(ones, wsel, asu);                                                                 \
    }                                                                                                \
  }
    UQ(0, 24) STAMP(2); UQ(1, 16) UQ(2, 8) UQ(3, 0)
#undef UQ
    if (q == 0) ((LAS float*)(lds + L_SU))[((w >> 1) * 2 + (w & 1)) * 16 + i16] = asu[0];
    if (tid < 16) ((LAS unsigned*)(lds + L_CNT))[tid] = 0u;
    bar();
    STAMP(3);

    cpx u[2][8];
    {
      f32x2 stt[2][4];
#pragma unroll
      for (int b = 0; b < 2; b++) {
        f32x4 s0 = *(LAS f32x4*)(lds + L_STAT + (w * 32 + b * 16 + 4 * q) * 8);
        f32x4 s1 = *(LAS f32x4*)(lds + L_STAT + (w * 32 + b * 16 + 4 * q) * 8 + 16);
        stt[b][0] = f32x2{s0[0], s0[1]}; stt[b][1] = f32x2{s0[2], s0[3]};
        stt[b][2] = f32x2{s1[0], s1[1]}; stt[b][3] = f32x2{s1[2], s1[3]};
      }
#pragma unroll
      for (int nb = 0; nb < 2; nb++) {
        const float sre = ((LAS float*)(lds + L_SU))[(nb * 2 + 0) * 16 + i16];
        const float sim = ((LAS float*)(lds + L_SU))[(nb * 2 + 1) * 16 + i16];
        cpx h = {0.f, 0.f};
#pragma unroll
        for (int k = 0; k < 8; k++) {
          const int b = k >> 2, rg = k & 3;
          const float m = stt[b][rg][0], rr = stt[b][rg][1];
          u[nb][k].r = fmaf(-m, sre, au[b][nb][0][rg]) * rr;
          u[nb][k].i = fmaf(-m, sim, au[b][nb][1][rg]) * rr;
          h = cfma(a1[nb], h, u[nb][k]);
        }
        *(LAS f32x2*)(lds + L_E + ((nb * 16 + i16) * 16 + 4 * w + q) * 8) = f32x2{h.r, h.i};
      }
    }
    cnt_arrive(lds, C_E);
    cnt_wait(lds, C_E, 4);
    cpx cl[2];
    {
      cpx c[2] = {{0.f, 0.f}, {0.f, 0.f}};
      LAS f32x4* e4a = (LAS f32x4*)(lds + L_E + i16 * 128);
      LAS f32x4* e4b = (LAS f32x4*)(lds + L_E + (16 + i16) * 128);
#pragma unroll 1
      for (int k = 0; k < 2 * w; k++) {
        f32x4 ea = e4a[k], eb = e4b[k];
        c[0] = cfma(a8[0], c[0], cpx{ea[0], ea[1]}); c[1] = cfma(a8[1], c[1], cpx{eb[0], eb[1]});
        c[0] = cfma(a8[0], c[0], cpx{ea[2], ea[3]}); c[1] = cfma(a8[1], c[1], cpx{eb[2], eb[3]});
      }
      cpx tot[2];
#pragma unroll
      for (int nb = 0; nb < 2; nb++) {
        LAS f32x4* e4 = nb ? e4b : e4a;
        f32x4 e0 = e4[2 * w], e1 = e4[2 * w + 1];
        cpx c1 = cfma(a8[nb], c[nb], cpx{e0[0], e0[1]});
        cpx c2 = cfma(a8[nb], c1, cpx{e0[2], e0[3]});
        cpx c3 = cfma(a8[nb], c2, cpx{e1[0], e1[1]});
        tot[nb] = cfma(a8[nb], c3, cpx{e1[2], e1[3]});
        cl[nb] = csel(q & 2, csel(q & 1, c3, c2), csel(q & 1, c1, c[nb]));
      }
      if (w == 3 && j < 15) {
        const cpx t = (q & 2) ? tot[1] : tot[0];
        const float v = (q & 1) ? t.i : t.r;
        unsigned vb = __builtin_bit_cast(unsigned, v);
        vb = (vb + 0x80u) >> 8;
        const u64 g = (epoch << 24) | (u64)(vb & 0xffffffu);
        GAS u64* gp = (GAS u64*)(p.ws + WS_GRAN + (size_t)(j * 16 + s) * 512) + (((q >> 1) * 16 + i16) * 2 + (q & 1));
        __hip_atomic_store(gp, g, RLX_AGENT);
      }
    }
    v4u cf[8][4];
    const unsigned cvoff = (unsigned)((64 * ct + (l >> 2)) * 1024 + (l & 3) * 16);
#define CLOAD(K0)                                                                         \
  _Pragma("unroll") for (int k = K0; k < K0 + 4; k++) {                                   \
    const int sidx = 4 * w + 2 * (k >> 2) + (k & 1);                                      \
    const char* cb = (((k >> 1) & 1) ? p.cim : p.cre) + sidx * 64;                        \
    _Pragma("unroll") for (int n = 0; n < 4; n++) gld16<0>(cf[k][n], cvoff, cb + n * 16384); \
  }
#define CWAIT(K0, N)                                                                      \
  asm volatile("s_waitcnt vmcnt(%16)" : "+v"(cf[K0][0]), "+v"(cf[K0][1]), "+v"(cf[K0][2]), "+v"(cf[K0][3]), "+v"(cf[K0 + 1][0]),   \
               "+v"(cf[K0 + 1][1]), "+v"(cf[K0 + 1][2]), "+v"(cf[K0 + 1][3]), "+v"(cf[K0 + 2][0]), "+v"(cf[K0 + 2][1]),          \
               "+v"(cf[K0 + 2][2]), "+v"(cf[K0 + 2][3]), "+v"(cf[K0 + 3][0]), "+v"(cf[K0 + 3][1]), "+v"(cf[K0 + 3][2]),          \
               "+v"(cf[K0 + 3][3]) : "i"(N) : "memory");
#define CXPOSE(K0)                                                                        \
  _Pragma("unroll") for (int k = K0; k < K0 + 4; k++) {                                   \
    _Pragma("unroll") for (int n = 0; n < 4; n++) *(LAS v4u*)(lp(cwa) + n * 1024) = cf[k][n]; \
    if (k < 7) { _Pragma("unroll") for (int n = 0; n < 4; n++) cf[k][n] = lds_rd16(lp(cra) + n * 1024); } \
  }
    CLOAD(0)
    STAMP(4);
    {
      const int wb0 = w + 2 * odd;
      const int wb1 = (w < 2) ? (w + 4 + 2 * odd) : (odd ? 1 : 6);
      unsigned t0 = ldsb + L_W1 + tr_row + (unsigned)((wb0 ^ trkey) * 32);
      unsigned t1 = ldsb + L_W1 + tr_row + (unsigned)((wb1 ^ trkey) * 32);
      f32x4 s0 = {0, 0, 0, 0}, s1 = {0, 0, 0, 0};
#pragma unroll 1
      for (int hf = 0; hf < 2; hf++) {
        if (hf) cnt_wait(lds, C_W, 4);
#pragma unroll
        for (int t = 0; t < 8; t++) {
          v4u f0 = lds_tr(lp(t0) + t * 8192, lp(t0) + t * 8192 + 1024);
          v4u f1 = lds_tr(lp(t1) + t * 8192, lp(t1) + t * 8192 + 1024);
          s0 = mfma16(f0, ones, s0);
          s1 = mfma16(f1, ones, s1);
        }
        t0 -= 65536u; t1 -= 65536u;
      }
      if (i16 == 0) {
        *(LAS f32x4*)(lds + L_SH + (w * 16 + 4 * q) * 4) = s0;
        if (w < 3) *(LAS f32x4*)(lds + L_SH + ((4 + w) * 16 + 4 * q) * 4) = s1;
      }
      cnt_arrive(lds, C_SH);
    }
    STAMP(5);
    v4u gr[2][4];
    const unsigned gvoff = (unsigned)(q * 16 * 512 + i16 * 16);
    const char* gb = p.ws + WS_GRAN + (size_t)s * 512;
#define GATHER()                                                                       \
  _Pragma("unroll") for (int m = 0; m < 4; m++) {                                      \
    gld16_sc1<0>(gr[0][m], gvoff, gb + (size_t)m * 4 * 16 * 512);                      \
    gld16_sc1<256>(gr[1][m], gvoff, gb + (size_t)m * 4 * 16 * 512);                    \
  }
    GATHER()
    const unsigned cwa = opaque(ldsb + L_CST + w * 4096 + (l >> 2) * 64 + (((l & 3) ^ ((-(l >> 4)) & 3)) * 16));
    const unsigned cra = opaque(ldsb + L_CST + w * 4096 + i16 * 64 + ((q ^ ((-(i16 >> 2)) & 3)) * 16));
    CWAIT(0, 8)
    CXPOSE(0)
    STAMP(6);
    {
      asm volatile("s_waitcnt vmcnt(0)" : "+v"(gr[0][0]), "+v"(gr[0][1]), "+v"(gr[0][2]), "+v"(gr[0][3]),
                   "+v"(gr[1][0]), "+v"(gr[1][1]), "+v"(gr[1][2]), "+v"(gr[1][3]) :: "memory");
      const unsigned tlo = (unsigned)(epoch & 0xffu), thi = (unsigned)(epoch >> 8);
      unsigned spins = 0;
      for (;;) {
        bool ok = true;
#pragma unroll
        for (int nb = 0; nb < 2; nb++)
#pragma unroll
          for (int m = 0; m < 4; m++) {
            const bool need = (4 * m + q) < j;
            const bool good = (gr[nb][m].y == thi) && ((gr[nb][m].x >> 24) == tlo) && (gr[nb][m].w == thi) && ((gr[nb][m].z >> 24) == tlo);
            ok = ok && (good || !need);
          }
        if (__all(ok)) break;
        if (++spins > (1u << 22)) break;
        __builtin_amdgcn_s_sleep(1);
        GATHER()
        asm volatile("s_waitcnt vmcnt(0)" : "+v"(gr[0][0]), "+v"(gr[0][1]), "+v"(gr[0][2]), "+v"(gr[0][3]),
                     "+v"(gr[1][0]), "+v"(gr[1][1]), "+v"(gr[1][2]), "+v"(gr[1][3]) :: "memory");
      }
    }
#undef GATHER
    STAMP(7);
    {
      LAS char* ht = lds + L_HT + w * 2048;
      unsigned hpk[2][2][2][2];
#pragma unroll
      for (int nb = 0; nb < 2; nb++) {
        const cpx a256 = cmul(a128[nb], a128[nb]);
        const cpx a512 = cmul(a256, a256);
        cpx acc = {0.f, 0.f};
#pragma unroll
        for (int m = 0; m < 4; m++) {
          const bool need = (4 * m + q) < j;
          cpx e = {__builtin_bit_cast(float, gr[nb][m].x << 8), __builtin_bit_cast(float, gr[nb][m].z << 8)};
          acc = csel(need, cfma(a512, acc, e), acc);
        }
        const int dd = (j - 1 - q) & 3;
        cpx one = {1.f, 0.f};
        cpx fz = cmul(csel(dd & 1, a128[nb], one), csel(dd & 2, a256, one));
        acc = cmul(acc, fz);
        acc.r += __shfl_xor(acc.r, 16); acc.i += __shfl_xor(acc.i, 16);
        acc.r += __shfl_xor(acc.r, 32); acc.i += __shfl_xor(acc.i, 32);
        cpx h = cfma(pw[nb], acc, cl[nb]);
        float orr[8], oi[8];
#pragma unroll
        for (int k = 0; k < 8; k++) {
          h = cfma(a1[nb], h, u[nb][k]);
          orr[k] = cc[nb].r * h.r - cc[nb].i * h.i;
          oi[k] = -(cc[nb].r * h.i + cc[nb].i * h.r);
        }
#pragma unroll
        for (int b = 0; b < 2; b++) {
          hpk[nb][0][b][0] = pk_bf16(orr[4 * b], orr[4 * b + 1]); hpk[nb][0][b][1] = pk_bf16(orr[4 * b + 2], orr[4 * b + 3]);
          hpk[nb][1][b][0] = pk_bf16(oi[4 * b], oi[4 * b + 1]);   hpk[nb][1][b][1] = pk_bf16(oi[4 * b + 2], oi[4 * b + 3]);
        }
      }
      const int ksy = 56 + 4 * (s >> 1) + (s & 1);
#pragma unroll
      for (int b = 0; b < 2; b++) {
#pragma unroll
        for (int nb = 0; nb < 2; nb++)
#pragma unroll
          for (int part = 0; part < 2; part++)
            *(LAS v2u*)(ht + (part * 32 + nb * 16 + i16) * 32 + q * 8) = v2u{hpk[nb][part][b][0], hpk[nb][part][b][1]};
#pragma unroll
        for (int part = 0; part < 2; part++) {
          LAS char* rp = ht + (part * 32 + 8 * q + ta) * 32 + tp * 8;
          v4u v = lds_tr(rp, rp + 4 * 32);
          st16(rs, (unsigned)WS_Z + zrow + (unsigned)(ksy + 2 * part) * 4096u + b * 1024u, v);
        }
      }
    }
    CLOAD(4)
    wait_vm<16>();
    if (cnt_arrive(lds, C_HS) == 3 && l == 0)
      __hip_atomic_store((GAS u64*)(p.ws + WS_FLAG) + (j * 16 + s), epoch, RLX_AGENT);
    CWAIT(4, 0)
    CXPOSE(4)
    STAMP(8);
    poll_flags(p.ws + WS_FLAG + (size_t)(j * 16 + 4 * w) * 8, l, epoch);
    STAMP(9);
    f32x4 acc[4][4];
#pragma unroll
    for (int a = 0; a < 4; a++)
#pragma unroll
      for (int b = 0; b < 4; b++) acc[a][b] = f32x4{0, 0, 0, 0};
    v4u af[8][4];
    const char* ay = ab + (size_t)(56 + 8 * w) * 4096;
#define YI(K)                                                                                       \
  {                                                                                                 \
    gld16_sc1<0>(af[K][0], avoff, ay + (K) * 4096); gld16_sc1<1024>(af[K][1], avoff, ay + (K) * 4096); \
    gld16_sc1<2048>(af[K][2], avoff, ay + (K) * 4096); gld16_sc1<3072>(af[K][3], avoff, ay + (K) * 4096); \
  }
#define YS(K)                                                                                       \
  {                                                                                                 \
    wait_vm4<y_wait(K)>(af[K][0], af[K][1], af[K][2], af[K][3]);                                    \
    if (K == 7) { _Pragma("unroll") for (int n = 0; n < 4; n++) cf[7][n] = lds_rd16(lp(cra) + n * 1024); } \
    _Pragma("unroll") for (int nb = 0; nb < 4; nb++) _Pragma("unroll") for (int tb = 0; tb < 4; tb++)  \
        acc[nb][tb] = mfma16(cf[K][nb], af[K][tb], acc[nb][tb]);                                    \
    __builtin_amdgcn_sched_barrier(0);                                                              \
  }
    YI(0) YI(1) YI(2)
    YS(0) YI(3) YI(4) YS(1) YI(5) YI(6) YS(2) YI(7) YS(3) YS(4) YS(5) YS(6) YS(7)
#undef YI
#undef YS
    STAMP(10);
    cnt_arrive(lds, C_Y0);
    cnt_wait(lds, C_Y0, 4);
    cnt_wait(lds, C_B2, 4);
    {
      LAS char* yb = lds + ((w & 1) ? L_YB1 : L_YB0);
      if (w >= 2) cnt_wait(lds, C_Y1 + (w & 1), 1);
#pragma unroll
      for (int tb = 0; tb < 4; tb++) {
        const int tk = 32 * (tb >> 1) + 8 * (i16 >> 2) + 4 * (tb & 1) + (i16 & 3);
#pragma unroll
        for (int nb = 0; nb < 4; nb++) {
          LAS f32x4* d = (LAS f32x4*)(yb + tk * 256 + (((4 * nb + q) ^ i16) & 15) * 16);
          if (w >= 2) *d = *d + acc[nb][tb]; else *d = acc[nb][tb];
        }
      }
      if (w < 2) cnt_arrive(lds, C_Y1 + (w & 1));
    }
    STAMP(11);
  } else {
    gld8_sc1(ep, 0u, p.ws + WS_EPOCH);
#pragma unroll
    for (int m = 0; m < 4; m++) gld16<0>(xl[0][m], xvoff, xb + (8 * m) * 1024);
#pragma unroll
    for (int m = 0; m < 4; m++) gld16<256>(xl[1][m], xvoff, xb + (8 * m) * 1024);
#pragma unroll
    for (int m = 0; m < 4; m++) gld16<512>(xl[2][m], xvoff, xb + (8 * m) * 1024);
#pragma unroll
    for (int m = 0; m < 4; m++) gld16<768>(xl[3][m], xvoff, xb + (8 * m) * 1024);
    unsigned w1v[4];
    {
      const int winoff = 448 * (s >> 1) + (odd ? 192 : 0);
#pragma unroll
      for (int n = 0; n < 4; n++) {
        const int row = 16 * w + 4 * n + q;
        const int f = q | (((n >> 1) & 1) << 2);
        const int c16 = (((i16 >> 1) ^ f) << 1) | (i16 & 1);
        w1v[n] = (unsigned)(row * 3584 + winoff + c16 * 16 - 1024 * n);
      }
    }
#define W1DMA(c) dma4(ldsb + ((c) < 4 ? L_W1 + (c) * 16384 : L_WU + ((c) - 4) * 16384) + w * 4096, p.w1 + (size_t)(c) * 64 * 3584, w1v[0], w1v[1], w1v[2], w1v[3])
    W1DMA(0); W1DMA(1); W1DMA(2); W1DMA(3);
    STAMP(1);
    v4u xf[2][16];
    f32x4 asum[2], agr[2];
    asum[0] = asum[1] = agr[0] = agr[1] = f32x4{0, 0, 0, 0};
    unsigned xra[4];
#pragma unroll
    for (int t = 0; t < 4; t++) xra[t] = opaque(ldsb + L_XS + w * 8192 + sw4[t]);
#define FQ(QK, NW)                                                                                   \
  {                                                                                                  \
    wait_vm4<NW>(xl[QK][0], xl[QK][1], xl[QK][2], xl[QK][3]);                                        \
    _Pragma("unroll") for (int m = 0; m < 4; m++) *(LAS v4u*)lp(xwa[m]) = xl[QK][m];                 \
    bar();                                                                                           \
    if (QK > 0) W1DMA(3 + QK);                                                                       \
    _Pragma("unroll") for (int t = 0; t < 4; t++) {                                                  \
      xf[0][QK * 4 + t] = lds_rd16(lp(xra[t])); xf[1][QK * 4 + t] = lds_rd16(lp(xra[t]) + 4096);     \
    }                                                                                                \
    bar();                                                                                           \
    _Pragma("unroll") for (int t = 0; t < 4; t++) _Pragma("unroll") for (int b = 0; b < 2; b++) {    \
      asum[b] = mfma16(xf[b][QK * 4 + t], ones, asum[b]);                                            \
      agr[b] = mfma16(xf[b][QK * 4 + t], xf[b][QK * 4 + t], agr[b]);                                 \
    }                                                                                                \
  }
    FQ(0, 28) STAMP(2); FQ(1, 24) FQ(2, 24) FQ(3, 24)
#undef FQ
    {
#pragma unroll
      for (int b = 0; b < 2; b++) {
        const float sx = tp == 0 ? asum[b][0] : tp == 1 ? asum[b][1] : tp == 2 ? asum[b][2] : asum[b][3];
        const float sq = tp == 0 ? agr[b][0] : tp == 1 ? agr[b][1] : tp == 2 ? agr[b][2] : agr[b][3];
        const float mean = sx * (1.f / 512.f);
        float var = fmaf(-mean, mean, sq * (1.f / 512.f));
        var = fmaxf(var, 0.f);
        const float rstd = rsqrtf(var + 1e-5f);
        if (ta == q) *(LAS f32x2*)(lds + L_STAT + (w * 32 + b * 16 + i16) * 8) = f32x2{mean, rstd};
      }
    }
    asm volatile("s_waitcnt vmcnt(12)" : "+v"(ep) :: "memory");
    epoch = (((u64)ep.y << 32) | ep.x) + 1ull;
    bar();
    W1DMA(7);
#undef W1DMA
    STAMP(3);
    f32x4 ah[7][2];
#pragma unroll
    for (int mb = 0; mb < 7; mb++) { ah[mb][0] = f32x4{0, 0, 0, 0}; ah[mb][1] = f32x4{0, 0, 0, 0}; }
    {
      unsigned tro[7];
#pragma unroll
      for (int mb = 0; mb < 7; mb++) {
        const int wb = mb < 6 ? mb + 2 * odd : (odd ? 1 : 6);
        tro[mb] = opaque(ldsb + L_W1 + tr_row + (unsigned)((wb ^ trkey) * 32));
      }
#define HK(T)                                                                                      \
  {                                                                                                \
    v4u wf[7];                                                                                     \
    _Pragma("unroll") for (int mb = 0; mb < 7; mb++)                                               \
        wf[mb] = lds_tr(lp(tro[mb]) + ((T) & 7) * 8192, lp(tro[mb]) + ((T) & 7) * 8192 + 1024);    \
    _Pragma("unroll") for (int mb = 0; mb < 7; mb++) {                                             \
      ah[mb][0] = mfma16(wf[mb], xf[0][T], ah[mb][0]);                                             \
      ah[mb][1] = mfma16(wf[mb], xf[1][T], ah[mb][1]);                                             \
    }                                                                                              \
  }
      HK(0) HK(1) HK(2) HK(3) HK(4) HK(5) HK(6) HK(7)
      STAMP(4);
      wait_vm<0>();
      cnt_arrive(lds, C_W);
      cnt_wait(lds, C_W, 4);
#pragma unroll
      for (int mb = 0; mb < 7; mb++) tro[mb] = opaque(tro[mb] - 65536u);
      HK(8) HK(9) HK(10) HK(11) HK(12) HK(13) HK(14) HK(15)
#undef HK
    }
    STAMP(5);
    cnt_arrive(lds, C_B2);
    cnt_wait(lds, C_B2, 4);
    cnt_wait(lds, C_SH, 4);
    const unsigned ring = ldsb + L_RING + w * 28672;
    unsigned fv[4];
#pragma unroll
    for (int n = 0; n < 4; n++) {
      const int row = 8 * n + (l >> 3), c16p = l & 7;
      const int c16 = ((((c16p >> 1) ^ ((row >> 1) & 3)) << 1) | (c16p & 1));
      fv[n] = (unsigned)(row * 1024 + ct * 128 + c16 * 16 - 1024 * n);
    }
    const char* w2b = p.w2 + (size_t)(14 * w) * 32 * 1024;
#define WI(K) dma4(ring + ((K) % 7) * 4096, w2b + (size_t)(K) * 32 * 1024, fv[0], fv[1], fv[2], fv[3])
    (void)0;
    STAMP(6);
    {
      f32x2 stc[2];
      stc[0] = *(LAS f32x2*)(lds + L_STAT + (w * 32 + i16) * 8);
      stc[1] = *(LAS f32x2*)(lds + L_STAT + (w * 32 + 16 + i16) * 8);
      unsigned pkd[7][2][2];
#pragma unroll
      for (int mb = 0; mb < 7; mb++) {
        const f32x4 sh = *(LAS f32x4*)(lds + L_SH + (mb * 16 + 4 * q) * 4);
#pragma unroll
        for (int b = 0; b < 2; b++) {
          const float nm = -stc[b][0], rr = stc[b][1], r2 = stc[b][1] * -1.44269504088896f;
#pragma unroll
          for (int e = 0; e < 2; e++) {
            const f32x2 d = f32x2{sh[2 * e], sh[2 * e + 1]} * nm + f32x2{ah[mb][b][2 * e], ah[mb][b][2 * e + 1]};
            const f32x2 v = d * rr;
            const f32x2 tt = d * r2;
            f32x2 den = f32x2{__builtin_amdgcn_exp2f(tt[0]), __builtin_amdgcn_exp2f(tt[1])} + 1.f;
            const f32x2 y = v * f32x2{__builtin_amdgcn_rcpf(den[0]), __builtin_amdgcn_rcpf(den[1])};
            pkd[mb][b][e] = pk_bf16v(y);
          }
        }
      }
      const int ks0 = 7 * (s >> 1);
#pragma unroll
      for (int k = 0; k < 3; k++)
#pragma unroll
        for (int b = 0; b < 2; b++) {
          v4u v = {pkd[2 * k][b][0], pkd[2 * k][b][1], pkd[2 * k + 1][b][0], pkd[2 * k + 1][b][1]};
          st16(rs, (unsigned)WS_Z + zrow + (unsigned)(ks0 + k + 4 * odd) * 4096u + b * 1024u, v);
        }
#pragma unroll
      for (int b = 0; b < 2; b++) {
        v2u v = {pkd[6][b][0], pkd[6][b][1]};
        st8(rs, (unsigned)WS_Z + zrow + (unsigned)(ks0 + 3) * 4096u + b * 1024u + odd * 8u, v);
      }
    }
    STAMP(7);
    WI(0); WI(1); WI(2); WI(3); WI(4); WI(5); WI(6);
    wait_vm<28>();
    if (cnt_arrive(lds, C_FS) == 3 && l == 0)
      __hip_atomic_store((GAS u64*)(p.ws + WS_FLAG) + (256 + j * 16 + s), epoch, RLX_AGENT);
    STAMP(8);
    poll_flags(p.ws + WS_FLAG + (size_t)(256 + j * 16 + 4 * w) * 8, l, epoch);
    STAMP(9);
    f32x4 acc[4][4];
#pragma unroll
    for (int a = 0; a < 4; a++)
#pragma unroll
      for (int b = 0; b < 4; b++) acc[a][b] = f32x4{0, 0, 0, 0};
    v4u af[7][4];
    const char* afb = ab + (size_t)(14 * w) * 4096;
    unsigned ftr[4];
#pragma unroll
    for (int nb = 0; nb < 4; nb++) {
      const int row = 4 * q + ta;
      ftr[nb] = opaque(ring + (unsigned)(row * 128 + ((nb ^ ((row >> 1) & 3)) * 32) + tp * 8));
    }
#define FI(K)                                                                                       \
  {                                                                                                 \
    gld16_sc1<0>(af[(K) % 7][0], avoff, afb + (K) * 4096); gld16_sc1<1024>(af[(K) % 7][1], avoff, afb + (K) * 4096); \
    gld16_sc1<2048>(af[(K) % 7][2], avoff, afb + (K) * 4096); gld16_sc1<3072>(af[(K) % 7][3], avoff, afb + (K) * 4096); \
  }
#define FS(K)                                                                                       \
  {                                                                                                 \
    wait_vm4<f_wait(K)>(af[(K) % 7][0], af[(K) % 7][1], af[(K) % 7][2], af[(K) % 7][3]);            \
    v4u wf[4];                                                                                      \
    _Pragma("unroll") for (int nb = 0; nb < 4; nb++)                                                \
        wf[nb] = lds_tr(lp(ftr[nb]) + ((K) % 7) * 4096, lp(ftr[nb]) + ((K) % 7) * 4096 + 2048);     \
    _Pragma("unroll") for (int nb = 0; nb < 4; nb++) _Pragma("unroll") for (int tb = 0; tb < 4; tb++)  \
        acc[nb][tb] = mfma16(wf[nb], af[(K) % 7][tb], acc[nb][tb]);                                 \
    __builtin_amdgcn_sched_barrier(0);                                                              \
    if ((K) + 7 < 14) { FI((K) + 7) wait_lgkm(); WI((K) + 7); }                                     \
  }
    FI(0) FI(1) FI(2) FI(3) FI(4) FI(5) FI(6)
    FS(0) FS(1) FS(2) FS(3) FS(4) FS(5) FS(6) FS(7) FS(8) FS(9) FS(10) FS(11) FS(12) FS(13)
#undef FI
#undef FS
#undef WI
    STAMP(10);
    {
      LAS char* dst = lp(ring);
#pragma unroll
      for (int tb = 0; tb < 4; tb++) {
        const int tk = 32 * (tb >> 1) + 8 * (i16 >> 2) + 4 * (tb & 1) + (i16 & 3);
#pragma unroll
        for (int nb = 0; nb < 4; nb++)
          *(LAS f32x4*)(dst + tk * 256 + (((4 * nb + q) ^ i16) & 15) * 16) = acc[nb][tb];
      }
    }
    STAMP(11);
  }
  {
    const int ec = tid & 15, et = tid >> 4;
    v2u xe[2];
    const v2u dk = *(const GAS v2u*)(p.dskip + (64 * ct + 4 * ec) * 2);
#pragma unroll
    for (int n = 0; n < 2; n++)
      xe[n] = *(const GAS v2u*)(p.x + (size_t)(64 * hp2 + et + 32 * n) * 1024 + (64 * ct + 4 * ec) * 2);
    bar();
    STAMP(12);
    if (j == 15 && (r & 7) == 0 && (xcd & 1) == 0 && tid == 0)
      __hip_atomic_store((GAS u64*)(p.ws + WS_EPOCH), epoch, RLX_AGENT);
    const float dkf[4] = {bf2f(dk.x & 0xffff), bf2f(dk.x >> 16), bf2f(dk.y & 0xffff), bf2f(dk.y >> 16)};
#pragma unroll
    for (int n = 0; n < 2; n++) {
      const int tk = et + 32 * n;
      const int key = (tk & 3) | (((tk >> 3) & 3) << 2);
      const int o = tk * 256 + ((ec ^ key) & 15) * 16;
      LAS char* fb = lds + L_RING + o;
      const f32x4 f = (*(LAS f32x4*)(fb) + *(LAS f32x4*)(fb + 28672)) + (*(LAS f32x4*)(fb + 2 * 28672) + *(LAS f32x4*)(fb + 3 * 28672));
      const f32x4 y = *(LAS f32x4*)(lds + L_YB0 + o) + *(LAS f32x4*)(lds + L_YB1 + o);
      const int tkp = 64 * (hp2 & 1) + tk;
      const f32x2 se = *(LAS f32x2*)(lds + L_STAT + ((tkp >> 5) * 32 + ((tkp >> 2) & 1) * 16 + key) * 8);
      const float xv[4] = {bf2f(xe[n].x & 0xffff), bf2f(xe[n].x >> 16), bf2f(xe[n].y & 0xffff), bf2f(xe[n].y >> 16)};
      f32x4 o4;
#pragma unroll
      for (int e = 0; e < 4; e++) {
        const float xn = (xv[e] - se[0]) * se[1];
        const float sv = fmaf(xn, dkf[e], y[e]);
        const float g = f[e] * __builtin_amdgcn_rcpf(1.f + __builtin_amdgcn_exp2f(f[e] * -1.44269504088896f));
        o4[e] = fmaf(sv, g, xv[e]);
      }
      *(GAS f32x4*)(p.out + (size_t)(64 * hp2 + tk) * 512 + 64 * ct + 4 * ec) = o4;
    }
  }
  STAMP(13);
  STAMP_OUT();
}

extern "C" void kernel_launch(void* const* d_in, const int* in_sizes, int n_in, void* d_out, int out_size, void* d_ws,
                              size_t ws_size, hipStream_t stream) {
  Params p;
  p.x = (const char*)d_in[0]; p.lre = (const char*)d_in[1]; p.lim = (const char*)d_in[2];
  p.bre = (const char*)d_in[3]; p.bim = (const char*)d_in[4]; p.cre = (const char*)d_in[5]; p.cim = (const char*)d_in[6];
  p.logdt = (const char*)d_in[7]; p.dskip = (const char*)d_in[8]; p.w1 = (const char*)d_in[10]; p.w2 = (const char*)d_in[11];
  p.out = (float*)d_out; p.ws = (char*)d_ws;
  (void)hipFuncSetAttribute((const void*)k_fused, hipFuncAttributeMaxDynamicSharedMemorySize, LDS_BYTES);
  k_fused<<<256, 512, LDS_BYTES, stream>>>(p);
}
```

```cpp
#include <hip/hip_runtime.h>
#include <hip/hip_bf16.h>
#include <type_traits>

typedef unsigned v4u __attribute__((ext_vector_type(4)));
typedef unsigned v2u __attribute__((ext_vector_type(2)));
typedef float f32x4 __attribute__((ext_vector_type(4)));
typedef float f32x2 __attribute__((ext_vector_type(2)));
typedef short bf16x8 __attribute__((ext_vector_type(8)));
typedef short s16x4 __attribute__((ext_vector_type(4)));
typedef __bf16 bf16x2_t __attribute__((ext_vector_type(2)));
typedef unsigned long long u64;
#define LAS __attribute__((address_space(3)))
#define GAS __attribute__((address_space(1)))
#if defined(__HIP_DEVICE_COMPILE__)
#define NOPK __attribute__((target("no-packed-fp32-ops")))
#else
#define NOPK
#endif
#define DI __device__ __forceinline__ NOPK
#define RLX_AGENT __ATOMIC_RELAXED, __HIP_MEMORY_SCOPE_AGENT
#define RLX_WG __ATOMIC_RELAXED, __HIP_MEMORY_SCOPE_WORKGROUP

static constexpr unsigned HP_BYTES = 16 * 14336 + 32 * 4096;
static constexpr unsigned Y_OFF = 16 * 14336;
static constexpr size_t WS_Z = 0;
static constexpr size_t WS_Z_BYTES = (size_t)32 * HP_BYTES;
static constexpr size_t WS_GRAN = WS_Z + WS_Z_BYTES;
static constexpr size_t WS_SG = WS_GRAN + 16 * 16 * 512;
static constexpr size_t WS_FLAG = WS_SG + 2048 * 16;
static constexpr size_t WS_EPOCH = WS_FLAG + 2 * 16 * 16 * 8;
static constexpr size_t WS_END = WS_EPOCH + 128;

static constexpr int LDS_BYTES = 160 * 1024;
static constexpr int STG = 24576;
static constexpr int L_W1A = 96 * 1024;
static constexpr int L_W1B = 32 * 1024;
static constexpr int L_CST = 0;
static constexpr int L_YB1 = L_CST;
static constexpr int L_HT = 16 * 1024;
static constexpr int L_E = 24 * 1024;
static constexpr int L_KEEP = 28 * 1024;
static constexpr int L_YB0 = 32 * 1024;
static constexpr int L_RING = 48 * 1024;
static constexpr int L_STAT = L_KEEP;
static constexpr int L_SU = L_KEEP + 1024;
static constexpr int L_SH = L_KEEP + 1280;
static constexpr int L_CNT = L_KEEP + 1792;
static constexpr int RING = 28672;
static constexpr int L_HIN = L_KEEP + 2048;
enum { C_W = 9, C_SH = 10, C_B2 = 11, C_E = 0, C_CX = 1, C_HIN = 2, C_HS = 4, C_FS = 5, C_Y1 = 7 };
#define ROT 1
#define CIT 3
#define NOWARM 0
#define ALGKM 0
#define LATEC 1
#define VM0 0
#define CPACE 2
#define ROT2 1

DI bool wall(bool x) { return __builtin_amdgcn_ballot_w64(!x) == 0ull; }
DI f32x4 mfma16(v4u a, v4u b, f32x4 c) {
  return __builtin_amdgcn_mfma_f32_16x16x32_bf16(__builtin_bit_cast(bf16x8, a), __builtin_bit_cast(bf16x8, b), c, 0, 0, 0);
}
DI unsigned pk_bf16(float lo, float hi) {
  f32x2 v = {lo, hi};
  return __builtin_bit_cast(unsigned, __builtin_convertvector(v, bf16x2_t));
}
DI unsigned pk_bf16v(f32x2 v) { return __builtin_bit_cast(unsigned, __builtin_convertvector(v, bf16x2_t)); }
DI float bf2f(unsigned u) { return __builtin_bit_cast(float, u << 16); }
DI unsigned opaque(unsigned v) { asm volatile("" : "+v"(v)); return v; }
DI LAS char* lp(unsigned a) { return (LAS char*)(size_t)a; }

template <int IMM> DI void gld16(v4u& d, unsigned voff, const void* sb) {
  asm volatile("global_load_dwordx4 %0, %1, %2 offset:%3" : "=v"(d) : "v"(voff), "s"(sb), "i"(IMM) : "memory");
}
template <int IMM> DI void gld16_sc1(v4u& d, unsigned voff, const void* sb) {
  asm volatile("global_load_dwordx4 %0, %1, %2 offset:%3 sc1" : "=v"(d) : "v"(voff), "s"(sb), "i"(IMM) : "memory");
}
template <int IMM> DI void gld_u16(unsigned& d, unsigned voff, const void* sb) {
  asm volatile("global_load_ushort %0, %1, %2 offset:%3" : "=v"(d) : "v"(voff), "s"(sb), "i"(IMM) : "memory");
}
DI void gld8_sc1(v2u& d, unsigned voff, const void* sb) {
  asm volatile("global_load_dwordx2 %0, %1, %2 sc1" : "=v"(d) : "v"(voff), "s"(sb) : "memory");
}
DI void dma4(unsigned lds, const void* sb, unsigned v0, unsigned v1, unsigned v2, unsigned v3) {
  asm volatile(
      "s_mov_b32 m0, %0\n\ts_nop 0\n\t"
      "global_load_lds_dwordx4 %2, %1\n\t"
      "global_load_lds_dwordx4 %3, %1 offset:1024\n\t"
      "global_load_lds_dwordx4 %4, %1 offset:2048\n\t"
      "global_load_lds_dwordx4 %5, %1 offset:3072"
      :: "s"(lds), "s"(sb), "v"(v0), "v"(v1), "v"(v2), "v"(v3) : "memory");
}
template <int N> DI void wait_vm() { asm volatile("s_waitcnt vmcnt(%0)" :: "i"(N) : "memory"); }
template <int N> DI void wait_vm4(v4u& a, v4u& b, v4u& c, v4u& d) {
  asm volatile("s_waitcnt vmcnt(%4)" : "+v"(a), "+v"(b), "+v"(c), "+v"(d) : "i"(N) : "memory");
}
DI void wait_lgkm() { asm volatile("s_waitcnt lgkmcnt(0)" ::: "memory"); }
DI void bar() { wait_lgkm(); __builtin_amdgcn_s_barrier(); asm volatile("" ::: "memory"); }

DI v4u lds_rd16(LAS char* p) { return *(LAS v4u*)p; }
DI v4u lds_tr(LAS char* p0, LAS char* p1) {
  s16x4 a = __builtin_amdgcn_ds_read_tr16_b64_v4i16((LAS s16x4*)p0);
  s16x4 b = __builtin_amdgcn_ds_read_tr16_b64_v4i16((LAS s16x4*)p1);
  bf16x8 f = __builtin_shufflevector(a, b, 0, 1, 2, 3, 4, 5, 6, 7);
  return __builtin_bit_cast(v4u, f);
}
DI unsigned cnt_arrive(LAS char* lds, int c) {
  wait_lgkm();
  unsigned old = 0;
  if ((threadIdx.x & 63) == 0) old = __hip_atomic_fetch_add((LAS unsigned*)(lds + L_CNT) + c, 1u, RLX_WG);
  return __builtin_amdgcn_readfirstlane(old);
}
DI void cnt_wait(LAS char* lds, int c, unsigned target) {
  unsigned spins = 0;
  while ((unsigned)__builtin_amdgcn_readfirstlane(__hip_atomic_load((LAS unsigned*)(lds + L_CNT) + c, RLX_WG)) < target) {
    if (++spins > (1u << 24)) break;
    __builtin_amdgcn_s_sleep(1);
  }
  asm volatile("" ::: "memory");
}
struct cpx { float r, i; };
DI cpx cmul(cpx a, cpx b) { return {a.r * b.r - a.i * b.i, a.r * b.i + a.i * b.r}; }
DI cpx cfma(cpx a, cpx h, cpx u) {
  return {fmaf(a.r, h.r, fmaf(-a.i, h.i, u.r)), fmaf(a.r, h.i, fmaf(a.i, h.r, u.i))};
}
DI float fma1(float a, float b, float c) { float d; asm("v_fma_f32 %0, %1, %2, %3" : "=v"(d) : "v"(a), "v"(b), "v"(c)); return d; }
DI float fms1(float a, float b, float c) { float d; asm("v_fma_f32 %0, -%1, %2, %3" : "=v"(d) : "v"(a), "v"(b), "v"(c)); return d; }
DI cpx cfma_s(cpx a, cpx h, cpx u) {
  return {fma1(a.r, h.r, fms1(a.i, h.i, u.r)), fma1(a.r, h.i, fma1(a.i, h.r, u.i))};
}
DI cpx csel(bool c, cpx a, cpx b) { return {c ? a.r : b.r, c ? a.i : b.i}; }

#define STAMP(i)
#define STAMP_DECL
#define STAMP_OUT()

struct Params {
  const char *x, *lre, *lim, *bre, *bim, *cre, *cim, *logdt, *dskip, *w1, *w2;
  float* out;
  char* ws;
};

DI void st16(__amdgpu_buffer_rsrc_t rs, unsigned off, v4u v) { __builtin_amdgcn_raw_buffer_store_b128(v, rs, (int)off, 0, 16); }
DI void st8(__amdgpu_buffer_rsrc_t rs, unsigned off, v2u v) { __builtin_amdgcn_raw_buffer_store_b64(v, rs, (int)off, 0, 16); }

DI void poll_flags(const char* fl, int l, u64 epoch) {
  GAS u64* fp = (GAS u64*)fl + (l & 3);
  unsigned spins = 0;
  while (!wall(__hip_atomic_load(fp, RLX_AGENT) == epoch)) {
    if (++spins > (1u << 20)) break;
    __builtin_amdgcn_s_sleep(4);
  }
  asm volatile("" ::: "memory");
}

DI f32x4 mfma16h(v2u a, v2u b, f32x4 c) {
  return __builtin_amdgcn_mfma_f32_16x16x16bf16_1k(__builtin_bit_cast(s16x4, a), __builtin_bit_cast(s16x4, b), c, 0, 0, 0);
}
DI v2u lds_tr1(LAS char* p0) { return __builtin_bit_cast(v2u, __builtin_amdgcn_ds_read_tr16_b64_v4i16((LAS s16x4*)p0)); }
DI void dma2(unsigned lds, const void* sb, unsigned v0, unsigned v1) {
  asm volatile(
      "s_mov_b32 m0, %0\n\ts_nop 0\n\t"
      "global_load_lds_dwordx4 %2, %1\n\t"
      "global_load_lds_dwordx4 %3, %1 offset:1024"
      :: "s"(lds), "s"(sb), "v"(v0), "v"(v1) : "memory");
}
DI void dma_stage(unsigned lds, const void* xb, const void* ub, const void* wb, unsigned vx0, unsigned vx1, unsigned vu, unsigned vw0, unsigned vw1) {
  asm volatile(
      "s_mov_b32 m0, %0\n\ts_nop 0\n\t"
      "global_load_lds_dwordx4 %5, %2\n\t"
      "global_load_lds_dwordx4 %6, %2 offset:1024\n\t"
      "global_load_lds_dwordx4 %7, %3 offset:2048\n\t"
      "global_load_lds_dwordx4 %8, %4 offset:3072\n\t"
      "s_mov_b32 m0, %1\n\ts_nop 0\n\t"
      "global_load_lds_dwordx4 %9, %4"
      :: "s"(lds), "s"(lds + 4096u), "s"(xb), "s"(ub), "s"(wb), "v"(vx0), "v"(vx1), "v"(vu), "v"(vw0), "v"(vw1) : "memory");
}
template <int CTRL> DI float dpp_add(float v) {
  return v + __builtin_bit_cast(float, __builtin_amdgcn_update_dpp(0, __builtin_bit_cast(int, v), CTRL, 0xf, 0xf, true));
}
DI float wave_sum(float v) {
  v = dpp_add<0x111>(v); v = dpp_add<0x112>(v); v = dpp_add<0x114>(v); v = dpp_add<0x118>(v);
  v = v + __builtin_bit_cast(float, __builtin_amdgcn_update_dpp(0, __builtin_bit_cast(int, v), 0x142, 0xa, 0xf, false));
  v = v + __builtin_bit_cast(float, __builtin_amdgcn_update_dpp(0, __builtin_bit_cast(int, v), 0x143, 0xc, 0xf, false));
  return __builtin_bit_cast(float, __builtin_amdgcn_readlane(__builtin_bit_cast(int, v), 63));
}
DI u64 mk_gran(u64 epoch, float v) {
  unsigned vb = __builtin_bit_cast(unsigned, v);
  vb = (vb + 0x80u) >> 8;
  return (epoch << 24) | (u64)(vb & 0xffffffu);
}
DI void dma1(unsigned lds, const void* sb, unsigned v) {
  asm volatile("s_mov_b32 m0, %0\n\ts_nop 0\n\tglobal_load_lds_dwordx4 %2, %1" :: "s"(lds), "s"(sb), "v"(v) : "memory");
}
#define FD 3
constexpr int usz(int u) { return (u & 3) == 3 ? 2 : 4; }
constexpr int f_cnt(bool kindA, int u, int at, int st) {
  int n = 0; bool seen = false;
  for (int a = 0; a < FD; a++) { if (seen) n += usz(a); if (kindA && a == u) seen = true; }
  for (int v = 0; v <= at; v++) {
    const int steps = v < at ? usz(v) : st;
    for (int t = 0; t < steps; t++) {
      if (v + FD < 16) {
        const int np = usz(v + FD), ns = usz(v);
        for (int i = 0; i < np; i++)
          if (i * ns / np == t) { if (seen) n++; if (kindA && v + FD == u && i == np - 1) seen = true; }
      }
      if (v + 4 < 16) { if (seen) n++; if (!kindA && v + 4 == u && t == usz(v) - 1) seen = true; }
    }
  }
  return n;
}
#define SB0() __builtin_amdgcn_sched_barrier(0)

__global__ __launch_bounds__(512, 2) NOPK void k_fused(Params p) {
  extern __shared__ __attribute__((aligned(16))) char smem[];
  LAS char* lds = (LAS char*)smem;
  STAMP_DECL
  STAMP(0);
  const int tid = threadIdx.x, l = tid & 63;
  const int w8 = __builtin_amdgcn_readfirstlane(tid >> 6);
  const int role = w8 >> 2, w = w8 & 3;
  const int bid = blockIdx.x, xcd = bid & 7, r = bid >> 3;
  const int j = 4 * (xcd >> 1) + (r >> 3);
  const int s = 8 * (xcd & 1) + (r & 7);
  const int hp2 = 8 * (xcd >> 1) + (r >> 2);
  const int ct = 4 * (xcd & 1) + (r & 3);
  const int odd = s & 1;
  const int i16 = l & 15, q = l >> 4;
  const int ta = i16 >> 2, tp = i16 & 3;
  const unsigned ldsb = (unsigned)(size_t)lds;
  const __amdgpu_buffer_rsrc_t rs = __builtin_amdgcn_make_buffer_rsrc((void*)p.ws, 0, (int)WS_END, 0x00020000);
  const v4u ones = {0x3F803F80u, 0x3F803F80u, 0x3F803F80u, 0x3F803F80u};

  const int rot = ROT ? ((2 * (r >> 3) + (r & 7)) & 7) : 0;
  u64 ep0;
  asm volatile("s_load_dwordx2 %0, %1, 0x0" : "=s"(ep0) : "s"(p.ws + WS_EPOCH) : "memory");
  unsigned plr[2], pli[2], pdt[2];
  v4u xr[2];
  if (role == 0) {
    const char* rb = p.x + (size_t)(128 * j + 8 * s + 2 * w) * 1024;
    gld16<0>(xr[0], (unsigned)l * 16, rb); gld16<1024>(xr[1], (unsigned)l * 16, rb);
    const unsigned co = (unsigned)(32 * s + i16) * 2;
    gld_u16<0>(plr[0], co, p.lre); gld_u16<32>(plr[1], co, p.lre);
    gld_u16<0>(pli[0], co, p.lim); gld_u16<32>(pli[1], co, p.lim);
    gld_u16<0>(pdt[0], co, p.logdt); gld_u16<32>(pdt[1], co, p.logdt);
  }
  unsigned vx0, vx1, vu, vwA, vwB;
  {
    const int r8 = l >> 3, c8 = l & 7;
    const int tok0 = 8 * (r8 >> 2) + (r8 & 3);
    vx0 = (unsigned)(tok0 * 1024 + ((c8 ^ (r8 >> 1)) & 7) * 16);
    vx1 = (unsigned)((tok0 + 16) * 1024 + ((c8 ^ (4 + (r8 >> 1))) & 7) * 16);
    const int ch = 8 * w + r8, iu = 8 * (w & 1) + r8;
    vu = (unsigned)(ch * 1024 + ((c8 ^ (iu >> 1)) & 7) * 16);
    const int winoff = 448 * (s >> 1) + (odd ? 192 : 0);
    vwA = (unsigned)((16 * w + q) * 3584 + winoff + ((((i16 >> 1) ^ q) << 1) | (i16 & 1)) * 16);
    vwB = (unsigned)((16 * w + 8 + q) * 3584 + winoff + ((((i16 >> 1) ^ (q | 4)) << 1) | (i16 & 1)) * 16);
  }
  const char* xb = p.x + (size_t)(128 * j + 32 * (w >> 1) + 4 * (w & 1)) * 1024;
  const char* ub0 = p.bre + (size_t)(32 * s) * 1024;
  const char* ub1 = p.bim + (size_t)(32 * s) * 1024;
#define PIECE(N, I)                                                                                  \
  {                                                                                                  \
    constexpr int h_ = (N) / 3, n_ = (N) % 3;                                                        \
    const int kq_ = ((I) + rot) & 7;                                                                 \
    const unsigned d_ = ldsb + w * 3072 + ((I) & 3) * STG + h_ * 12288 + n_ * 1024;                  \
    if (n_ == 0) dma1(d_, xb + h_ * 65536 + kq_ * 128, vx0);                                         \
    if (n_ == 1) dma1(d_, xb + h_ * 65536 + kq_ * 128, vx1);                                         \
    if (n_ == 2) dma1(d_, (h_ ? ub1 : ub0) + kq_ * 128, vu);                                         \
  }
#define PALL(I) PIECE(0, I) PIECE(1, I) PIECE(2, I) PIECE(3, I) PIECE(4, I) PIECE(5, I)
#define W1P(C, n) dma1(ldsb + ((C) < 4 ? L_W1A + (C) * 16384 : L_W1B + ((C) - 4) * 16384) + (16 * w + 4 * (n)) * 256,          \
                       p.w1 + (size_t)((((C) + rot) & 7) * 64 + ((n) & 1) * 4) * 3584, ((n) & 2) ? vwB : vwA)
  if (role == 1) {
    PALL(0)
    __builtin_amdgcn_s_barrier();
    PALL(1)
  } else {
    __builtin_amdgcn_s_barrier();
  }
  asm volatile("s_waitcnt lgkmcnt(0)" : "+s"(ep0) :: "memory");
  const u64 epoch = ep0 + 1ull;
  const unsigned tlo = (unsigned)(epoch & 0xffu), thi = (unsigned)(epoch >> 8);

  const unsigned sw0 = (unsigned)((i16 & 7) * 128 + (((0 + q) ^ (i16 >> 1)) & 7) * 16);
  const unsigned sw1 = (unsigned)((i16 & 7) * 128 + (((4 + q) ^ (i16 >> 1)) & 7) * 16);
  const unsigned xo = (unsigned)(2 * w * 3072 + (i16 >> 3) * 1024);
  const unsigned tro = (unsigned)((8 * q + ta) * 256 + 8 * tp);
  const int trkey = ta | ((q & 1) << 2);
  const unsigned zb = (unsigned)(2 * j + (w >> 1)) * HP_BYTES + (unsigned)l * 16u;
  const char* ab = p.ws + WS_Z + (size_t)hp2 * HP_BYTES;
  const unsigned avoff = (unsigned)l * 16;
  const int rot2 = ROT2 ? (hp2 & 3) : 0;

  if (role == 0) {
    asm volatile("s_waitcnt vmcnt(0)" : "+v"(plr[0]), "+v"(plr[1]), "+v"(pli[0]), "+v"(pli[1]), "+v"(pdt[0]), "+v"(pdt[1]), "+v"(xr[0]), "+v"(xr[1]) :: "memory");
#pragma unroll
    for (int n = 0; n < 2; n++) {
      float v[8];
#pragma unroll
      for (int e = 0; e < 4; e++) { v[2 * e] = bf2f(xr[n][e] & 0xffffu); v[2 * e + 1] = __builtin_bit_cast(float, xr[n][e] & 0xffff0000u); }
      const float mean = wave_sum(((v[0] + v[1]) + (v[2] + v[3])) + ((v[4] + v[5]) + (v[6] + v[7]))) * (1.f / 512.f);
      float sq = 0.f;
#pragma unroll
      for (int e = 0; e < 8; e++) { const float d = v[e] - mean; sq = fmaf(d, d, sq); }
      const float rstd = rsqrtf(wave_sum(sq) * (1.f / 512.f) + 1e-5f);
      if (l < 2)
        __hip_atomic_store((GAS u64*)(p.ws + WS_SG) + (size_t)(128 * j + 8 * s + 2 * w + n) * 2 + l, mk_gran(epoch, l ? rstd : mean), RLX_AGENT);
    }
    cpx a1[2], cc[2], a8[2], pw[2], a128[2];
#pragma unroll
    for (int nb = 0; nb < 2; nb++) {
      const float lr = bf2f(plr[nb]), li = bf2f(pli[nb]);
      const float dtf = expf(bf2f(pdt[nb]));
      const float dt = bf2f(pk_bf16(dtf, 0.f) & 0xffffu);
      const float e = expf(dt * lr);
      float sn, cs;
      sincosf(dt * li, &sn, &cs);
      cpx a = {e * cs, e * sn};
      a1[nb] = a;
      const float nr = a.r - 1.f, ni = a.i, den = lr * lr + li * li;
      cc[nb] = {2.f * (nr * lr + ni * li) / den, 2.f * (ni * lr - nr * li) / den};
      cpx t = cmul(a, a); t = cmul(t, t); t = cmul(t, t);
      a8[nb] = t;
      cpx a16 = cmul(t, t), a32 = cmul(a16, a16), a64 = cmul(a32, a32);
      a128[nb] = cmul(a64, a64);
      cpx one = {1.f, 0.f};
      cpx pq = cmul(csel(q & 1, t, one), csel(q & 2, a16, one));
      cpx pwv = cmul(csel(w & 1, a32, one), csel(w & 2, a64, one));
      pw[nb] = cmul(pq, pwv);
    }
#pragma unroll
    for (int nb = 0; nb < 2; nb++)
      asm volatile("" : "+v"(a1[nb].r), "+v"(a1[nb].i), "+v"(cc[nb].r), "+v"(cc[nb].i), "+v"(a8[nb].r), "+v"(a8[nb].i), "+v"(pw[nb].r),
                   "+v"(pw[nb].i), "+v"(a128[nb].r), "+v"(a128[nb].i));
    STAMP(1);
    v4u cf[8][4];
    const unsigned cvoff = (unsigned)((64 * ct + (l >> 2)) * 1024 + (l & 3) * 16);
#define CLOAD(K0)                                                                         \
  _Pragma("unroll") for (int k = K0; k < K0 + 4; k++) {                                   \
    const int kr = (k + 2 * rot2) & 7;                                                    \
    const int sidx = 4 * w + 2 * (kr >> 2) + (kr & 1);                                    \
    const char* cb = (((kr >> 1) & 1) ? p.cim : p.cre) + sidx * 64;                       \
    _Pragma("unroll") for (int n = 0; n < 4; n++) gld16<0>(cf[k][n], cvoff, cb + n * 16384); \
    if (CPACE) __builtin_amdgcn_s_sleep(CPACE);                                           \
  }
#define CWAIT(K0, N)                                                                      \
  asm volatile("s_waitcnt vmcnt(%16)" : "+v"(cf[K0][0]), "+v"(cf[K0][1]), "+v"(cf[K0][2]), "+v"(cf[K0][3]), "+v"(cf[K0 + 1][0]),   \
               "+v"(cf[K0 + 1][1]), "+v"(cf[K0 + 1][2]), "+v"(cf[K0 + 1][3]), "+v"(cf[K0 + 2][0]), "+v"(cf[K0 + 2][1]),          \
               "+v"(cf[K0 + 2][2]), "+v"(cf[K0 + 2][3]), "+v"(cf[K0 + 3][0]), "+v"(cf[K0 + 3][1]), "+v"(cf[K0 + 3][2]),          \
               "+v"(cf[K0 + 3][3]) : "i"(N) : "memory");
#define CXPOSE(K0)                                                                        \
  _Pragma("unroll") for (int k = K0; k < K0 + 4; k++) {                                   \
    _Pragma("unroll") for (int n = 0; n < 4; n++) *(LAS v4u*)(lp(cwa) + n * 1024) = cf[k][n]; \
    _Pragma("unroll") for (int n = 0; n < 4; n++) cf[k][n] = lds_rd16(lp(cra) + n * 1024);  \
  }
    v2u sg;
    const unsigned sgoff = (unsigned)l * 8;
    const char* sgb = p.ws + WS_SG + (size_t)(128 * j + 32 * w) * 16;
    f32x4 au[2][2][2], asu;
#pragma unroll
    for (int b = 0; b < 2; b++)
#pragma unroll
      for (int nb = 0; nb < 2; nb++) { au[b][nb][0] = f32x4{0, 0, 0, 0}; au[b][nb][1] = f32x4{0, 0, 0, 0}; }
    asu = f32x4{0, 0, 0, 0};
    {
      const unsigned uo = (unsigned)((i16 >> 3) * 3072 + 2048);
      const int gs = (w & 1) * 2 + (w >> 1);
      unsigned cur = 0;
      v4u xd[2][2], wd[2][4], sd[2];
#pragma unroll
      for (int t = 0; t < 2; t++) xd[t][0] = xd[t][1] = wd[t][0] = wd[t][1] = wd[t][2] = wd[t][3] = sd[t] = v4u{0, 0, 0, 0};
#define A_STEP(RD, T, PX, PU, PS)                                                                     \
  {                                                                                                   \
    _Pragma("unroll") for (int g = 0; g < 4; g++) {                                                   \
      au[0][g & 1][g >> 1] = mfma16(xd[T][0], wd[T][g], au[0][g & 1][g >> 1]);                        \
      au[1][g & 1][g >> 1] = mfma16(xd[T][1], wd[T][g], au[1][g & 1][g >> 1]);                        \
      if (RD) wd[T][g] = lds_rd16(PU + g * 6144);                                                     \
      SB0();                                                                                          \
    }                                                                                                 \
    asu = mfma16(ones, sd[T], asu);                                                                   \
    if (RD) { xd[T][0] = lds_rd16(PX); xd[T][1] = lds_rd16(PX + 3072); sd[T] = lds_rd16(PS); }        \
    SB0();                                                                                            \
  }
#pragma unroll 1
      for (int it = 0; it < 8; it++) {
        const unsigned b0 = ldsb + cur;
        LAS char* px0 = lp(opaque(b0 + xo + sw0)); LAS char* px1 = lp(opaque(b0 + xo + sw1));
        LAS char* pu0 = lp(opaque(b0 + uo + sw0)); LAS char* pu1 = lp(opaque(b0 + uo + sw1));
        LAS char* ps0 = lp(opaque(b0 + uo + sw0 + gs * 6144)); LAS char* ps1 = lp(opaque(b0 + uo + sw1 + gs * 6144));
        cur = cur >= 3 * STG ? 0 : cur + STG;
        if (ALGKM) wait_lgkm();
        asm volatile("" ::: "memory");
        __builtin_amdgcn_s_barrier();
        asm volatile("" ::: "memory");
        SB0();
        A_STEP(true, 0, px0, pu0, ps0)
        A_STEP(true, 1, px1, pu1, ps1)
      }
      gld8_sc1(sg, sgoff, sgb);
      A_STEP(false, 0, lds, lds, lds)
      A_STEP(false, 1, lds, lds, lds)
#undef A_STEP
    }
    STAMP(2);
    {
      asm volatile("s_waitcnt vmcnt(0)" : "+v"(sg) :: "memory");
      if (tid < 16) ((LAS unsigned*)(lds + L_CNT))[tid] = 0u;
      unsigned spins = 0;
      while (!wall(sg.y == thi && (sg.x >> 24) == tlo)) {
        if (++spins > (1u << 22)) break;
        __builtin_amdgcn_s_sleep(1);
        gld8_sc1(sg, sgoff, sgb);
        asm volatile("s_waitcnt vmcnt(0)" : "+v"(sg) :: "memory");
      }
      const int tk = l >> 1;
      ((LAS unsigned*)(lds + L_STAT))[(w * 32 + ((tk >> 2) & 1) * 16 + ((tk & 3) | ((tk >> 3) << 2))) * 2 + (l & 1)] = sg.x << 8;
      if (q == 0) ((LAS float*)(lds + L_SU))[w * 16 + i16] = asu[0];
    }
    bar();
    STAMP(3);
    if (!LATEC) { CLOAD(0) }
    cpx u[2][8];
    {
      f32x2 stt[2][4];
#pragma unroll
      for (int b = 0; b < 2; b++) {
        f32x4 s0 = *(LAS f32x4*)(lds + L_STAT + (w * 32 + b * 16 + 4 * q) * 8);
        f32x4 s1 = *(LAS f32x4*)(lds + L_STAT + (w * 32 + b * 16 + 4 * q) * 8 + 16);
        stt[b][0] = f32x2{s0[0], s0[1]}; stt[b][1] = f32x2{s0[2], s0[3]};
        stt[b][2] = f32x2{s1[0], s1[1]}; stt[b][3] = f32x2{s1[2], s1[3]};
      }
#pragma unroll
      for (int nb = 0; nb < 2; nb++) {
        const float sre = ((LAS float*)(lds + L_SU))[(nb * 2 + 0) * 16 + i16];
        const float sim = ((LAS float*)(lds + L_SU))[(nb * 2 + 1) * 16 + i16];
        cpx h = {0.f, 0.f};
#pragma unroll
        for (int k = 0; k < 8; k++) {
          const int b = k >> 2, rg = k & 3;
          const float m = stt[b][rg][0], rr = stt[b][rg][1];
          u[nb][k].r = fmaf(-m, sre, au[b][nb][0][rg]) * rr;
          u[nb][k].i = fmaf(-m, sim, au[b][nb][1][rg]) * rr;
          h = cfma(a1[nb], h, u[nb][k]);
        }
        *(LAS f32x2*)(lds + L_E + ((nb * 16 + i16) * 16 + 4 * w + q) * 8) = f32x2{h.r, h.i};
      }
    }
    cnt_arrive(lds, C_E);
    cnt_wait(lds, C_E, 4);
    cpx cl[2];
    {
      cpx c[2] = {{0.f, 0.f}, {0.f, 0.f}};
      LAS f32x4* e4a = (LAS f32x4*)(lds + L_E + i16 * 128);
      LAS f32x4* e4b = (LAS f32x4*)(lds + L_E + (16 + i16) * 128);
#pragma unroll 1
      for (int k = 0; k < 2 * w; k++) {
        f32x4 ea = e4a[k], eb = e4b[k];
        c[0] = cfma(a8[0], c[0], cpx{ea[0], ea[1]}); c[1] = cfma(a8[1], c[1], cpx{eb[0], eb[1]});
        c[0] = cfma(a8[0], c[0], cpx{ea[2], ea[3]}); c[1] = cfma(a8[1], c[1], cpx{eb[2], eb[3]});
      }
      cpx tot[2];
#pragma unroll
      for (int nb = 0; nb < 2; nb++) {
        LAS f32x4* e4 = nb ? e4b : e4a;
        f32x4 e0 = e4[2 * w], e1 = e4[2 * w + 1];
        cpx c1 = cfma(a8[nb], c[nb], cpx{e0[0], e0[1]});
        cpx c2 = cfma(a8[nb], c1, cpx{e0[2], e0[3]});
        cpx c3 = cfma(a8[nb], c2, cpx{e1[0], e1[1]});
        tot[nb] = cfma(a8[nb], c3, cpx{e1[2], e1[3]});
        cl[nb] = csel(q & 2, csel(q & 1, c3, c2), csel(q & 1, c1, c[nb]));
      }
      if (w == 3 && j < 15) {
        const cpx t = (q & 2) ? tot[1] : tot[0];
        const float v = (q & 1) ? t.i : t.r;
        const u64 g = mk_gran(epoch, v);
        GAS u64* gp = (GAS u64*)(p.ws + WS_GRAN + (size_t)(j * 16 + s) * 512) + (((q >> 1) * 16 + i16) * 2 + (q & 1));
        __hip_atomic_store(gp, g, RLX_AGENT);
      }
    }
    STAMP(4);
    const unsigned cwa = opaque(ldsb + L_CST + w * 4096 + (l >> 2) * 64 + (((l & 3) ^ ((-(l >> 4)) & 3)) * 16));
    const unsigned cra = opaque(ldsb + L_CST + w * 4096 + i16 * 64 + ((q ^ ((-(i16 >> 2)) & 3)) * 16));
    if (w == 0) {
      const char* gb = p.ws + WS_GRAN + (size_t)s * 512;
      unsigned spins = 0;
      if (j > 0) {
        GAS u64* pp = (GAS u64*)(gb + (size_t)(l < j ? l : 0) * 8192 + 504);
        while (!wall((__hip_atomic_load(pp, RLX_AGENT) >> 24) == (epoch & 0xffffffffffull))) {
          if (++spins > (1u << 20)) break;
          __builtin_amdgcn_s_sleep(4);
        }
      }
      v4u gr[2][4];
      for (;;) {
#pragma unroll
        for (int m = 0; m < 4; m++) {
          const int pn = (4 * m + q) < j ? 4 * m + q : 0;
          const unsigned go = (unsigned)(pn * 8192 + i16 * 16);
          gld16_sc1<0>(gr[0][m], go, gb);
          gld16_sc1<256>(gr[1][m], go, gb);
        }
        asm volatile("s_waitcnt vmcnt(0)" : "+v"(gr[0][0]), "+v"(gr[0][1]), "+v"(gr[0][2]), "+v"(gr[0][3]),
                     "+v"(gr[1][0]), "+v"(gr[1][1]), "+v"(gr[1][2]), "+v"(gr[1][3]) :: "memory");
        bool ok = true;
#pragma unroll
        for (int nb = 0; nb < 2; nb++)
#pragma unroll
          for (int m = 0; m < 4; m++) {
            const bool need = (4 * m + q) < j;
            const bool good = (gr[nb][m].y == thi) && ((gr[nb][m].x >> 24) == tlo) && (gr[nb][m].w == thi) && ((gr[nb][m].z >> 24) == tlo);
            ok = ok && (good || !need);
          }
        if (wall(ok)) break;
        if (++spins > (1u << 20)) break;
        __builtin_amdgcn_s_sleep(4);
      }
#pragma unroll
      for (int nb = 0; nb < 2; nb++) {
        const cpx a256 = cmul(a128[nb], a128[nb]);
        const cpx a512 = cmul(a256, a256);
        cpx acc = {0.f, 0.f};
#pragma unroll
        for (int m = 0; m < 4; m++) {
          const bool need = (4 * m + q) < j;
          cpx e = {__builtin_bit_cast(float, gr[nb][m].x << 8), __builtin_bit_cast(float, gr[nb][m].z << 8)};
          acc = csel(need, cfma(a512, acc, e), acc);
        }
        const int dd = (j - 1 - q) & 3;
        cpx one = {1.f, 0.f};
        cpx fz = cmul(csel(dd & 1, a128[nb], one), csel(dd & 2, a256, one));
        acc = cmul(acc, fz);
        *(LAS f32x2*)(lds + L_HIN + ((nb * 16 + i16) * 4 + q) * 8) = f32x2{acc.r, acc.i};
      }
      cnt_arrive(lds, C_HIN);
    } else {
      const int m0 = w == 1 ? 0 : 2 * w - 1, m2 = w == 1 ? 2 : 2 * w;
      unsigned t0 = ldsb + L_W1A + tro + (unsigned)(((m0 + odd) ^ trkey) * 32);
      unsigned t1 = ldsb + L_W1A + tro + (unsigned)(((m0 + 1 + odd) ^ trkey) * 32);
      unsigned t2 = ldsb + L_W1A + tro + (unsigned)(((m2 + odd) ^ trkey) * 32);
      f32x4 s0 = {0, 0, 0, 0}, s1 = {0, 0, 0, 0}, s2 = {0, 0, 0, 0};
#pragma unroll 1
      for (int hf = 0; hf < 4; hf++) {
        if (hf == 2) { cnt_wait(lds, C_W, 4); t0 -= 131072u; t1 -= 131072u; t2 -= 131072u; }
        v4u f0[4], f1[4], f2[4];
#pragma unroll
        for (int t = 0; t < 4; t++) {
          f0[t] = lds_tr(lp(t0) + t * 8192, lp(t0) + t * 8192 + 1024);
          f1[t] = lds_tr(lp(t1) + t * 8192, lp(t1) + t * 8192 + 1024);
          f2[t] = lds_tr(lp(t2) + t * 8192, lp(t2) + t * 8192 + 1024);
        }
#pragma unroll
        for (int t = 0; t < 4; t++) { s0 = mfma16(f0[t], ones, s0); s1 = mfma16(f1[t], ones, s1); s2 = mfma16(f2[t], ones, s2); }
        t0 += 32768u; t1 += 32768u; t2 += 32768u;
      }
      if (i16 == 0) {
        *(LAS f32x4*)(lds + L_SH + (m0 * 16 + 4 * q) * 4) = s0;
        *(LAS f32x4*)(lds + L_SH + ((m0 + 1) * 16 + 4 * q) * 4) = s1;
        if (w == 1) *(LAS f32x4*)(lds + L_SH + (2 * 16 + 4 * q) * 4) = s2;
      }
      cnt_arrive(lds, C_SH);
    }
    if (LATEC) { CLOAD(0) }
    CWAIT(0, 0)
    CXPOSE(0)
    cnt_wait(lds, C_HIN, 1);
    STAMP(5);
    {
      LAS char* ht = lds + L_HT + w * 2048;
      unsigned hpk[2][2][2][2];
#pragma unroll
      for (int nb = 0; nb < 2; nb++) {
        const f32x4 h01 = *(LAS f32x4*)(lds + L_HIN + (nb * 16 + i16) * 32);
        const f32x4 h23 = *(LAS f32x4*)(lds + L_HIN + (nb * 16 + i16) * 32 + 16);
        const cpx acc = {(h01[0] + h01[2]) + (h23[0] + h23[2]), (h01[1] + h01[3]) + (h23[1] + h23[3])};
        cpx h = cfma(pw[nb], acc, cl[nb]);
        float orr[8], oi[8];
#pragma unroll
        for (int k = 0; k < 8; k++) {
          h = cfma(a1[nb], h, u[nb][k]);
          orr[k] = cc[nb].r * h.r - cc[nb].i * h.i;
          oi[k] = -(cc[nb].r * h.i + cc[nb].i * h.r);
        }
#pragma unroll
        for (int b = 0; b < 2; b++) {
          hpk[nb][0][b][0] = pk_bf16(orr[4 * b], orr[4 * b + 1]); hpk[nb][0][b][1] = pk_bf16(orr[4 * b + 2], orr[4 * b + 3]);
          hpk[nb][1][b][0] = pk_bf16(oi[4 * b], oi[4 * b + 1]);   hpk[nb][1][b][1] = pk_bf16(oi[4 * b + 2], oi[4 * b + 3]);
        }
      }
      const unsigned zy = zb + Y_OFF + (unsigned)(4 * (s >> 1) + (s & 1)) * 4096u + (unsigned)(2 * (w & 1)) * 1024u;
#pragma unroll
      for (int b = 0; b < 2; b++) {
#pragma unroll
        for (int nb = 0; nb < 2; nb++)
#pragma unroll
          for (int part = 0; part < 2; part++)
            *(LAS v2u*)(ht + (part * 32 + nb * 16 + i16) * 32 + q * 8) = v2u{hpk[nb][part][b][0], hpk[nb][part][b][1]};
#pragma unroll
        for (int part = 0; part < 2; part++) {
          LAS char* rp = ht + (part * 32 + 8 * q + ta) * 32 + tp * 8;
          v4u v = lds_tr(rp, rp + 4 * 32);
          st16(rs, (unsigned)WS_Z + zy + (unsigned)part * 8192u + b * 1024u, v);
        }
      }
    }
    STAMP(6);
    wait_vm<0>();
    if (cnt_arrive(lds, C_HS) == 3 && l == 0)
      __hip_atomic_store((GAS u64*)(p.ws + WS_FLAG) + (j * 16 + s), epoch, RLX_AGENT);
    STAMP(7);
    CLOAD(4)
    CWAIT(4, 0)
    CXPOSE(4)
    cnt_arrive(lds, C_CX);
    STAMP(8);
    poll_flags(p.ws + WS_FLAG + (size_t)(j * 16 + 4 * w) * 8, l, epoch);
    STAMP(9);
    f32x4 acc[4][4];
#pragma unroll
    for (int a = 0; a < 4; a++)
#pragma unroll
      for (int b = 0; b < 4; b++) acc[a][b] = f32x4{0, 0, 0, 0};
    v4u af[3][4];
    const char* ay = ab + Y_OFF + (size_t)(8 * w) * 4096;
#define YL(K, i) gld16_sc1<(i) * 1024>(af[(K) % 3][i], avoff, ay + ((((K) + 2 * rot2) & 7) * 4096))
#define YST(K, tb)                                                                                  \
  {                                                                                                 \
    _Pragma("unroll") for (int nb = 0; nb < 4; nb++) acc[nb][tb] = mfma16(cf[K][nb], af[(K) % 3][tb], acc[nb][tb]); \
    SB0();                                                                                          \
    if ((K) + 2 < 8) YL((K) + 2, tb);                                                               \
    SB0();                                                                                          \
  }
#define YS(K)                                                                                       \
  {                                                                                                 \
    wait_vm4<((K) < 7 ? 4 : 0)>(af[(K) % 3][0], af[(K) % 3][1], af[(K) % 3][2], af[(K) % 3][3]);    \
    YST(K, 0) YST(K, 1) YST(K, 2) YST(K, 3)                                                         \
  }
    YL(0, 0); YL(0, 1); YL(0, 2); YL(0, 3); YL(1, 0); YL(1, 1); YL(1, 2); YL(1, 3);
    YS(0) YS(1) YS(2) YS(3) YS(4) YS(5) YS(6) YS(7)
#undef YL
#undef YS
#undef YST
    STAMP(10);
    cnt_wait(lds, C_CX, 4);
    cnt_wait(lds, C_B2, 4);
    {
      LAS char* yb = lds + ((w & 1) ? L_YB1 : L_YB0);
      if (w < 2) cnt_wait(lds, C_Y1 + (w & 1), 1);
#pragma unroll
      for (int tb = 0; tb < 4; tb++) {
        const int tk = 32 * (tb >> 1) + 8 * (i16 >> 2) + 4 * (tb & 1) + (i16 & 3);
#pragma unroll
        for (int nb = 0; nb < 4; nb++) {
          LAS f32x4* d = (LAS f32x4*)(yb + tk * 256 + (((4 * nb + q) ^ i16) & 15) * 16);
          if (w < 2) *d = *d + acc[nb][tb]; else *d = acc[nb][tb];
        }
      }
      if (w >= 2) cnt_arrive(lds, C_Y1 + (w & 1));
    }
    STAMP(11);
  } else {
    v4u xf[2][16];
    {
      LAS char* px0 = lp(opaque(ldsb + xo + sw0)); LAS char* px1 = lp(opaque(ldsb + xo + sw1));
#define L1IT(I, NW)                                                                                   \
  {                                                                                                   \
    wait_vm<VM0 ? 0 : NW>();                                                                                    \
    __builtin_amdgcn_s_barrier();                                                                     \
    asm volatile("" ::: "memory");                                                                    \
    xf[0][2 * (I)] = lds_rd16(px0 + ((I) & 3) * STG); xf[1][2 * (I)] = lds_rd16(px0 + ((I) & 3) * STG + 3072); \
    xf[0][2 * (I) + 1] = lds_rd16(px1 + ((I) & 3) * STG); xf[1][2 * (I) + 1] = lds_rd16(px1 + ((I) & 3) * STG + 3072); \
    SB0();                                                                                            \
    if ((I) < 6) { PALL((I) + 2) }                                                                    \
    W1P((I) >> 1, 2 * ((I) & 1)); W1P((I) >> 1, 2 * ((I) & 1) + 1);                                   \
    wait_lgkm();                                                                                      \
  }
      L1IT(0, 6) L1IT(1, 8) L1IT(2, 10) STAMP(15); L1IT(3, 10) L1IT(4, 10) L1IT(5, 10) L1IT(6, 10) L1IT(7, 4)
#undef L1IT
    }
    STAMP(2);
    wait_vm<0>();
    bar();
    STAMP(3);
    f32x4 ah[7][2];
#pragma unroll
    for (int mb = 0; mb < 7; mb++) { ah[mb][0] = f32x4{0, 0, 0, 0}; ah[mb][1] = f32x4{0, 0, 0, 0}; }
    {
      unsigned pt[7];
#pragma unroll
      for (int mb = 0; mb < 7; mb++) pt[mb] = opaque(ldsb + L_W1A + tro + (unsigned)(((mb + odd) ^ trkey) * 32));
      v4u wd[7];
      unsigned wdum = 0;
      const unsigned wv2 = (unsigned)((l >> 5) * 1024 + (l & 31) * 16);
#pragma unroll
      for (int mb = 0; mb < 7; mb++) wd[mb] = lds_tr(lp(pt[mb]), lp(pt[mb]) + 1024);
#define WARM(K)                                                                                       \
  {                                                                                                   \
    const int pid = ((K) * 32 + r) * 4 + w, c = (pid - 896) & 511;                                    \
    const char* src = pid < 896 ? p.w2 + (size_t)pid * 2048 + (xcd & 1) * 512                         \
                                : ((c & 256) ? p.cim : p.cre) + (size_t)((xcd & 1) * 256 + (c & 255)) * 1024; \
    asm volatile("global_load_dword %0, %1, %2" : "+v"(wdum) : "v"(pid < 896 ? wv2 : avoff), "s"(src) : "memory"); \
  }
#define HK(T)                                                                                         \
  {                                                                                                   \
    if ((T) == 7) {                                                                                   \
      wait_vm<NOWARM ? 0 : 3>();                                                                                   \
      cnt_arrive(lds, C_W);                                                                           \
      cnt_wait(lds, C_W, 4);                                                                          \
      _Pragma("unroll") for (int mb = 0; mb < 7; mb++) pt[mb] = opaque(pt[mb] - 65536u);              \
    }                                                                                                 \
    _Pragma("unroll") for (int mb = 0; mb < 7; mb++) {                                                \
      ah[mb][0] = mfma16(wd[mb], xf[0][T], ah[mb][0]);                                                \
      ah[mb][1] = mfma16(wd[mb], xf[1][T], ah[mb][1]);                                                \
      if ((T) < 15) wd[mb] = lds_tr(lp(pt[mb]) + (((T) + 1) & 7) * 8192, lp(pt[mb]) + (((T) + 1) & 7) * 8192 + 1024); \
      if ((T) < 4 && (mb & 1) == 0) W1P(4 + (T), mb >> 1);                                            \
      if (!NOWARM && (T) >= 4 && mb == 3) WARM((T) - 4)                                                          \
      SB0();                                                                                          \
    }                                                                                                 \
  }
      HK(0) HK(1) HK(2) HK(3) HK(4) HK(5) HK(6) HK(7) HK(8) HK(9) HK(10) HK(11) HK(12) HK(13) HK(14) HK(15)
#undef HK
#undef WARM
      STAMP(1);
      cnt_arrive(lds, C_B2);
      cnt_wait(lds, C_B2, 4);
      cnt_wait(lds, C_SH, 3);
      asm volatile("s_waitcnt vmcnt(0)" : "+v"(wdum) :: "memory");
    }
    const unsigned ring = ldsb + L_RING + w * RING;
    unsigned fv;
    {
      const int row = l >> 3, c16p = l & 7;
      fv = (unsigned)(row * 1024 + ct * 128 + ((((c16p >> 1) ^ ((row >> 1) & 3)) << 1) | (c16p & 1)) * 16);
    }
    const char* w2b = p.w2 + (size_t)(448 * w) * 1024;
#define WP(N, n) dma1(ring + ((N) & 1) * 14336 + (n) * 1024, w2b + (size_t)(((N) + rot2) & 3) * (112 * 1024) + (n) * 8192, fv)
#pragma unroll
    for (int n = 0; n < 14; n++) WP(0, n);
    {
      f32x2 stc[2];
      stc[0] = *(LAS f32x2*)(lds + L_STAT + (w * 32 + i16) * 8);
      stc[1] = *(LAS f32x2*)(lds + L_STAT + (w * 32 + 16 + i16) * 8);
      const unsigned zf = zb + (unsigned)s * 14336u;
      unsigned pkd[7][2][2];
#pragma unroll
      for (int mb = 0; mb < 7; mb++) {
        const f32x4 sh = *(LAS f32x4*)(lds + L_SH + (mb * 16 + 4 * q) * 4);
#pragma unroll
        for (int b = 0; b < 2; b++) {
          const float nm = -stc[b][0], rr = stc[b][1], r2 = stc[b][1] * -1.44269504088896f;
#pragma unroll
          for (int e = 0; e < 2; e++) {
            const f32x2 d = f32x2{sh[2 * e], sh[2 * e + 1]} * nm + f32x2{ah[mb][b][2 * e], ah[mb][b][2 * e + 1]};
            const f32x2 v = d * rr;
            const f32x2 tt = d * r2;
            f32x2 den = f32x2{__builtin_amdgcn_exp2f(tt[0]), __builtin_amdgcn_exp2f(tt[1])} + 1.f;
            const f32x2 y = v * f32x2{__builtin_amdgcn_rcpf(den[0]), __builtin_amdgcn_rcpf(den[1])};
            pkd[mb][b][e] = pk_bf16v(y);
          }
        }
        if (mb & 1) {
#pragma unroll
          for (int b = 0; b < 2; b++) {
            v4u v = {pkd[mb - 1][b][0], pkd[mb - 1][b][1], pkd[mb][b][0], pkd[mb][b][1]};
            st16(rs, (unsigned)WS_Z + zf + (unsigned)(mb >> 1) * 4096u + (unsigned)(2 * (w & 1) + b) * 1024u, v);
          }
        }
      }
      v4u v = {pkd[6][0][0], pkd[6][0][1], pkd[6][1][0], pkd[6][1][1]};
      st16(rs, (unsigned)WS_Z + zf + 12288u + (unsigned)(w & 1) * 1024u, v);
    }
    STAMP(4);
    wait_vm<0>();
    if (cnt_arrive(lds, C_FS) == 3 && l == 0)
      __hip_atomic_store((GAS u64*)(p.ws + WS_FLAG) + (256 + j * 16 + s), epoch, RLX_AGENT);
    STAMP(5);
    poll_flags(p.ws + WS_FLAG + (size_t)(256 + j * 16 + 4 * w) * 8, l, epoch);
    STAMP(6);
    f32x4 acc[4][4];
#pragma unroll
    for (int a = 0; a < 4; a++)
#pragma unroll
      for (int b = 0; b < 4; b++) acc[a][b] = f32x4{0, 0, 0, 0};
    v4u af[4][4];
    const char* afb = ab + (size_t)(4 * w) * 14336;
    unsigned ftr[4];
#pragma unroll
    for (int nb = 0; nb < 4; nb++) {
      const int row = 4 * q + ta;
      ftr[nb] = opaque(ring + (unsigned)(row * 128 + ((nb ^ ((row >> 1) & 3)) * 32) + tp * 8));
    }
#define ALD(U, i) gld16_sc1<(i) * 1024>(af[(U) & 3][i], avoff, afb + (size_t)((((U) >> 2) + rot2) & 3) * 14336 + ((U) & 3) * 4096)
#define ALDS(U, V, t)                             \
  {                                                                                                 \
    if ((V) < 16) {                                                                                 \
      if (0 * usz(U) / usz(V) == (t)) ALD(V, 0);                                                    \
      if (1 * usz(U) / usz(V) == (t)) ALD(V, 1);                                                    \
      if (usz(V) == 4 && 2 * usz(U) / usz(V) == (t)) ALD(V, 2);                                     \
      if (usz(V) == 4 && 3 * usz(U) / usz(V) == (t)) ALD(V, 3);                                     \
    }                                                                                               \
  }
#define WRD(U)                                           \
  {                                                                                                 \
    _Pragma("unroll") for (int nb = 0; nb < 4; nb++) {                                              \
      LAS char* b_ = lp(ftr[nb]) + (((U) >> 2) & 1) * 14336 + ((U) & 3) * 4096;                     \
      if (((U) & 3) == 3) { v2u t_ = lds_tr1(b_); wf[(U) & 1][nb].x = t_.x; wf[(U) & 1][nb].y = t_.y; } \
      else wf[(U) & 1][nb] = lds_tr(b_, b_ + 2048);                                                 \
    }                                                                                               \
  }
#define FSTEP(U, t)                                                                                 \
  {                                                                                                 \
    if (((U) & 3) < 3) {                                                                            \
      _Pragma("unroll") for (int nb = 0; nb < 4; nb++)                                              \
          acc[nb][t] = mfma16(wf[(U) & 1][nb], af[(U) & 3][t], acc[nb][t]);                         \
    } else {                                                                                        \
      _Pragma("unroll") for (int nb = 0; nb < 4; nb++) {                                            \
        const v2u wh_ = {wf[(U) & 1][nb].x, wf[(U) & 1][nb].y};                                     \
        acc[nb][(2 * (t)) & 3] = mfma16h(wh_, v2u{af[3][t].x, af[3][t].y}, acc[nb][(2 * (t)) & 3]); \
        acc[nb][(2 * (t) + 1) & 3] = mfma16h(wh_, v2u{af[3][t].z, af[3][t].w}, acc[nb][(2 * (t) + 1) & 3]); \
      }                                                                                             \
    }                                                                                               \
    SB0();                                                                                          \
    ALDS(U, (U) + FD, t)                                                                            \
    if ((U) < 12) WP(((U) >> 2) + 1, 4 * ((U) & 3) + (t));                                          \
    if ((t) == usz(U) - 2 && (U) < 15) {                                                            \
      if ((U) + 1 >= 4) wait_vm<f_cnt(false, (U) + 1, U, (t) + 1)>();                               \
      WRD((U) + 1)                                                                                  \
    }                                                                                               \
    SB0();                                                                                          \
  }
#define FU(U)                                                                                       \
  {                                                                                                 \
    if (((U) & 3) < 3) {                                                                            \
      wait_vm4<f_cnt(true, U, U, 0)>(af[(U) & 3][0], af[(U) & 3][1], af[(U) & 3][2], af[(U) & 3][3]); \
      FSTEP(U, 0) FSTEP(U, 1) FSTEP(U, 2) FSTEP(U, 3)                                               \
    } else {                                                                                        \
      asm volatile("s_waitcnt vmcnt(%2)" : "+v"(af[3][0]), "+v"(af[3][1]) : "i"(f_cnt(true, U, U, 0)) : "memory"); \
      FSTEP(U, 0) FSTEP(U, 1)                                                                       \
    }                                                                                               \
  }
    v4u wf[2][4];
    ALD(0, 0); ALD(0, 1); ALD(0, 2); ALD(0, 3);
    ALD(1, 0); ALD(1, 1); ALD(1, 2); ALD(1, 3);
    ALD(2, 0); ALD(2, 1); ALD(2, 2); ALD(2, 3);
    static_assert(FD == 3, "window");
    WRD(0)
    FU(0) STAMP(7); FU(1) FU(2) FU(3) STAMP(8); FU(4) FU(5) FU(6) FU(7) STAMP(9); FU(8) FU(9) FU(10) FU(11) STAMP(14); FU(12) FU(13) FU(14) FU(15)
#undef FU
#undef FSTEP
#undef WRD
#undef ALD
#undef ALDS
#undef WP
    STAMP(10);
    {
      LAS char* dst = lp(ring);
#pragma unroll
      for (int tb = 0; tb < 4; tb++) {
        const int tk = 32 * (tb >> 1) + 8 * (i16 >> 2) + 4 * (tb & 1) + (i16 & 3);
#pragma unroll
        for (int nb = 0; nb < 4; nb++)
          *(LAS f32x4*)(dst + tk * 256 + (((4 * nb + q) ^ i16) & 15) * 16) = acc[nb][tb];
      }
    }
    STAMP(11);
  }
  {
    const int ec = tid & 15, et = tid >> 4;
    v2u xe[2];
    const v2u dk = *(const GAS v2u*)(p.dskip + (64 * ct + 4 * ec) * 2);
#pragma unroll
    for (int n = 0; n < 2; n++)
      xe[n] = *(const GAS v2u*)(p.x + (size_t)(64 * hp2 + et + 32 * n) * 1024 + (64 * ct + 4 * ec) * 2);
    bar();
    STAMP(12);
    if (j == 15 && s == 0 && tid == 0)
      __hip_atomic_store((GAS u64*)(p.ws + WS_EPOCH), epoch, RLX_AGENT);
    const float dkf[4] = {bf2f(dk.x & 0xffff), bf2f(dk.x >> 16), bf2f(dk.y & 0xffff), bf2f(dk.y >> 16)};
#pragma unroll
    for (int n = 0; n < 2; n++) {
      const int tk = et + 32 * n;
      const int key = (tk & 3) | (((tk >> 3) & 3) << 2);
      const int o = tk * 256 + ((ec ^ key) & 15) * 16;
      LAS char* fb = lds + L_RING + o;
      const f32x4 f = (*(LAS f32x4*)(fb) + *(LAS f32x4*)(fb + RING)) + (*(LAS f32x4*)(fb + 2 * RING) + *(LAS f32x4*)(fb + 3 * RING));
      const f32x4 y = *(LAS f32x4*)(lds + L_YB0 + o) + *(LAS f32x4*)(lds + L_YB1 + o);
      const int tkp = 64 * (hp2 & 1) + tk;
      const f32x2 se = *(LAS f32x2*)(lds + L_STAT + ((tkp >> 5) * 32 + ((tkp >> 2) & 1) * 16 + key) * 8);
      const float xv[4] = {bf2f(xe[n].x & 0xffff), bf2f(xe[n].x >> 16), bf2f(xe[n].y & 0xffff), bf2f(xe[n].y >> 16)};
      f32x4 o4;
#pragma unroll
      for (int e = 0; e < 4; e++) {
        const float xn = (xv[e] - se[0]) * se[1];
        const float sv = fmaf(xn, dkf[e], y[e]);
        const float g = f[e] * __builtin_amdgcn_rcpf(1.f + __builtin_amdgcn_exp2f(f[e] * -1.44269504088896f));
        o4[e] = fmaf(sv, g, xv[e]);
      }
      *(GAS f32x4*)(p.out + (size_t)(64 * hp2 + tk) * 512 + 64 * ct + 4 * ec) = o4;
    }
  }
  STAMP(13);
  STAMP_OUT();
}

extern "C" void kernel_launch(void* const* d_in, const int* in_sizes, int n_in, void* d_out, int out_size, void* d_ws,
                              size_t ws_size, hipStream_t stream) {
  Params p;
  p.x = (const char*)d_in[0]; p.lre = (const char*)d_in[1]; p.lim = (const char*)d_in[2];
  p.bre = (const char*)d_in[3]; p.bim = (const char*)d_in[4]; p.cre = (const char*)d_in[5]; p.cim = (const char*)d_in[6];
  p.logdt = (const char*)d_in[7]; p.dskip = (const char*)d_in[8]; p.w1 = (const char*)d_in[10]; p.w2 = (const char*)d_in[11];
  p.out = (float*)d_out; p.ws = (char*)d_ws;
  (void)hipFuncSetAttribute((const void*)k_fused, hipFuncAttributeMaxDynamicSharedMemorySize, LDS_BYTES);
  k_fused<<<256, 512, LDS_BYTES, stream>>>(p);
}
```

```cpp
#include <hip/hip_runtime.h>
#include <hip/hip_bf16.h>
#include <type_traits>

typedef unsigned v4u __attribute__((ext_vector_type(4)));
typedef unsigned v2u __attribute__((ext_vector_type(2)));
typedef float f32x4 __attribute__((ext_vector_type(4)));
typedef float f32x2 __attribute__((ext_vector_type(2)));
typedef short bf16x8 __attribute__((ext_vector_type(8)));
typedef short s16x4 __attribute__((ext_vector_type(4)));
typedef __bf16 bf16x2_t __attribute__((ext_vector_type(2)));
typedef unsigned long long u64;
#define LAS __attribute__((address_space(3)))
#define GAS __attribute__((address_space(1)))
#if defined(__HIP_DEVICE_COMPILE__)
#define NOPK __attribute__((target("no-packed-fp32-ops")))
#else
#define NOPK
#endif
#define DI __device__ __forceinline__ NOPK
#define RLX_AGENT __ATOMIC_RELAXED, __HIP_MEMORY_SCOPE_AGENT
#define RLX_WG __ATOMIC_RELAXED, __HIP_MEMORY_SCOPE_WORKGROUP

static constexpr unsigned HP_BYTES = 16 * 14336 + 32 * 4096;
static constexpr unsigned Y_OFF = 16 * 14336;
static constexpr size_t WS_Z = 0;
static constexpr size_t WS_Z_BYTES = (size_t)32 * HP_BYTES;
static constexpr size_t WS_GRAN = WS_Z + WS_Z_BYTES;
static constexpr size_t WS_SG = WS_GRAN + 16 * 16 * 512;
static constexpr size_t WS_FLAG = WS_SG + 2048 * 16;
static constexpr size_t WS_EPOCH = WS_FLAG + 2 * 16 * 16 * 8;
static constexpr size_t WS_END = WS_EPOCH + 128;

static constexpr int LDS_BYTES = 160 * 1024;
static constexpr int STG = 24576;
static constexpr int L_W1A = 96 * 1024;
static constexpr int L_W1B = 32 * 1024;
static constexpr int L_CST = 0;
static constexpr int L_YB1 = L_CST;
static constexpr int L_HT = 16 * 1024;
static constexpr int L_E = 24 * 1024;
static constexpr int L_KEEP = 28 * 1024;
static constexpr int L_YB0 = 32 * 1024;
static constexpr int L_RING = 48 * 1024;
static constexpr int L_STAT = L_KEEP;
static constexpr int L_SU = L_KEEP + 1024;
static constexpr int L_SH = L_KEEP + 1280;
static constexpr int L_CNT = L_KEEP + 1792;
static constexpr int RING = 28672;
static constexpr int L_HIN = L_KEEP + 2048;
enum { C_W = 9, C_SH = 10, C_B2 = 11, C_E = 0, C_CX = 1, C_HIN = 2, C_HS = 4, C_FS = 5, C_Y1 = 7 };
#define ROT 1
#define CIT 3
#define NOWARM 0
#define ALGKM 0
#define LATEC 1
#define VM0 0
#define CPACE 2
#define CODEPF 40
#define ROT2 1

DI bool wall(bool x) { return __builtin_amdgcn_ballot_w64(!x) == 0ull; }
DI f32x4 mfma16(v4u a, v4u b, f32x4 c) {
  return __builtin_amdgcn_mfma_f32_16x16x32_bf16(__builtin_bit_cast(bf16x8, a), __builtin_bit_cast(bf16x8, b), c, 0, 0, 0);
}
DI unsigned pk_bf16(float lo, float hi) {
  f32x2 v = {lo, hi};
  return __builtin_bit_cast(unsigned, __builtin_convertvector(v, bf16x2_t));
}
DI unsigned pk_bf16v(f32x2 v) { return __builtin_bit_cast(unsigned, __builtin_convertvector(v, bf16x2_t)); }
DI float bf2f(unsigned u) { return __builtin_bit_cast(float, u << 16); }
DI unsigned opaque(unsigned v) { asm volatile("" : "+v"(v)); return v; }
DI LAS char* lp(unsigned a) { return (LAS char*)(size_t)a; }

template <int IMM> DI void gld16(v4u& d, unsigned voff, const void* sb) {
  asm volatile("global_load_dwordx4 %0, %1, %2 offset:%3" : "=v"(d) : "v"(voff), "s"(sb), "i"(IMM) : "memory");
}
template <int IMM> DI void gld16_sc1(v4u& d, unsigned voff, const void* sb) {
  asm volatile("global_load_dwordx4 %0, %1, %2 offset:%3 sc1" : "=v"(d) : "v"(voff), "s"(sb), "i"(IMM) : "memory");
}
template <int IMM> DI void gld_u16(unsigned& d, unsigned voff, const void* sb) {
  asm volatile("global_load_ushort %0, %1, %2 offset:%3" : "=v"(d) : "v"(voff), "s"(sb), "i"(IMM) : "memory");
}
DI void gld8_sc1(v2u& d, unsigned voff, const void* sb) {
  asm volatile("global_load_dwordx2 %0, %1, %2 sc1" : "=v"(d) : "v"(voff), "s"(sb) : "memory");
}
DI void dma4(unsigned lds, const void* sb, unsigned v0, unsigned v1, unsigned v2, unsigned v3) {
  asm volatile(
      "s_mov_b32 m0, %0\n\ts_nop 0\n\t"
      "global_load_lds_dwordx4 %2, %1\n\t"
      "global_load_lds_dwordx4 %3, %1 offset:1024\n\t"
      "global_load_lds_dwordx4 %4, %1 offset:2048\n\t"
      "global_load_lds_dwordx4 %5, %1 offset:3072"
      :: "s"(lds), "s"(sb), "v"(v0), "v"(v1), "v"(v2), "v"(v3) : "memory");
}
template <int N> DI void wait_vm() { asm volatile("s_waitcnt vmcnt(%0)" :: "i"(N) : "memory"); }
template <int N> DI void wait_vm4(v4u& a, v4u& b, v4u& c, v4u& d) {
  asm volatile("s_waitcnt vmcnt(%4)" : "+v"(a), "+v"(b), "+v"(c), "+v"(d) : "i"(N) : "memory");
}
DI void wait_lgkm() { asm volatile("s_waitcnt lgkmcnt(0)" ::: "memory"); }
DI void bar() { wait_lgkm(); __builtin_amdgcn_s_barrier(); asm volatile("" ::: "memory"); }

DI v4u lds_rd16(LAS char* p) { return *(LAS v4u*)p; }
DI v4u lds_tr(LAS char* p0, LAS char* p1) {
  s16x4 a = __builtin_amdgcn_ds_read_tr16_b64_v4i16((LAS s16x4*)p0);
  s16x4 b = __builtin_amdgcn_ds_read_tr16_b64_v4i16((LAS s16x4*)p1);
  bf16x8 f = __builtin_shufflevector(a, b, 0, 1, 2, 3, 4, 5, 6, 7);
  return __builtin_bit_cast(v4u, f);
}
DI unsigned cnt_arrive(LAS char* lds, int c) {
  wait_lgkm();
  unsigned old = 0;
  if ((threadIdx.x & 63) == 0) old = __hip_atomic_fetch_add((LAS unsigned*)(lds + L_CNT) + c, 1u, RLX_WG);
  return __builtin_amdgcn_readfirstlane(old);
}
DI void cnt_wait(LAS char* lds, int c, unsigned target) {
  unsigned spins = 0;
  while ((unsigned)__builtin_amdgcn_readfirstlane(__hip_atomic_load((LAS unsigned*)(lds + L_CNT) + c, RLX_WG)) < target) {
    if (++spins > (1u << 24)) break;
    __builtin_amdgcn_s_sleep(1);
  }
  asm volatile("" ::: "memory");
}
struct cpx { float r, i; };
DI cpx cmul(cpx a, cpx b) { return {a.r * b.r - a.i * b.i, a.r * b.i + a.i * b.r}; }
DI cpx cfma(cpx a, cpx h, cpx u) {
  return {fmaf(a.r, h.r, fmaf(-a.i, h.i, u.r)), fmaf(a.r, h.i, fmaf(a.i, h.r, u.i))};
}
DI float fma1(float a, float b, float c) { float d; asm("v_fma_f32 %0, %1, %2, %3" : "=v"(d) : "v"(a), "v"(b), "v"(c)); return d; }
DI float fms1(float a, float b, float c) { float d; asm("v_fma_f32 %0, -%1, %2, %3" : "=v"(d) : "v"(a), "v"(b), "v"(c)); return d; }
DI cpx cfma_s(cpx a, cpx h, cpx u) {
  return {fma1(a.r, h.r, fms1(a.i, h.i, u.r)), fma1(a.r, h.i, fma1(a.i, h.r, u.i))};
}
DI cpx csel(bool c, cpx a, cpx b) { return {c ? a.r : b.r, c ? a.i : b.i}; }

#define STAMP(i)
#define STAMP_DECL
#define STAMP_OUT()

struct Params {
  const char *x, *lre, *lim, *bre, *bim, *cre, *cim, *logdt, *dskip, *w1, *w2;
  float* out;
  char* ws;
};

DI void st16(__amdgpu_buffer_rsrc_t rs, unsigned off, v4u v) { __builtin_amdgcn_raw_buffer_store_b128(v, rs, (int)off, 0, 16); }
DI void st8(__amdgpu_buffer_rsrc_t rs, unsigned off, v2u v) { __builtin_amdgcn_raw_buffer_store_b64(v, rs, (int)off, 0, 16); }

DI void poll_flags(const char* fl, int l, u64 epoch) {
  GAS u64* fp = (GAS u64*)fl + (l & 3);
  unsigned spins = 0;
  while (!wall(__hip_atomic_load(fp, RLX_AGENT) == epoch)) {
    if (++spins > (1u << 20)) break;
    __builtin_amdgcn_s_sleep(4);
  }
  asm volatile("" ::: "memory");
}

DI f32x4 mfma16h(v2u a, v2u b, f32x4 c) {
  return __builtin_amdgcn_mfma_f32_16x16x16bf16_1k(__builtin_bit_cast(s16x4, a), __builtin_bit_cast(s16x4, b), c, 0, 0, 0);
}
DI v2u lds_tr1(LAS char* p0) { return __builtin_bit_cast(v2u, __builtin_amdgcn_ds_read_tr16_b64_v4i16((LAS s16x4*)p0)); }
DI void dma2(unsigned lds, const void* sb, unsigned v0, unsigned v1) {
  asm volatile(
      "s_mov_b32 m0, %0\n\ts_nop 0\n\t"
      "global_load_lds_dwordx4 %2, %1\n\t"
      "global_load_lds_dwordx4 %3, %1 offset:1024"
      :: "s"(lds), "s"(sb), "v"(v0), "v"(v1) : "memory");
}
DI void dma_stage(unsigned lds, const void* xb, const void* ub, const void* wb, unsigned vx0, unsigned vx1, unsigned vu, unsigned vw0, unsigned vw1) {
  asm volatile(
      "s_mov_b32 m0, %0\n\ts_nop 0\n\t"
      "global_load_lds_dwordx4 %5, %2\n\t"
      "global_load_lds_dwordx4 %6, %2 offset:1024\n\t"
      "global_load_lds_dwordx4 %7, %3 offset:2048\n\t"
      "global_load_lds_dwordx4 %8, %4 offset:3072\n\t"
      "s_mov_b32 m0, %1\n\ts_nop 0\n\t"
      "global_load_lds_dwordx4 %9, %4"
      :: "s"(lds), "s"(lds + 4096u), "s"(xb), "s"(ub), "s"(wb), "v"(vx0), "v"(vx1), "v"(vu), "v"(vw0), "v"(vw1) : "memory");
}
template <int CTRL> DI float dpp_add(float v) {
  return v + __builtin_bit_cast(float, __builtin_amdgcn_update_dpp(0, __builtin_bit_cast(int, v), CTRL, 0xf, 0xf, true));
}
DI float wave_sum(float v) {
  v = dpp_add<0x111>(v); v = dpp_add<0x112>(v); v = dpp_add<0x114>(v); v = dpp_add<0x118>(v);
  v = v + __builtin_bit_cast(float, __builtin_amdgcn_update_dpp(0, __builtin_bit_cast(int, v), 0x142, 0xa, 0xf, false));
  v = v + __builtin_bit_cast(float, __builtin_amdgcn_update_dpp(0, __builtin_bit_cast(int, v), 0x143, 0xc, 0xf, false));
  return __builtin_bit_cast(float, __builtin_amdgcn_readlane(__builtin_bit_cast(int, v), 63));
}
DI u64 mk_gran(u64 epoch, float v) {
  unsigned vb = __builtin_bit_cast(unsigned, v);
  vb = (vb + 0x80u) >> 8;
  return (epoch << 24) | (u64)(vb & 0xffffffu);
}
DI void dma1(unsigned lds, const void* sb, unsigned v) {
  asm volatile("s_mov_b32 m0, %0\n\ts_nop 0\n\tglobal_load_lds_dwordx4 %2, %1" :: "s"(lds), "s"(sb), "v"(v) : "memory");
}
#define FD 3
constexpr int usz(int u) { return (u & 3) == 3 ? 2 : 4; }
constexpr int f_cnt(bool kindA, int u, int at, int st) {
  int n = 0; bool seen = false;
  for (int a = 0; a < FD; a++) { if (seen) n += usz(a); if (kindA && a == u) seen = true; }
  for (int v = 0; v <= at; v++) {
    const int steps = v < at ? usz(v) : st;
    for (int t = 0; t < steps; t++) {
      if (v + FD < 16) {
        const int np = usz(v + FD), ns = usz(v);
        for (int i = 0; i < np; i++)
          if (i * ns / np == t) { if (seen) n++; if (kindA && v + FD == u && i == np - 1) seen = true; }
      }
      if (v + 4 < 16) { if (seen) n++; if (!kindA && v + 4 == u && t == usz(v) - 1) seen = true; }
    }
  }
  return n;
}
#define SB0() __builtin_amdgcn_sched_barrier(0)

__global__ __launch_bounds__(512, 2) NOPK void k_fused(Params p) {
  extern __shared__ __attribute__((aligned(16))) char smem[];
  LAS char* lds = (LAS char*)smem;
  STAMP_DECL
  STAMP(0);
  const int tid = threadIdx.x, l = tid & 63;
  const int w8 = __builtin_amdgcn_readfirstlane(tid >> 6);
  const int role = w8 >> 2, w = w8 & 3;
  const int bid = blockIdx.x, xcd = bid & 7, r = bid >> 3;
  const int j = 4 * (xcd >> 1) + (r >> 3);
  const int s = 8 * (xcd & 1) + (r & 7);
  const int hp2 = 8 * (xcd >> 1) + (r >> 2);
  const int ct = 4 * (xcd & 1) + (r & 3);
  const int odd = s & 1;
  const int i16 = l & 15, q = l >> 4;
  const int ta = i16 >> 2, tp = i16 & 3;
  const unsigned ldsb = (unsigned)(size_t)lds;
  const __amdgpu_buffer_rsrc_t rs = __builtin_amdgcn_make_buffer_rsrc((void*)p.ws, 0, (int)WS_END, 0x00020000);
  const v4u ones = {0x3F803F80u, 0x3F803F80u, 0x3F803F80u, 0x3F803F80u};

  const int rot = ROT ? ((2 * (r >> 3) + (r & 7)) & 7) : 0;
  u64 ep0;
  asm volatile("s_load_dwordx2 %0, %1, 0x0" : "=s"(ep0) : "s"(p.ws + WS_EPOCH) : "memory");
  unsigned plr[2], pli[2], pdt[2];
  v4u xr[2];
  unsigned cdum = 0;
  if (role == 0) {
    const char* rb = p.x + (size_t)(128 * j + 8 * s + 2 * w) * 1024;
    gld16<0>(xr[0], (unsigned)l * 16, rb); gld16<1024>(xr[1], (unsigned)l * 16, rb);
    const unsigned co = (unsigned)(32 * s + i16) * 2;
    gld_u16<0>(plr[0], co, p.lre); gld_u16<32>(plr[1], co, p.lre);
    gld_u16<0>(pli[0], co, p.lim); gld_u16<32>(pli[1], co, p.lim);
    gld_u16<0>(pdt[0], co, p.logdt); gld_u16<32>(pdt[1], co, p.logdt);
    {
      u64 pc;
      asm volatile("s_getpc_b64 %0" : "=s"(pc));
      const int ci = r * 4 + w;
      const char* cb = (const char*)(pc & ~255ull) + (ci < CODEPF ? ci : 0) * 1024;
      asm volatile("global_load_dword %0, %1, %2" : "=v"(cdum) : "v"((unsigned)l * 16), "s"(cb) : "memory");
    }
  }
  unsigned vx0, vx1, vu, vwA, vwB;
  {
    const int r8 = l >> 3, c8 = l & 7;
    const int tok0 = 8 * (r8 >> 2) + (r8 & 3);
    vx0 = (unsigned)(tok0 * 1024 + ((c8 ^ (r8 >> 1)) & 7) * 16);
    vx1 = (unsigned)((tok0 + 16) * 1024 + ((c8 ^ (4 + (r8 >> 1))) & 7) * 16);
    const int ch = 8 * w + r8, iu = 8 * (w & 1) + r8;
    vu = (unsigned)(ch * 1024 + ((c8 ^ (iu >> 1)) & 7) * 16);
    const int winoff = 448 * (s >> 1) + (odd ? 192 : 0);
    vwA = (unsigned)((16 * w + q) * 3584 + winoff + ((((i16 >> 1) ^ q) << 1) | (i16 & 1)) * 16);
    vwB = (unsigned)((16 * w + 8 + q) * 3584 + winoff + ((((i16 >> 1) ^ (q | 4)) << 1) | (i16 & 1)) * 16);
  }
  const char* xb = p.x + (size_t)(128 * j + 32 * (w >> 1) + 4 * (w & 1)) * 1024;
  const char* ub0 = p.bre + (size_t)(32 * s) * 1024;
  const char* ub1 = p.bim + (size_t)(32 * s) * 1024;
#define PIECE(N, I)                                                                                  \
  {                                                                                                  \
    constexpr int h_ = (N) / 3, n_ = (N) % 3;                                                        \
    const int kq_ = ((I) + rot) & 7;                                                                 \
    const unsigned d_ = ldsb + w * 3072 + ((I) & 3) * STG + h_ * 12288 + n_ * 1024;                  \
    if (n_ == 0) dma1(d_, xb + h_ * 65536 + kq_ * 128, vx0);                                         \
    if (n_ == 1) dma1(d_, xb + h_ * 65536 + kq_ * 128, vx1);                                         \
    if (n_ == 2) dma1(d_, (h_ ? ub1 : ub0) + kq_ * 128, vu);                                         \
  }
#define PALL(I) PIECE(0, I) PIECE(1, I) PIECE(2, I) PIECE(3, I) PIECE(4, I) PIECE(5, I)
#define W1P(C, n) dma1(ldsb + ((C) < 4 ? L_W1A + (C) * 16384 : L_W1B + ((C) - 4) * 16384) + (16 * w + 4 * (n)) * 256,          \
                       p.w1 + (size_t)((((C) + rot) & 7) * 64 + ((n) & 1) * 4) * 3584, ((n) & 2) ? vwB : vwA)
  if (role == 1) {
    PALL(0)
    __builtin_amdgcn_s_barrier();
    PALL(1)
  } else {
    __builtin_amdgcn_s_barrier();
  }
  asm volatile("s_waitcnt lgkmcnt(0)" : "+s"(ep0) :: "memory");
  const u64 epoch = ep0 + 1ull;
  const unsigned tlo = (unsigned)(epoch & 0xffu), thi = (unsigned)(epoch >> 8);

  const unsigned sw0 = (unsigned)((i16 & 7) * 128 + (((0 + q) ^ (i16 >> 1)) & 7) * 16);
  const unsigned sw1 = (unsigned)((i16 & 7) * 128 + (((4 + q) ^ (i16 >> 1)) & 7) * 16);
  const unsigned xo = (unsigned)(2 * w * 3072 + (i16 >> 3) * 1024);
  const unsigned tro = (unsigned)((8 * q + ta) * 256 + 8 * tp);
  const int trkey = ta | ((q & 1) << 2);
  const unsigned zb = (unsigned)(2 * j + (w >> 1)) * HP_BYTES + (unsigned)l * 16u;
  const char* ab = p.ws + WS_Z + (size_t)hp2 * HP_BYTES;
  const unsigned avoff = (unsigned)l * 16;
  const int rot2 = ROT2 ? (hp2 & 3) : 0;

  if (role == 0) {
    asm volatile("s_waitcnt vmcnt(%8)" : "+v"(plr[0]), "+v"(plr[1]), "+v"(pli[0]), "+v"(pli[1]), "+v"(pdt[0]), "+v"(pdt[1]), "+v"(xr[0]), "+v"(xr[1]) : "i"(CODEPF ? 1 : 0) : "memory");
#pragma unroll
    for (int n = 0; n < 2; n++) {
      float v[8];
#pragma unroll
      for (int e = 0; e < 4; e++) { v[2 * e] = bf2f(xr[n][e] & 0xffffu); v[2 * e + 1] = __builtin_bit_cast(float, xr[n][e] & 0xffff0000u); }
      const float mean = wave_sum(((v[0] + v[1]) + (v[2] + v[3])) + ((v[4] + v[5]) + (v[6] + v[7]))) * (1.f / 512.f);
      float sq = 0.f;
#pragma unroll
      for (int e = 0; e < 8; e++) { const float d = v[e] - mean; sq = fmaf(d, d, sq); }
      const float rstd = rsqrtf(wave_sum(sq) * (1.f / 512.f) + 1e-5f);
      if (l < 2)
        __hip_atomic_store((GAS u64*)(p.ws + WS_SG) + (size_t)(128 * j + 8 * s + 2 * w + n) * 2 + l, mk_gran(epoch, l ? rstd : mean), RLX_AGENT);
    }
    cpx a1[2], cc[2], a8[2], pw[2], a128[2];
#pragma unroll
    for (int nb = 0; nb < 2; nb++) {
      const float lr = bf2f(plr[nb]), li = bf2f(pli[nb]);
      const float dtf = expf(bf2f(pdt[nb]));
      const float dt = bf2f(pk_bf16(dtf, 0.f) & 0xffffu);
      const float e = expf(dt * lr);
      float sn, cs;
      sincosf(dt * li, &sn, &cs);
      cpx a = {e * cs, e * sn};
      a1[nb] = a;
      const float nr = a.r - 1.f, ni = a.i, den = lr * lr + li * li;
      cc[nb] = {2.f * (nr * lr + ni * li) / den, 2.f * (ni * lr - nr * li) / den};
      cpx t = cmul(a, a); t = cmul(t, t); t = cmul(t, t);
      a8[nb] = t;
      cpx a16 = cmul(t, t), a32 = cmul(a16, a16), a64 = cmul(a32, a32);
      a128[nb] = cmul(a64, a64);
      cpx one = {1.f, 0.f};
      cpx pq = cmul(csel(q & 1, t, one), csel(q & 2, a16, one));
      cpx pwv = cmul(csel(w & 1, a32, one), csel(w & 2, a64, one));
      pw[nb] = cmul(pq, pwv);
    }
#pragma unroll
    for (int nb = 0; nb < 2; nb++)
      asm volatile("" : "+v"(a1[nb].r), "+v"(a1[nb].i), "+v"(cc[nb].r), "+v"(cc[nb].i), "+v"(a8[nb].r), "+v"(a8[nb].i), "+v"(pw[nb].r),
                   "+v"(pw[nb].i), "+v"(a128[nb].r), "+v"(a128[nb].i));
    STAMP(1);
    v4u cf[8][4];
    const unsigned cvoff = (unsigned)((64 * ct + (l >> 2)) * 1024 + (l & 3) * 16);
#define CLOAD(K0)                                                                         \
  _Pragma("unroll") for (int k = K0; k < K0 + 4; k++) {                                   \
    const int kr = (k + 2 * rot2) & 7;                                                    \
    const int sidx = 4 * w + 2 * (kr >> 2) + (kr & 1);                                    \
    const char* cb = (((kr >> 1) & 1) ? p.cim : p.cre) + sidx * 64;                       \
    _Pragma("unroll") for (int n = 0; n < 4; n++) gld16<0>(cf[k][n], cvoff, cb + n * 16384); \
    if (CPACE) __builtin_amdgcn_s_sleep(CPACE);                                           \
  }
#define CWAIT(K0, N)                                                                      \
  asm volatile("s_waitcnt vmcnt(%16)" : "+v"(cf[K0][0]), "+v"(cf[K0][1]), "+v"(cf[K0][2]), "+v"(cf[K0][3]), "+v"(cf[K0 + 1][0]),   \
               "+v"(cf[K0 + 1][1]), "+v"(cf[K0 + 1][2]), "+v"(cf[K0 + 1][3]), "+v"(cf[K0 + 2][0]), "+v"(cf[K0 + 2][1]),          \
               "+v"(cf[K0 + 2][2]), "+v"(cf[K0 + 2][3]), "+v"(cf[K0 + 3][0]), "+v"(cf[K0 + 3][1]), "+v"(cf[K0 + 3][2]),          \
               "+v"(cf[K0 + 3][3]) : "i"(N) : "memory");
#define CXPOSE(K0)                                                                        \
  _Pragma("unroll") for (int k = K0; k < K0 + 4; k++) {                                   \
    _Pragma("unroll") for (int n = 0; n < 4; n++) *(LAS v4u*)(lp(cwa) + n * 1024) = cf[k][n]; \
    _Pragma("unroll") for (int n = 0; n < 4; n++) cf[k][n] = lds_rd16(lp(cra) + n * 1024);  \
  }
    v2u sg;
    const unsigned sgoff = (unsigned)l * 8;
    const char* sgb = p.ws + WS_SG + (size_t)(128 * j + 32 * w) * 16;
    f32x4 au[2][2][2], asu;
#pragma unroll
    for (int b = 0; b < 2; b++)
#pragma unroll
      for (int nb = 0; nb < 2; nb++) { au[b][nb][0] = f32x4{0, 0, 0, 0}; au[b][nb][1] = f32x4{0, 0, 0, 0}; }
    asu = f32x4{0, 0, 0, 0};
    {
      const unsigned uo = (unsigned)((i16 >> 3) * 3072 + 2048);
      const int gs = (w & 1) * 2 + (w >> 1);
      unsigned cur = 0;
      v4u xd[2][2], wd[2][4], sd[2];
#pragma unroll
      for (int t = 0; t < 2; t++) xd[t][0] = xd[t][1] = wd[t][0] = wd[t][1] = wd[t][2] = wd[t][3] = sd[t] = v4u{0, 0, 0, 0};
#define A_STEP(RD, T, PX, PU, PS)                                                                     \
  {                                                                                                   \
    _Pragma("unroll") for (int g = 0; g < 4; g++) {                                                   \
      au[0][g & 1][g >> 1] = mfma16(xd[T][0], wd[T][g], au[0][g & 1][g >> 1]);                        \
      au[1][g & 1][g >> 1] = mfma16(xd[T][1], wd[T][g], au[1][g & 1][g >> 1]);                        \
      if (RD) wd[T][g] = lds_rd16(PU + g * 6144);                                                     \
      SB0();                                                                                          \
    }                                                                                                 \
    asu = mfma16(ones, sd[T], asu);                                                                   \
    if (RD) { xd[T][0] = lds_rd16(PX); xd[T][1] = lds_rd16(PX + 3072); sd[T] = lds_rd16(PS); }        \
    SB0();                                                                                            \
  }
#pragma unroll 1
      for (int it = 0; it < 8; it++) {
        const unsigned b0 = ldsb + cur;
        LAS char* px0 = lp(opaque(b0 + xo + sw0)); LAS char* px1 = lp(opaque(b0 + xo + sw1));
        LAS char* pu0 = lp(opaque(b0 + uo + sw0)); LAS char* pu1 = lp(opaque(b0 + uo + sw1));
        LAS char* ps0 = lp(opaque(b0 + uo + sw0 + gs * 6144)); LAS char* ps1 = lp(opaque(b0 + uo + sw1 + gs * 6144));
        cur = cur >= 3 * STG ? 0 : cur + STG;
        if (ALGKM) wait_lgkm();
        asm volatile("" ::: "memory");
        __builtin_amdgcn_s_barrier();
        asm volatile("" ::: "memory");
        SB0();
        A_STEP(true, 0, px0, pu0, ps0)
        A_STEP(true, 1, px1, pu1, ps1)
      }
      gld8_sc1(sg, sgoff, sgb);
      A_STEP(false, 0, lds, lds, lds)
      A_STEP(false, 1, lds, lds, lds)
#undef A_STEP
    }
    STAMP(2);
    {
      asm volatile("s_waitcnt vmcnt(0)" : "+v"(sg), "+v"(cdum) :: "memory");
      if (tid < 16) ((LAS unsigned*)(lds + L_CNT))[tid] = 0u;
      unsigned spins = 0;
      while (!wall(sg.y == thi && (sg.x >> 24) == tlo)) {
        if (++spins > (1u << 22)) break;
        __builtin_amdgcn_s_sleep(1);
        gld8_sc1(sg, sgoff, sgb);
        asm volatile("s_waitcnt vmcnt(0)" : "+v"(sg), "+v"(cdum) :: "memory");
      }
      const int tk = l >> 1;
      ((LAS unsigned*)(lds + L_STAT))[(w * 32 + ((tk >> 2) & 1) * 16 + ((tk & 3) | ((tk >> 3) << 2))) * 2 + (l & 1)] = sg.x << 8;
      if (q == 0) ((LAS float*)(lds + L_SU))[w * 16 + i16] = asu[0];
    }
    bar();
    STAMP(3);
    if (!LATEC) { CLOAD(0) }
    cpx u[2][8];
    {
      f32x2 stt[2][4];
#pragma unroll
      for (int b = 0; b < 2; b++) {
        f32x4 s0 = *(LAS f32x4*)(lds + L_STAT + (w * 32 + b * 16 + 4 * q) * 8);
        f32x4 s1 = *(LAS f32x4*)(lds + L_STAT + (w * 32 + b * 16 + 4 * q) * 8 + 16);
        stt[b][0] = f32x2{s0[0], s0[1]}; stt[b][1] = f32x2{s0[2], s0[3]};
        stt[b][2] = f32x2{s1[0], s1[1]}; stt[b][3] = f32x2{s1[2], s1[3]};
      }
#pragma unroll
      for (int nb = 0; nb < 2; nb++) {
        const float sre = ((LAS float*)(lds + L_SU))[(nb * 2 + 0) * 16 + i16];
        const float sim = ((LAS float*)(lds + L_SU))[(nb * 2 + 1) * 16 + i16];
        cpx h = {0.f, 0.f};
#pragma unroll
        for (int k = 0; k < 8; k++) {
          const int b = k >> 2, rg = k & 3;
          const float m = stt[b][rg][0], rr = stt[b][rg][1];
          u[nb][k].r = fmaf(-m, sre, au[b][nb][0][rg]) * rr;
          u[nb][k].i = fmaf(-m, sim, au[b][nb][1][rg]) * rr;
          h = cfma(a1[nb], h, u[nb][k]);
        }
        *(LAS f32x2*)(lds + L_E + ((nb * 16 + i16) * 16 + 4 * w + q) * 8) = f32x2{h.r, h.i};
      }
    }
    cnt_arrive(lds, C_E);
    cnt_wait(lds, C_E, 4);
    cpx cl[2];
    {
      cpx c[2] = {{0.f, 0.f}, {0.f, 0.f}};
      LAS f32x4* e4a = (LAS f32x4*)(lds + L_E + i16 * 128);
      LAS f32x4* e4b = (LAS f32x4*)(lds + L_E + (16 + i16) * 128);
#pragma unroll 1
      for (int k = 0; k < 2 * w; k++) {
        f32x4 ea = e4a[k], eb = e4b[k];
        c[0] = cfma(a8[0], c[0], cpx{ea[0], ea[1]}); c[1] = cfma(a8[1], c[1], cpx{eb[0], eb[1]});
        c[0] = cfma(a8[0], c[0], cpx{ea[2], ea[3]}); c[1] = cfma(a8[1], c[1], cpx{eb[2], eb[3]});
      }
      cpx tot[2];
#pragma unroll
      for (int nb = 0; nb < 2; nb++) {
        LAS f32x4* e4 = nb ? e4b : e4a;
        f32x4 e0 = e4[2 * w], e1 = e4[2 * w + 1];
        cpx c1 = cfma(a8[nb], c[nb], cpx{e0[0], e0[1]});
        cpx c2 = cfma(a8[nb], c1, cpx{e0[2], e0[3]});
        cpx c3 = cfma(a8[nb], c2, cpx{e1[0], e1[1]});
        tot[nb] = cfma(a8[nb], c3, cpx{e1[2], e1[3]});
        cl[nb] = csel(q & 2, csel(q & 1, c3, c2), csel(q & 1, c1, c[nb]));
      }
      if (w == 3 && j < 15) {
        const cpx t = (q & 2) ? tot[1] : tot[0];
        const float v = (q & 1) ? t.i : t.r;
        const u64 g = mk_gran(epoch, v);
        GAS u64* gp = (GAS u64*)(p.ws + WS_GRAN + (size_t)(j * 16 + s) * 512) + (((q >> 1) * 16 + i16) * 2 + (q & 1));
        __hip_atomic_store(gp, g, RLX_AGENT);
      }
    }
    STAMP(4);
    const unsigned cwa = opaque(ldsb + L_CST + w * 4096 + (l >> 2) * 64 + (((l & 3) ^ ((-(l >> 4)) & 3)) * 16));
    const unsigned cra = opaque(ldsb + L_CST + w * 4096 + i16 * 64 + ((q ^ ((-(i16 >> 2)) & 3)) * 16));
    if (w == 0) {
      const char* gb = p.ws + WS_GRAN + (size_t)s * 512;
      unsigned spins = 0;
      if (j > 0) {
        GAS u64* pp = (GAS u64*)(gb + (size_t)(l < j ? l : 0) * 8192 + 504);
        while (!wall((__hip_atomic_load(pp, RLX_AGENT) >> 24) == (epoch & 0xffffffffffull))) {
          if (++spins > (1u << 20)) break;
          __builtin_amdgcn_s_sleep(4);
        }
      }
      v4u gr[2][4];
      for (;;) {
#pragma unroll
        for (int m = 0; m < 4; m++) {
          const int pn = (4 * m + q) < j ? 4 * m + q : 0;
          const unsigned go = (unsigned)(pn * 8192 + i16 * 16);
          gld16_sc1<0>(gr[0][m], go, gb);
          gld16_sc1<256>(gr[1][m], go, gb);
        }
        asm volatile("s_waitcnt vmcnt(0)" : "+v"(gr[0][0]), "+v"(gr[0][1]), "+v"(gr[0][2]), "+v"(gr[0][3]),
                     "+v"(gr[1][0]), "+v"(gr[1][1]), "+v"(gr[1][2]), "+v"(gr[1][3]) :: "memory");
        bool ok = true;
#pragma unroll
        for (int nb = 0; nb < 2; nb++)
#pragma unroll
          for (int m = 0; m < 4; m++) {
            const bool need = (4 * m + q) < j;
            const bool good = (gr[nb][m].y == thi) && ((gr[nb][m].x >> 24) == tlo) && (gr[nb][m].w == thi) && ((gr[nb][m].z >> 24) == tlo);
            ok = ok && (good || !need);
          }
        if (wall(ok)) break;
        if (++spins > (1u << 20)) break;
        __builtin_amdgcn_s_sleep(4);
      }
#pragma unroll
      for (int nb = 0; nb < 2; nb++) {
        const cpx a256 = cmul(a128[nb], a128[nb]);
        const cpx a512 = cmul(a256, a256);
        cpx acc = {0.f, 0.f};
#pragma unroll
        for (int m = 0; m < 4; m++) {
          const bool need = (4 * m + q) < j;
          cpx e = {__builtin_bit_cast(float, gr[nb][m].x << 8), __builtin_bit_cast(float, gr[nb][m].z << 8)};
          acc = csel(need, cfma(a512, acc, e), acc);
        }
        const int dd = (j - 1 - q) & 3;
        cpx one = {1.f, 0.f};
        cpx fz = cmul(csel(dd & 1, a128[nb], one), csel(dd & 2, a256, one));
        acc = cmul(acc, fz);
        *(LAS f32x2*)(lds + L_HIN + ((nb * 16 + i16) * 4 + q) * 8) = f32x2{acc.r, acc.i};
      }
      cnt_arrive(lds, C_HIN);
    } else {
      const int m0 = w == 1 ? 0 : 2 * w - 1, m2 = w == 1 ? 2 : 2 * w;
      unsigned t0 = ldsb + L_W1A + tro + (unsigned)(((m0 + odd) ^ trkey) * 32);
      unsigned t1 = ldsb + L_W1A + tro + (unsigned)(((m0 + 1 + odd) ^ trkey) * 32);
      unsigned t2 = ldsb + L_W1A + tro + (unsigned)(((m2 + odd) ^ trkey) * 32);
      f32x4 s0 = {0, 0, 0, 0}, s1 = {0, 0, 0, 0}, s2 = {0, 0, 0, 0};
#pragma unroll 1
      for (int hf = 0; hf < 4; hf++) {
        if (hf == 2) { cnt_wait(lds, C_W, 4); t0 -= 131072u; t1 -= 131072u; t2 -= 131072u; }
        v4u f0[4], f1[4], f2[4];
#pragma unroll
        for (int t = 0; t < 4; t++) {
          f0[t] = lds_tr(lp(t0) + t * 8192, lp(t0) + t * 8192 + 1024);
          f1[t] = lds_tr(lp(t1) + t * 8192, lp(t1) + t * 8192 + 1024);
          f2[t] = lds_tr(lp(t2) + t * 8192, lp(t2) + t * 8192 + 1024);
        }
#pragma unroll
        for (int t = 0; t < 4; t++) { s0 = mfma16(f0[t], ones, s0); s1 = mfma16(f1[t], ones, s1); s2 = mfma16(f2[t], ones, s2); }
        t0 += 32768u; t1 += 32768u; t2 += 32768u;
      }
      if (i16 == 0) {
        *(LAS f32x4*)(lds + L_SH + (m0 * 16 + 4 * q) * 4) = s0;
        *(LAS f32x4*)(lds + L_SH + ((m0 + 1) * 16 + 4 * q) * 4) = s1;
        if (w == 1) *(LAS f32x4*)(lds + L_SH + (2 * 16 + 4 * q) * 4) = s2;
      }
      cnt_arrive(lds, C_SH);
    }
    if (LATEC) { CLOAD(0) }
    CWAIT(0, 0)
    CXPOSE(0)
    cnt_wait(lds, C_HIN, 1);
    STAMP(5);
    {
      LAS char* ht = lds + L_HT + w * 2048;
      unsigned hpk[2][2][2][2];
#pragma unroll
      for (int nb = 0; nb < 2; nb++) {
        const f32x4 h01 = *(LAS f32x4*)(lds + L_HIN + (nb * 16 + i16) * 32);
        const f32x4 h23 = *(LAS f32x4*)(lds + L_HIN + (nb * 16 + i16) * 32 + 16);
        const cpx acc = {(h01[0] + h01[2]) + (h23[0] + h23[2]), (h01[1] + h01[3]) + (h23[1] + h23[3])};
        cpx h = cfma(pw[nb], acc, cl[nb]);
        float orr[8], oi[8];
#pragma unroll
        for (int k = 0; k < 8; k++) {
          h = cfma(a1[nb], h, u[nb][k]);
          orr[k] = cc[nb].r * h.r - cc[nb].i * h.i;
          oi[k] = -(cc[nb].r * h.i + cc[nb].i * h.r);
        }
#pragma unroll
        for (int b = 0; b < 2; b++) {
          hpk[nb][0][b][0] = pk_bf16(orr[4 * b], orr[4 * b + 1]); hpk[nb][0][b][1] = pk_bf16(orr[4 * b + 2], orr[4 * b + 3]);
          hpk[nb][1][b][0] = pk_bf16(oi[4 * b], oi[4 * b + 1]);   hpk[nb][1][b][1] = pk_bf16(oi[4 * b + 2], oi[4 * b + 3]);
        }
      }
      const unsigned zy = zb + Y_OFF + (unsigned)(4 * (s >> 1) + (s & 1)) * 4096u + (unsigned)(2 * (w & 1)) * 1024u;
#pragma unroll
      for (int b = 0; b < 2; b++) {
#pragma unroll
        for (int nb = 0; nb < 2; nb++)
#pragma unroll
          for (int part = 0; part < 2; part++)
            *(LAS v2u*)(ht + (part * 32 + nb * 16 + i16) * 32 + q * 8) = v2u{hpk[nb][part][b][0], hpk[nb][part][b][1]};
#pragma unroll
        for (int part = 0; part < 2; part++) {
          LAS char* rp = ht + (part * 32 + 8 * q + ta) * 32 + tp * 8;
          v4u v = lds_tr(rp, rp + 4 * 32);
          st16(rs, (unsigned)WS_Z + zy + (unsigned)part * 8192u + b * 1024u, v);
        }
      }
    }
    STAMP(6);
    wait_vm<0>();
    if (cnt_arrive(lds, C_HS) == 3 && l == 0)
      __hip_atomic_store((GAS u64*)(p.ws + WS_FLAG) + (j * 16 + s), epoch, RLX_AGENT);
    STAMP(7);
    CLOAD(4)
    CWAIT(4, 0)
    CXPOSE(4)
    cnt_arrive(lds, C_CX);
    STAMP(8);
    poll_flags(p.ws + WS_FLAG + (size_t)(j * 16 + 4 * w) * 8, l, epoch);
    STAMP(9);
    f32x4 acc[4][4];
#pragma unroll
    for (int a = 0; a < 4; a++)
#pragma unroll
      for (int b = 0; b < 4; b++) acc[a][b] = f32x4{0, 0, 0, 0};
    v4u af[3][4];
    const char* ay = ab + Y_OFF + (size_t)(8 * w) * 4096;
#define YL(K, i) gld16_sc1<(i) * 1024>(af[(K) % 3][i], avoff, ay + ((((K) + 2 * rot2) & 7) * 4096))
#define YST(K, tb)                                                                                  \
  {                                                                                                 \
    _Pragma("unroll") for (int nb = 0; nb < 4; nb++) acc[nb][tb] = mfma16(cf[K][nb], af[(K) % 3][tb], acc[nb][tb]); \
    SB0();                                                                                          \
    if ((K) + 2 < 8) YL((K) + 2, tb);                                                               \
    SB0();                                                                                          \
  }
#define YS(K)                                                                                       \
  {                                                                                                 \
    wait_vm4<((K) < 7 ? 4 : 0)>(af[(K) % 3][0], af[(K) % 3][1], af[(K) % 3][2], af[(K) % 3][3]);    \
    YST(K, 0) YST(K, 1) YST(K, 2) YST(K, 3)                                                         \
  }
    YL(0, 0); YL(0, 1); YL(0, 2); YL(0, 3); YL(1, 0); YL(1, 1); YL(1, 2); YL(1, 3);
    YS(0) YS(1) YS(2) YS(3) YS(4) YS(5) YS(6) YS(7)
#undef YL
#undef YS
#undef YST
    STAMP(10);
    cnt_wait(lds, C_CX, 4);
    cnt_wait(lds, C_B2, 4);
    {
      LAS char* yb = lds + ((w & 1) ? L_YB1 : L_YB0);
      if (w < 2) cnt_wait(lds, C_Y1 + (w & 1), 1);
#pragma unroll
      for (int tb = 0; tb < 4; tb++) {
        const int tk = 32 * (tb >> 1) + 8 * (i16 >> 2) + 4 * (tb & 1) + (i16 & 3);
#pragma unroll
        for (int nb = 0; nb < 4; nb++) {
          LAS f32x4* d = (LAS f32x4*)(yb + tk * 256 + (((4 * nb + q) ^ i16) & 15) * 16);
          if (w < 2) *d = *d + acc[nb][tb]; else *d = acc[nb][tb];
        }
      }
      if (w >= 2) cnt_arrive(lds, C_Y1 + (w & 1));
    }
    STAMP(11);
  } else {
    v4u xf[2][16];
    {
      LAS char* px0 = lp(opaque(ldsb + xo + sw0)); LAS char* px1 = lp(opaque(ldsb + xo + sw1));
#define L1IT(I, NW)                                                                                   \
  {                                                                                                   \
    wait_vm<VM0 ? 0 : NW>();                                                                                    \
    __builtin_amdgcn_s_barrier();                                                                     \
    asm volatile("" ::: "memory");                                                                    \
    xf[0][2 * (I)] = lds_rd16(px0 + ((I) & 3) * STG); xf[1][2 * (I)] = lds_rd16(px0 + ((I) & 3) * STG + 3072); \
    xf[0][2 * (I) + 1] = lds_rd16(px1 + ((I) & 3) * STG); xf[1][2 * (I) + 1] = lds_rd16(px1 + ((I) & 3) * STG + 3072); \
    SB0();                                                                                            \
    if ((I) < 6) { PALL((I) + 2) }                                                                    \
    W1P((I) >> 1, 2 * ((I) & 1)); W1P((I) >> 1, 2 * ((I) & 1) + 1);                                   \
    wait_lgkm();                                                                                      \
  }
      L1IT(0, 6) L1IT(1, 8) L1IT(2, 10) STAMP(15); L1IT(3, 10) L1IT(4, 10) L1IT(5, 10) L1IT(6, 10) L1IT(7, 4)
#undef L1IT
    }
    STAMP(2);
    wait_vm<0>();
    bar();
    STAMP(3);
    f32x4 ah[7][2];
#pragma unroll
    for (int mb = 0; mb < 7; mb++) { ah[mb][0] = f32x4{0, 0, 0, 0}; ah[mb][1] = f32x4{0, 0, 0, 0}; }
    {
      unsigned pt[7];
#pragma unroll
      for (int mb = 0; mb < 7; mb++) pt[mb] = opaque(ldsb + L_W1A + tro + (unsigned)(((mb + odd) ^ trkey) * 32));
      v4u wd[7];
      unsigned wdum = 0;
      const unsigned wv2 = (unsigned)((l >> 5) * 1024 + (l & 31) * 16);
#pragma unroll
      for (int mb = 0; mb < 7; mb++) wd[mb] = lds_tr(lp(pt[mb]), lp(pt[mb]) + 1024);
#define WARM(K)                                                                                       \
  {                                                                                                   \
    const int pid = ((K) * 32 + r) * 4 + w, c = (pid - 896) & 511;                                    \
    const char* src = pid < 896 ? p.w2 + (size_t)pid * 2048 + (xcd & 1) * 512                         \
                                : ((c & 256) ? p.cim : p.cre) + (size_t)((xcd & 1) * 256 + (c & 255)) * 1024; \
    asm volatile("global_load_dword %0, %1, %2" : "+v"(wdum) : "v"(pid < 896 ? wv2 : avoff), "s"(src) : "memory"); \
  }
#define HK(T)                                                                                         \
  {                                                                                                   \
    if ((T) == 7) {                                                                                   \
      wait_vm<NOWARM ? 0 : 3>();                                                                                   \
      cnt_arrive(lds, C_W);                                                                           \
      cnt_wait(lds, C_W, 4);                                                                          \
      _Pragma("unroll") for (int mb = 0; mb < 7; mb++) pt[mb] = opaque(pt[mb] - 65536u);              \
    }                                                                                                 \
    _Pragma("unroll") for (int mb = 0; mb < 7; mb++) {                                                \
      ah[mb][0] = mfma16(wd[mb], xf[0][T], ah[mb][0]);                                                \
      ah[mb][1] = mfma16(wd[mb], xf[1][T], ah[mb][1]);                                                \
      if ((T) < 15) wd[mb] = lds_tr(lp(pt[mb]) + (((T) + 1) & 7) * 8192, lp(pt[mb]) + (((T) + 1) & 7) * 8192 + 1024); \
      if ((T) < 4 && (mb & 1) == 0) W1P(4 + (T), mb >> 1);                                            \
      if (!NOWARM && (T) >= 4 && mb == 3) WARM((T) - 4)                                                          \
      SB0();                                                                                          \
    }                                                                                                 \
  }
      HK(0) HK(1) HK(2) HK(3) HK(4) HK(5) HK(6) HK(7) HK(8) HK(9) HK(10) HK(11) HK(12) HK(13) HK(14) HK(15)
#undef HK
#undef WARM
      STAMP(1);
      cnt_arrive(lds, C_B2);
      cnt_wait(lds, C_B2, 4);
      cnt_wait(lds, C_SH, 3);
      asm volatile("s_waitcnt vmcnt(0)" : "+v"(wdum) :: "memory");
    }
    const unsigned ring = ldsb + L_RING + w * RING;
    unsigned fv;
    {
      const int row = l >> 3, c16p = l & 7;
      fv = (unsigned)(row * 1024 + ct * 128 + ((((c16p >> 1) ^ ((row >> 1) & 3)) << 1) | (c16p & 1)) * 16);
    }
    const char* w2b = p.w2 + (size_t)(448 * w) * 1024;
#define WP(N, n) dma1(ring + ((N) & 1) * 14336 + (n) * 1024, w2b + (size_t)(((N) + rot2) & 3) * (112 * 1024) + (n) * 8192, fv)
#pragma unroll
    for (int n = 0; n < 14; n++) WP(0, n);
    {
      f32x2 stc[2];
      stc[0] = *(LAS f32x2*)(lds + L_STAT + (w * 32 + i16) * 8);
      stc[1] = *(LAS f32x2*)(lds + L_STAT + (w * 32 + 16 + i16) * 8);
      const unsigned zf = zb + (unsigned)s * 14336u;
      unsigned pkd[7][2][2];
#pragma unroll
      for (int mb = 0; mb < 7; mb++) {
        const f32x4 sh = *(LAS f32x4*)(lds + L_SH + (mb * 16 + 4 * q) * 4);
#pragma unroll
        for (int b = 0; b < 2; b++) {
          const float nm = -stc[b][0], rr = stc[b][1], r2 = stc[b][1] * -1.44269504088896f;
#pragma unroll
          for (int e = 0; e < 2; e++) {
            const f32x2 d = f32x2{sh[2 * e], sh[2 * e + 1]} * nm + f32x2{ah[mb][b][2 * e], ah[mb][b][2 * e + 1]};
            const f32x2 v = d * rr;
            const f32x2 tt = d * r2;
            f32x2 den = f32x2{__builtin_amdgcn_exp2f(tt[0]), __builtin_amdgcn_exp2f(tt[1])} + 1.f;
            const f32x2 y = v * f32x2{__builtin_amdgcn_rcpf(den[0]), __builtin_amdgcn_rcpf(den[1])};
            pkd[mb][b][e] = pk_bf16v(y);
          }
        }
        if (mb & 1) {
#pragma unroll
          for (int b = 0; b < 2; b++) {
            v4u v = {pkd[mb - 1][b][0], pkd[mb - 1][b][1], pkd[mb][b][0], pkd[mb][b][1]};
            st16(rs, (unsigned)WS_Z + zf + (unsigned)(mb >> 1) * 4096u + (unsigned)(2 * (w & 1) + b) * 1024u, v);
          }
        }
      }
      v4u v = {pkd[6][0][0], pkd[6][0][1], pkd[6][1][0], pkd[6][1][1]};
      st16(rs, (unsigned)WS_Z + zf + 12288u + (unsigned)(w & 1) * 1024u, v);
    }
    STAMP(4);
    wait_vm<0>();
    if (cnt_arrive(lds, C_FS) == 3 && l == 0)
      __hip_atomic_store((GAS u64*)(p.ws + WS_FLAG) + (256 + j * 16 + s), epoch, RLX_AGENT);
    STAMP(5);
    poll_flags(p.ws + WS_FLAG + (size_t)(256 + j * 16 + 4 * w) * 8, l, epoch);
    STAMP(6);
    f32x4 acc[4][4];
#pragma unroll
    for (int a = 0; a < 4; a++)
#pragma unroll
      for (int b = 0; b < 4; b++) acc[a][b] = f32x4{0, 0, 0, 0};
    v4u af[4][4];
    const char* afb = ab + (size_t)(4 * w) * 14336;
    unsigned ftr[4];
#pragma unroll
    for (int nb = 0; nb < 4; nb++) {
      const int row = 4 * q + ta;
      ftr[nb] = opaque(ring + (unsigned)(row * 128 + ((nb ^ ((row >> 1) & 3)) * 32) + tp * 8));
    }
#define ALD(U, i) gld16_sc1<(i) * 1024>(af[(U) & 3][i], avoff, afb + (size_t)((((U) >> 2) + rot2) & 3) * 14336 + ((U) & 3) * 4096)
#define ALDS(U, V, t)                             \
  {                                                                                                 \
    if ((V) < 16) {                                                                                 \
      if (0 * usz(U) / usz(V) == (t)) ALD(V, 0);                                                    \
      if (1 * usz(U) / usz(V) == (t)) ALD(V, 1);                                                    \
      if (usz(V) == 4 && 2 * usz(U) / usz(V) == (t)) ALD(V, 2);                                     \
      if (usz(V) == 4 && 3 * usz(U) / usz(V) == (t)) ALD(V, 3);                                     \
    }                                                                                               \
  }
#define WRD(U)                                           \
  {                                                                                                 \
    _Pragma("unroll") for (int nb = 0; nb < 4; nb++) {                                              \
      LAS char* b_ = lp(ftr[nb]) + (((U) >> 2) & 1) * 14336 + ((U) & 3) * 4096;                     \
      if (((U) & 3) == 3) { v2u t_ = lds_tr1(b_); wf[(U) & 1][nb].x = t_.x; wf[(U) & 1][nb].y = t_.y; } \
      else wf[(U) & 1][nb] = lds_tr(b_, b_ + 2048);                                                 \
    }                                                                                               \
  }
#define FSTEP(U, t)                                                                                 \
  {                                                                                                 \
    if (((U) & 3) < 3) {                                                                            \
      _Pragma("unroll") for (int nb = 0; nb < 4; nb++)                                              \
          acc[nb][t] = mfma16(wf[(U) & 1][nb], af[(U) & 3][t], acc[nb][t]);                         \
    } else {                                                                                        \
      _Pragma("unroll") for (int nb = 0; nb < 4; nb++) {                                            \
        const v2u wh_ = {wf[(U) & 1][nb].x, wf[(U) & 1][nb].y};                                     \
        acc[nb][(2 * (t)) & 3] = mfma16h(wh_, v2u{af[3][t].x, af[3][t].y}, acc[nb][(2 * (t)) & 3]); \
        acc[nb][(2 * (t) + 1) & 3] = mfma16h(wh_, v2u{af[3][t].z, af[3][t].w}, acc[nb][(2 * (t) + 1) & 3]); \
      }                                                                                             \
    }                                                                                               \
    SB0();                                                                                          \
    ALDS(U, (U) + FD, t)                                                                            \
    if ((U) < 12) WP(((U) >> 2) + 1, 4 * ((U) & 3) + (t));                                          \
    if ((t) == usz(U) - 2 && (U) < 15) {                                                            \
      if ((U) + 1 >= 4) wait_vm<f_cnt(false, (U) + 1, U, (t) + 1)>();                               \
      WRD((U) + 1)                                                                                  \
    }                                                                                               \
    SB0();                                                                                          \
  }
#define FU(U)                                                                                       \
  {                                                                                                 \
    if (((U) & 3) < 3) {                                                                            \
      wait_vm4<f_cnt(true, U, U, 0)>(af[(U) & 3][0], af[(U) & 3][1], af[(U) & 3][2], af[(U) & 3][3]); \
      FSTEP(U, 0) FSTEP(U, 1) FSTEP(U, 2) FSTEP(U, 3)                                               \
    } else {                                                                                        \
      asm volatile("s_waitcnt vmcnt(%2)" : "+v"(af[3][0]), "+v"(af[3][1]) : "i"(f_cnt(true, U, U, 0)) : "memory"); \
      FSTEP(U, 0) FSTEP(U, 1)                                                                       \
    }                                                                                               \
  }
    v4u wf[2][4];
    ALD(0, 0); ALD(0, 1); ALD(0, 2); ALD(0, 3);
    ALD(1, 0); ALD(1, 1); ALD(1, 2); ALD(1, 3);
    ALD(2, 0); ALD(2, 1); ALD(2, 2); ALD(2, 3);
    static_assert(FD == 3, "window");
    WRD(0)
    FU(0) STAMP(7); FU(1) FU(2) FU(3) STAMP(8); FU(4) FU(5) FU(6) FU(7) STAMP(9); FU(8) FU(9) FU(10) FU(11) STAMP(14); FU(12) FU(13) FU(14) FU(15)
#undef FU
#undef FSTEP
#undef WRD
#undef ALD
#undef ALDS
#undef WP
    STAMP(10);
    {
      LAS char* dst = lp(ring);
#pragma unroll
      for (int tb = 0; tb < 4; tb++) {
        const int tk = 32 * (tb >> 1) + 8 * (i16 >> 2) + 4 * (tb & 1) + (i16 & 3);
#pragma unroll
        for (int nb = 0; nb < 4; nb++)
          *(LAS f32x4*)(dst + tk * 256 + (((4 * nb + q) ^ i16) & 15) * 16) = acc[nb][tb];
      }
    }
    STAMP(11);
  }
  {
    const int ec = tid & 15, et = tid >> 4;
    v2u xe[2];
    const v2u dk = *(const GAS v2u*)(p.dskip + (64 * ct + 4 * ec) * 2);
#pragma unroll
    for (int n = 0; n < 2; n++)
      xe[n] = *(const GAS v2u*)(p.x + (size_t)(64 * hp2 + et + 32 * n) * 1024 + (64 * ct + 4 * ec) * 2);
    bar();
    STAMP(12);
    if (j == 15 && s == 0 && tid == 0)
      __hip_atomic_store((GAS u64*)(p.ws + WS_EPOCH), epoch, RLX_AGENT);
    const float dkf[4] = {bf2f(dk.x & 0xffff), bf2f(dk.x >> 16), bf2f(dk.y & 0xffff), bf2f(dk.y >> 16)};
#pragma unroll
    for (int n = 0; n < 2; n++) {
      const int tk = et + 32 * n;
      const int key = (tk & 3) | (((tk >> 3) & 3) << 2);
      const int o = tk * 256 + ((ec ^ key) & 15) * 16;
      LAS char* fb = lds + L_RING + o;
      const f32x4 f = (*(LAS f32x4*)(fb) + *(LAS f32x4*)(fb + RING)) + (*(LAS f32x4*)(fb + 2 * RING) + *(LAS f32x4*)(fb + 3 * RING));
      const f32x4 y = *(LAS f32x4*)(lds + L_YB0 + o) + *(LAS f32x4*)(lds + L_YB1 + o);
      const int tkp = 64 * (hp2 & 1) + tk;
      const f32x2 se = *(LAS f32x2*)(lds + L_STAT + ((tkp >> 5) * 32 + ((tkp >> 2) & 1) * 16 + key) * 8);
      const float xv[4] = {bf2f(xe[n].x & 0xffff), bf2f(xe[n].x >> 16), bf2f(xe[n].y & 0xffff), bf2f(xe[n].y >> 16)};
      f32x4 o4;
#pragma unroll
      for (int e = 0; e < 4; e++) {
        const float xn = (xv[e] - se[0]) * se[1];
        const float sv = fmaf(xn, dkf[e], y[e]);
        const float g = f[e] * __builtin_amdgcn_rcpf(1.f + __builtin_amdgcn_exp2f(f[e] * -1.44269504088896f));
        o4[e] = fmaf(sv, g, xv[e]);
      }
      *(GAS f32x4*)(p.out + (size_t)(64 * hp2 + tk) * 512 + 64 * ct + 4 * ec) = o4;
    }
  }
  STAMP(13);
  STAMP_OUT();
}

extern "C" void kernel_launch(void* const* d_in, const int* in_sizes, int n_in, void* d_out, int out_size, void* d_ws,
                              size_t ws_size, hipStream_t stream) {
  Params p;
  p.x = (const char*)d_in[0]; p.lre = (const char*)d_in[1]; p.lim = (const char*)d_in[2];
  p.bre = (const char*)d_in[3]; p.bim = (const char*)d_in[4]; p.cre = (const char*)d_in[5]; p.cim = (const char*)d_in[6];
  p.logdt = (const char*)d_in[7]; p.dskip = (const char*)d_in[8]; p.w1 = (const char*)d_in[10]; p.w2 = (const char*)d_in[11];
  p.out = (float*)d_out; p.ws = (char*)d_ws;
  (void)hipFuncSetAttribute((const void*)k_fused, hipFuncAttributeMaxDynamicSharedMemorySize, LDS_BYTES);
  k_fused<<<256, 512, LDS_BYTES, stream>>>(p);
}
```

```cpp
#include <hip/hip_runtime.h>
#include <hip/hip_bf16.h>
#include <type_traits>
#include <utility>

typedef unsigned v4u __attribute__((ext_vector_type(4)));
typedef unsigned v2u __attribute__((ext_vector_type(2)));
typedef float f32x4 __attribute__((ext_vector_type(4)));
typedef float f32x2 __attribute__((ext_vector_type(2)));
typedef short bf16x8 __attribute__((ext_vector_type(8)));
typedef short s16x4 __attribute__((ext_vector_type(4)));
typedef __bf16 bf16x2_t __attribute__((ext_vector_type(2)));
typedef unsigned long long u64;
#define LAS __attribute__((address_space(3)))
#define GAS __attribute__((address_space(1)))
#if defined(__HIP_DEVICE_COMPILE__)
#define NOPK __attribute__((target("no-packed-fp32-ops")))
#else
#define NOPK
#endif
#define DI __device__ __forceinline__ NOPK
#define RLX_AGENT __ATOMIC_RELAXED, __HIP_MEMORY_SCOPE_AGENT
#define RLX_WG __ATOMIC_RELAXED, __HIP_MEMORY_SCOPE_WORKGROUP

static constexpr unsigned HP_BYTES = 16 * 14336 + 32 * 4096;
static constexpr unsigned Y_OFF = 16 * 14336;
static constexpr size_t WS_Z = 0;
static constexpr size_t WS_Z_BYTES = (size_t)32 * HP_BYTES;
static constexpr size_t WS_GRAN = WS_Z + WS_Z_BYTES;
static constexpr size_t WS_SG = WS_GRAN + 16 * 16 * 512;
static constexpr size_t WS_FLAG = WS_SG + 2048 * 16;
static constexpr size_t WS_EPOCH = WS_FLAG + 2 * 16 * 16 * 8;
static constexpr size_t WS_END = WS_EPOCH + 128;

static constexpr int LDS_BYTES = 160 * 1024;
static constexpr int STG = 24576;
static constexpr int L_W1A = 96 * 1024;
static constexpr int L_W1B = 32 * 1024;
static constexpr int L_CST = 0;
static constexpr int L_YB1 = L_CST;
static constexpr int L_HT = 16 * 1024;
static constexpr int L_E = 24 * 1024;
static constexpr int L_KEEP = 28 * 1024;
static constexpr int L_YB0 = 32 * 1024;
static constexpr int L_RING = 48 * 1024;
static constexpr int L_STAT = L_KEEP;
static constexpr int L_SU = L_KEEP + 1024;
static constexpr int L_SH = L_KEEP + 1280;
static constexpr int L_CNT = L_KEEP + 1792;
static constexpr int RING = 28672;
static constexpr int L_HIN = L_KEEP + 2048;
enum { C_W = 9, C_SH = 10, C_B2 = 11, C_E = 0, C_CX = 1, C_HIN = 2, C_HS = 4, C_FS = 5, C_Y1 = 7 };
#define ROT 1
#define CIT 3
#define NOWARM 0
#define ALGKM 0
#define LATEC 1
#define VM0 0
#define CPACE 2
#define CODEPF 38
#define APRIO 2
#define NWARM 7
#define ROT2 1

DI bool wall(bool x) { return __builtin_amdgcn_ballot_w64(!x) == 0ull; }
DI f32x4 mfma16(v4u a, v4u b, f32x4 c) {
  return __builtin_amdgcn_mfma_f32_16x16x32_bf16(__builtin_bit_cast(bf16x8, a), __builtin_bit_cast(bf16x8, b), c, 0, 0, 0);
}
DI unsigned pk_bf16(float lo, float hi) {
  f32x2 v = {lo, hi};
  return __builtin_bit_cast(unsigned, __builtin_convertvector(v, bf16x2_t));
}
DI unsigned pk_bf16v(f32x2 v) { return __builtin_bit_cast(unsigned, __builtin_convertvector(v, bf16x2_t)); }
DI float bf2f(unsigned u) { return __builtin_bit_cast(float, u << 16); }
DI unsigned opaque(unsigned v) { asm volatile("" : "+v"(v)); return v; }
DI LAS char* lp(unsigned a) { return (LAS char*)(size_t)a; }

template <int IMM> DI void gld16(v4u& d, unsigned voff, const void* sb) {
  asm volatile("global_load_dwordx4 %0, %1, %2 offset:%3" : "=v"(d) : "v"(voff), "s"(sb), "i"(IMM) : "memory");
}
template <int IMM> DI void gld16_sc1(v4u& d, unsigned voff, const void* sb) {
  asm volatile("global_load_dwordx4 %0, %1, %2 offset:%3 sc1" : "=v"(d) : "v"(voff), "s"(sb), "i"(IMM) : "memory");
}
template <int IMM> DI void gld_u16(unsigned& d, unsigned voff, const void* sb) {
  asm volatile("global_load_ushort %0, %1, %2 offset:%3" : "=v"(d) : "v"(voff), "s"(sb), "i"(IMM) : "memory");
}
DI void gld8_sc1(v2u& d, unsigned voff, const void* sb) {
  asm volatile("global_load_dwordx2 %0, %1, %2 sc1" : "=v"(d) : "v"(voff), "s"(sb) : "memory");
}
DI void dma4(unsigned lds, const void* sb, unsigned v0, unsigned v1, unsigned v2, unsigned v3) {
  asm volatile(
      "s_mov_b32 m0, %0\n\ts_nop 0\n\t"
      "global_load_lds_dwordx4 %2, %1\n\t"
      "global_load_lds_dwordx4 %3, %1 offset:1024\n\t"
      "global_load_lds_dwordx4 %4, %1 offset:2048\n\t"
      "global_load_lds_dwordx4 %5, %1 offset:3072"
      :: "s"(lds), "s"(sb), "v"(v0), "v"(v1), "v"(v2), "v"(v3) : "memory");
}
template <int N> DI void wait_vm() { asm volatile("s_waitcnt vmcnt(%0)" :: "i"(N) : "memory"); }
template <int N> DI void wait_vm4(v4u& a, v4u& b, v4u& c, v4u& d) {
  asm volatile("s_waitcnt vmcnt(%4)" : "+v"(a), "+v"(b), "+v"(c), "+v"(d) : "i"(N) : "memory");
}
DI void wait_lgkm() { asm volatile("s_waitcnt lgkmcnt(0)" ::: "memory"); }
DI void bar() { wait_lgkm(); __builtin_amdgcn_s_barrier(); asm volatile("" ::: "memory"); }

DI v4u lds_rd16(LAS char* p) { return *(LAS v4u*)p; }
DI v4u lds_tr(LAS char* p0, LAS char* p1) {
  s16x4 a = __builtin_amdgcn_ds_read_tr16_b64_v4i16((LAS s16x4*)p0);
  s16x4 b = __builtin_amdgcn_ds_read_tr16_b64_v4i16((LAS s16x4*)p1);
  bf16x8 f = __builtin_shufflevector(a, b, 0, 1, 2, 3, 4, 5, 6, 7);
  return __builtin_bit_cast(v4u, f);
}
DI unsigned cnt_arrive(LAS char* lds, int c) {
  wait_lgkm();
  unsigned old = 0;
  if ((threadIdx.x & 63) == 0) old = __hip_atomic_fetch_add((LAS unsigned*)(lds + L_CNT) + c, 1u, RLX_WG);
  return __builtin_amdgcn_readfirstlane(old);
}
DI void cnt_wait(LAS char* lds, int c, unsigned target) {
  unsigned spins = 0;
  while ((unsigned)__builtin_amdgcn_readfirstlane(__hip_atomic_load((LAS unsigned*)(lds + L_CNT) + c, RLX_WG)) < target) {
    if (++spins > (1u << 24)) break;
    __builtin_amdgcn_s_sleep(1);
  }
  asm volatile("" ::: "memory");
}
struct cpx { float r, i; };
DI cpx cmul(cpx a, cpx b) { return {a.r * b.r - a.i * b.i, a.r * b.i + a.i * b.r}; }
DI cpx cfma(cpx a, cpx h, cpx u) {
  return {fmaf(a.r, h.r, fmaf(-a.i, h.i, u.r)), fmaf(a.r, h.i, fmaf(a.i, h.r, u.i))};
}
DI float fma1(float a, float b, float c) { float d; asm("v_fma_f32 %0, %1, %2, %3" : "=v"(d) : "v"(a), "v"(b), "v"(c)); return d; }
DI float fms1(float a, float b, float c) { float d; asm("v_fma_f32 %0, -%1, %2, %3" : "=v"(d) : "v"(a), "v"(b), "v"(c)); return d; }
DI cpx cfma_s(cpx a, cpx h, cpx u) {
  return {fma1(a.r, h.r, fms1(a.i, h.i, u.r)), fma1(a.r, h.i, fma1(a.i, h.r, u.i))};
}
DI cpx csel(bool c, cpx a, cpx b) { return {c ? a.r : b.r, c ? a.i : b.i}; }

#define STAMP(i)
#define STAMP_DECL
#define STAMP_OUT()

struct Params {
  const char *x, *lre, *lim, *bre, *bim, *cre, *cim, *logdt, *dskip, *w1, *w2;
  float* out;
  char* ws;
};

DI void st16(__amdgpu_buffer_rsrc_t rs, unsigned off, v4u v) { __builtin_amdgcn_raw_buffer_store_b128(v, rs, (int)off, 0, 16); }
DI void st8(__amdgpu_buffer_rsrc_t rs, unsigned off, v2u v) { __builtin_amdgcn_raw_buffer_store_b64(v, rs, (int)off, 0, 16); }

DI void poll_flags(const char* fl, int l, u64 epoch) {
  GAS u64* fp = (GAS u64*)fl + (l & 3);
  unsigned spins = 0;
  while (!wall(__hip_atomic_load(fp, RLX_AGENT) == epoch)) {
    if (++spins > (1u << 20)) break;
    __builtin_amdgcn_s_sleep(4);
  }
  asm volatile("" ::: "memory");
}

DI f32x4 mfma16h(v2u a, v2u b, f32x4 c) {
  return __builtin_amdgcn_mfma_f32_16x16x16bf16_1k(__builtin_bit_cast(s16x4, a), __builtin_bit_cast(s16x4, b), c, 0, 0, 0);
}
DI v2u lds_tr1(LAS char* p0) { return __builtin_bit_cast(v2u, __builtin_amdgcn_ds_read_tr16_b64_v4i16((LAS s16x4*)p0)); }
DI void dma2(unsigned lds, const void* sb, unsigned v0, unsigned v1) {
  asm volatile(
      "s_mov_b32 m0, %0\n\ts_nop 0\n\t"
      "global_load_lds_dwordx4 %2, %1\n\t"
      "global_load_lds_dwordx4 %3, %1 offset:1024"
      :: "s"(lds), "s"(sb), "v"(v0), "v"(v1) : "memory");
}
DI void dma_stage(unsigned lds, const void* xb, const void* ub, const void* wb, unsigned vx0, unsigned vx1, unsigned vu, unsigned vw0, unsigned vw1) {
  asm volatile(
      "s_mov_b32 m0, %0\n\ts_nop 0\n\t"
      "global_load_lds_dwordx4 %5, %2\n\t"
      "global_load_lds_dwordx4 %6, %2 offset:1024\n\t"
      "global_load_lds_dwordx4 %7, %3 offset:2048\n\t"
      "global_load_lds_dwordx4 %8, %4 offset:3072\n\t"
      "s_mov_b32 m0, %1\n\ts_nop 0\n\t"
      "global_load_lds_dwordx4 %9, %4"
      :: "s"(lds), "s"(lds + 4096u), "s"(xb), "s"(ub), "s"(wb), "v"(vx0), "v"(vx1), "v"(vu), "v"(vw0), "v"(vw1) : "memory");
}
template <int CTRL> DI float dpp_add(float v) {
  return v + __builtin_bit_cast(float, __builtin_amdgcn_update_dpp(0, __builtin_bit_cast(int, v), CTRL, 0xf, 0xf, true));
}
DI float wave_sum(float v) {
  v = dpp_add<0x111>(v); v = dpp_add<0x112>(v); v = dpp_add<0x114>(v); v = dpp_add<0x118>(v);
  v = v + __builtin_bit_cast(float, __builtin_amdgcn_update_dpp(0, __builtin_bit_cast(int, v), 0x142, 0xa, 0xf, false));
  v = v + __builtin_bit_cast(float, __builtin_amdgcn_update_dpp(0, __builtin_bit_cast(int, v), 0x143, 0xc, 0xf, false));
  return __builtin_bit_cast(float, __builtin_amdgcn_readlane(__builtin_bit_cast(int, v), 63));
}
DI u64 mk_gran(u64 epoch, float v) {
  unsigned vb = __builtin_bit_cast(unsigned, v);
  vb = (vb + 0x80u) >> 8;
  return (epoch << 24) | (u64)(vb & 0xffffffu);
}
DI void dma1(unsigned lds, const void* sb, unsigned v) {
  asm volatile("s_mov_b32 m0, %0\n\ts_nop 0\n\tglobal_load_lds_dwordx4 %2, %1" :: "s"(lds), "s"(sb), "v"(v) : "memory");
}
#define FD 3
constexpr int usz(int u) { return (u & 3) == 3 ? 2 : 4; }
constexpr int f_cnt(bool kindA, int u, int at, int st) {
  int n = 0; bool seen = false;
  for (int a = 0; a < FD; a++) { if (seen) n += usz(a); if (kindA && a == u) seen = true; }
  for (int v = 0; v <= at; v++) {
    const int steps = v < at ? usz(v) : st;
    for (int t = 0; t < steps; t++) {
      if (v + FD < 16) {
        const int np = usz(v + FD), ns = usz(v);
        for (int i = 0; i < np; i++)
          if (i * ns / np == t) { if (seen) n++; if (kindA && v + FD == u && i == np - 1) seen = true; }
      }
      if (v + 4 < 16) { if (seen) n++; if (!kindA && v + 4 == u && t == usz(v) - 1) seen = true; }
    }
  }
  return n;
}
constexpr int hv_t(int n) { return n < 32 ? 8 + n / 4 : 8 + (n - 32) / 3; }
constexpr int hv_m(int n) { return n < 32 ? n % 4 : 4 + (n - 32) % 3; }
template <int... I, class F> DI void static_for(std::integer_sequence<int, I...>, F f) { (f(std::integral_constant<int, I>{}), ...); }
#define SB0() __builtin_amdgcn_sched_barrier(0)

__global__ __launch_bounds__(512, 2) NOPK void k_fused(Params p) {
  extern __shared__ __attribute__((aligned(16))) char smem[];
  LAS char* lds = (LAS char*)smem;
  STAMP_DECL
  STAMP(0);
  const int tid = threadIdx.x, l = tid & 63;
  const int w8 = __builtin_amdgcn_readfirstlane(tid >> 6);
  const int role = w8 >> 2, w = w8 & 3;
  const int bid = blockIdx.x, xcd = bid & 7, r = bid >> 3;
  const int j = 4 * (xcd >> 1) + (r >> 3);
  const int s = 8 * (xcd & 1) + (r & 7);
  const int hp2 = 8 * (xcd >> 1) + (r >> 2);
  const int ct = 4 * (xcd & 1) + (r & 3);
  const int odd = s & 1;
  const int i16 = l & 15, q = l >> 4;
  const int ta = i16 >> 2, tp = i16 & 3;
  const unsigned ldsb = (unsigned)(size_t)lds;
  const __amdgpu_buffer_rsrc_t rs = __builtin_amdgcn_make_buffer_rsrc((void*)p.ws, 0, (int)WS_END, 0x00020000);
  const v4u ones = {0x3F803F80u, 0x3F803F80u, 0x3F803F80u, 0x3F803F80u};

  const int rot = ROT ? ((2 * (r >> 3) + (r & 7)) & 7) : 0;
  u64 ep0;
  asm volatile("s_load_dwordx2 %0, %1, 0x0" : "=s"(ep0) : "s"(p.ws + WS_EPOCH) : "memory");
  unsigned plr[2], pli[2], pdt[2];
  v4u xr[2];
  unsigned cdum = 0;
  if (role == 0) {
    const char* rb = p.x + (size_t)(128 * j + 8 * s + 2 * w) * 1024;
    gld16<0>(xr[0], (unsigned)l * 16, rb); gld16<1024>(xr[1], (unsigned)l * 16, rb);
    const unsigned co = (unsigned)(32 * s + i16) * 2;
    gld_u16<0>(plr[0], co, p.lre); gld_u16<32>(plr[1], co, p.lre);
    gld_u16<0>(pli[0], co, p.lim); gld_u16<32>(pli[1], co, p.lim);
    gld_u16<0>(pdt[0], co, p.logdt); gld_u16<32>(pdt[1], co, p.logdt);
    {
      u64 pc;
      asm volatile("s_getpc_b64 %0" : "=s"(pc));
      const int ci = r * 4 + w;
      const char* cb = (const char*)(pc & ~255ull) + (ci < CODEPF ? ci : 0) * 1024;
      asm volatile("global_load_dword %0, %1, %2" : "=v"(cdum) : "v"((unsigned)l * 16), "s"(cb) : "memory");
    }
  }
  unsigned vx0, vx1, vu, vwA, vwB;
  {
    const int r8 = l >> 3, c8 = l & 7;
    const int tok0 = 8 * (r8 >> 2) + (r8 & 3);
    vx0 = (unsigned)(tok0 * 1024 + ((c8 ^ (r8 >> 1)) & 7) * 16);
    vx1 = (unsigned)((tok0 + 16) * 1024 + ((c8 ^ (4 + (r8 >> 1))) & 7) * 16);
    const int ch = 8 * w + r8, iu = 8 * (w & 1) + r8;
    vu = (unsigned)(ch * 1024 + ((c8 ^ (iu >> 1)) & 7) * 16);
    const int winoff = 448 * (s >> 1) + (odd ? 192 : 0);
    vwA = (unsigned)((16 * w + q) * 3584 + winoff + ((((i16 >> 1) ^ q) << 1) | (i16 & 1)) * 16);
    vwB = (unsigned)((16 * w + 8 + q) * 3584 + winoff + ((((i16 >> 1) ^ (q | 4)) << 1) | (i16 & 1)) * 16);
  }
  const char* xb = p.x + (size_t)(128 * j + 32 * (w >> 1) + 4 * (w & 1)) * 1024;
  const char* ub0 = p.bre + (size_t)(32 * s) * 1024;
  const char* ub1 = p.bim + (size_t)(32 * s) * 1024;
#define PIECE(N, I)                                                                                  \
  {                                                                                                  \
    constexpr int h_ = (N) / 3, n_ = (N) % 3;                                                        \
    const int kq_ = ((I) + rot) & 7;                                                                 \
    const unsigned d_ = ldsb + w * 3072 + ((I) & 3) * STG + h_ * 12288 + n_ * 1024;                  \
    if (n_ == 0) dma1(d_, xb + h_ * 65536 + kq_ * 128, vx0);                                         \
    if (n_ == 1) dma1(d_, xb + h_ * 65536 + kq_ * 128, vx1);                                         \
    if (n_ == 2) dma1(d_, (h_ ? ub1 : ub0) + kq_ * 128, vu);                                         \
  }
#define PALL(I) PIECE(0, I) PIECE(1, I) PIECE(2, I) PIECE(3, I) PIECE(4, I) PIECE(5, I)
#define W1P(C, n) dma1(ldsb + L_W1A + ((C) & 3) * 16384 + (16 * w + 4 * (n)) * 256,          \
                       p.w1 + (size_t)((((C) + rot) & 7) * 64 + ((n) & 1) * 4) * 3584, ((n) & 2) ? vwB : vwA)
  if (role == 1) {
    PALL(0)
    __builtin_amdgcn_s_barrier();
    PALL(1)
  } else {
    __builtin_amdgcn_s_barrier();
  }
  asm volatile("s_waitcnt lgkmcnt(0)" : "+s"(ep0) :: "memory");
  const u64 epoch = ep0 + 1ull;
  const unsigned tlo = (unsigned)(epoch & 0xffu), thi = (unsigned)(epoch >> 8);

  const unsigned sw0 = (unsigned)((i16 & 7) * 128 + (((0 + q) ^ (i16 >> 1)) & 7) * 16);
  const unsigned sw1 = (unsigned)((i16 & 7) * 128 + (((4 + q) ^ (i16 >> 1)) & 7) * 16);
  const unsigned xo = (unsigned)(2 * w * 3072 + (i16 >> 3) * 1024);
  const unsigned tro = (unsigned)((8 * q + ta) * 256 + 8 * tp);
  const int trkey = ta | ((q & 1) << 2);
  const unsigned zb = (unsigned)(2 * j + (w >> 1)) * HP_BYTES + (unsigned)l * 16u;
  const char* ab = p.ws + WS_Z + (size_t)hp2 * HP_BYTES;
  const unsigned avoff = (unsigned)l * 16;
  const int rot2 = ROT2 == 1 ? (hp2 & 3) : ROT2 == 2 ? (ct & 3) : 0;

  if (role == 0) {
    if (APRIO) __builtin_amdgcn_s_setprio(APRIO);
    asm volatile("s_waitcnt vmcnt(%8)" : "+v"(plr[0]), "+v"(plr[1]), "+v"(pli[0]), "+v"(pli[1]), "+v"(pdt[0]), "+v"(pdt[1]), "+v"(xr[0]), "+v"(xr[1]) : "i"(CODEPF ? 1 : 0) : "memory");
#pragma unroll
    for (int n = 0; n < 2; n++) {
      float v[8];
#pragma unroll
      for (int e = 0; e < 4; e++) { v[2 * e] = bf2f(xr[n][e] & 0xffffu); v[2 * e + 1] = __builtin_bit_cast(float, xr[n][e] & 0xffff0000u); }
      const float mean = wave_sum(((v[0] + v[1]) + (v[2] + v[3])) + ((v[4] + v[5]) + (v[6] + v[7]))) * (1.f / 512.f);
      float sq = 0.f;
#pragma unroll
      for (int e = 0; e < 8; e++) { const float d = v[e] - mean; sq = fmaf(d, d, sq); }
      const float rstd = rsqrtf(wave_sum(sq) * (1.f / 512.f) + 1e-5f);
      if (l < 2)
        __hip_atomic_store((GAS u64*)(p.ws + WS_SG) + (size_t)(128 * j + 8 * s + 2 * w + n) * 2 + l, mk_gran(epoch, l ? rstd : mean), RLX_AGENT);
    }
    cpx a1[2], cc[2], a8[2], pw[2], a128[2];
#pragma unroll
    for (int nb = 0; nb < 2; nb++) {
      const float lr = bf2f(plr[nb]), li = bf2f(pli[nb]);
      const float dtf = expf(bf2f(pdt[nb]));
      const float dt = bf2f(pk_bf16(dtf, 0.f) & 0xffffu);
      const float e = expf(dt * lr);
      float sn, cs;
      sincosf(dt * li, &sn, &cs);
      cpx a = {e * cs, e * sn};
      a1[nb] = a;
      const float nr = a.r - 1.f, ni = a.i, den = lr * lr + li * li;
      cc[nb] = {2.f * (nr * lr + ni * li) / den, 2.f * (ni * lr - nr * li) / den};
      cpx t = cmul(a, a); t = cmul(t, t); t = cmul(t, t);
      a8[nb] = t;
      cpx a16 = cmul(t, t), a32 = cmul(a16, a16), a64 = cmul(a32, a32);
      a128[nb] = cmul(a64, a64);
      cpx one = {1.f, 0.f};
      cpx pq = cmul(csel(q & 1, t, one), csel(q & 2, a16, one));
      cpx pwv = cmul(csel(w & 1, a32, one), csel(w & 2, a64, one));
      pw[nb] = cmul(pq, pwv);
    }
#pragma unroll
    for (int nb = 0; nb < 2; nb++)
      asm volatile("" : "+v"(a1[nb].r), "+v"(a1[nb].i), "+v"(cc[nb].r), "+v"(cc[nb].i), "+v"(a8[nb].r), "+v"(a8[nb].i), "+v"(pw[nb].r),
                   "+v"(pw[nb].i), "+v"(a128[nb].r), "+v"(a128[nb].i));
    STAMP(1);
    v4u cf[8][4];
    const unsigned cvoff = (unsigned)((64 * ct + (l >> 2)) * 1024 + (l & 3) * 16);
#define CLOAD(K0)                                                                         \
  _Pragma("unroll") for (int k = K0; k < K0 + 4; k++) {                                   \
    const int kr = (k + 2 * rot2) & 7;                                                    \
    const int sidx = 4 * w + 2 * (kr >> 2) + (kr & 1);                                    \
    const char* cb = (((kr >> 1) & 1) ? p.cim : p.cre) + sidx * 64;                       \
    _Pragma("unroll") for (int n = 0; n < 4; n++) gld16<0>(cf[k][n], cvoff, cb + n * 16384); \
    if (CPACE) __builtin_amdgcn_s_sleep(CPACE);                                           \
  }
#define CWAIT(K0, N)                                                                      \
  asm volatile("s_waitcnt vmcnt(%16)" : "+v"(cf[K0][0]), "+v"(cf[K0][1]), "+v"(cf[K0][2]), "+v"(cf[K0][3]), "+v"(cf[K0 + 1][0]),   \
               "+v"(cf[K0 + 1][1]), "+v"(cf[K0 + 1][2]), "+v"(cf[K0 + 1][3]), "+v"(cf[K0 + 2][0]), "+v"(cf[K0 + 2][1]),          \
               "+v"(cf[K0 + 2][2]), "+v"(cf[K0 + 2][3]), "+v"(cf[K0 + 3][0]), "+v"(cf[K0 + 3][1]), "+v"(cf[K0 + 3][2]),          \
               "+v"(cf[K0 + 3][3]) : "i"(N) : "memory");
#define CXPOSE(K0)                                                                        \
  _Pragma("unroll") for (int k = K0; k < K0 + 4; k++) {                                   \
    _Pragma("unroll") for (int n = 0; n < 4; n++) *(LAS v4u*)(lp(cwa) + n * 1024) = cf[k][n]; \
    _Pragma("unroll") for (int n = 0; n < 4; n++) cf[k][n] = lds_rd16(lp(cra) + n * 1024);  \
  }
    v2u sg;
    const unsigned sgoff = (unsigned)l * 8;
    const char* sgb = p.ws + WS_SG + (size_t)(128 * j + 32 * w) * 16;
    f32x4 au[2][2][2], asu;
#pragma unroll
    for (int b = 0; b < 2; b++)
#pragma unroll
      for (int nb = 0; nb < 2; nb++) { au[b][nb][0] = f32x4{0, 0, 0, 0}; au[b][nb][1] = f32x4{0, 0, 0, 0}; }
    asu = f32x4{0, 0, 0, 0};
    {
      const unsigned uo = (unsigned)((i16 >> 3) * 3072 + 2048);
      const int gs = (w & 1) * 2 + (w >> 1);
      unsigned cur = 0;
      v4u xd[2][2], wd[2][4], sd[2];
#pragma unroll
      for (int t = 0; t < 2; t++) xd[t][0] = xd[t][1] = wd[t][0] = wd[t][1] = wd[t][2] = wd[t][3] = sd[t] = v4u{0, 0, 0, 0};
#define A_STEP(RD, T, PX, PU, PS)                                                                     \
  {                                                                                                   \
    _Pragma("unroll") for (int g = 0; g < 4; g++) {                                                   \
      au[0][g & 1][g >> 1] = mfma16(xd[T][0], wd[T][g], au[0][g & 1][g >> 1]);                        \
      au[1][g & 1][g >> 1] = mfma16(xd[T][1], wd[T][g], au[1][g & 1][g >> 1]);                        \
      if (RD) wd[T][g] = lds_rd16(PU + g * 6144);                                                     \
      SB0();                                                                                          \
    }                                                                                                 \
    asu = mfma16(ones, sd[T], asu);                                                                   \
    if (RD) { xd[T][0] = lds_rd16(PX); xd[T][1] = lds_rd16(PX + 3072); sd[T] = lds_rd16(PS); }        \
    SB0();                                                                                            \
  }
#pragma unroll 1
      for (int it = 0; it < 8; it++) {
        const unsigned b0 = ldsb + cur;
        LAS char* px0 = lp(opaque(b0 + xo + sw0)); LAS char* px1 = lp(opaque(b0 + xo + sw1));
        LAS char* pu0 = lp(opaque(b0 + uo + sw0)); LAS char* pu1 = lp(opaque(b0 + uo + sw1));
        LAS char* ps0 = lp(opaque(b0 + uo + sw0 + gs * 6144)); LAS char* ps1 = lp(opaque(b0 + uo + sw1 + gs * 6144));
        cur = cur >= 3 * STG ? 0 : cur + STG;
        if (ALGKM) wait_lgkm();
        asm volatile("" ::: "memory");
        __builtin_amdgcn_s_barrier();
        asm volatile("" ::: "memory");
        SB0();
        A_STEP(true, 0, px0, pu0, ps0)
        A_STEP(true, 1, px1, pu1, ps1)
      }
      gld8_sc1(sg, sgoff, sgb);
      A_STEP(false, 0, lds, lds, lds)
      A_STEP(false, 1, lds, lds, lds)
#undef A_STEP
    }
    STAMP(2);
    {
      asm volatile("s_waitcnt vmcnt(0)" : "+v"(sg), "+v"(cdum) :: "memory");
      if (tid < 16) ((LAS unsigned*)(lds + L_CNT))[tid] = 0u;
      unsigned spins = 0;
      while (!wall(sg.y == thi && (sg.x >> 24) == tlo)) {
        if (++spins > (1u << 22)) break;
        __builtin_amdgcn_s_sleep(1);
        gld8_sc1(sg, sgoff, sgb);
        asm volatile("s_waitcnt vmcnt(0)" : "+v"(sg), "+v"(cdum) :: "memory");
      }
      const int tk = l >> 1;
      ((LAS unsigned*)(lds + L_STAT))[(w * 32 + ((tk >> 2) & 1) * 16 + ((tk & 3) | ((tk >> 3) << 2))) * 2 + (l & 1)] = sg.x << 8;
      if (q == 0) ((LAS float*)(lds + L_SU))[w * 16 + i16] = asu[0];
    }
    bar();
    STAMP(3);
    cpx u[2][8];
    {
      f32x2 stt[2][4];
#pragma unroll
      for (int b = 0; b < 2; b++) {
        f32x4 s0 = *(LAS f32x4*)(lds + L_STAT + (w * 32 + b * 16 + 4 * q) * 8);
        f32x4 s1 = *(LAS f32x4*)(lds + L_STAT + (w * 32 + b * 16 + 4 * q) * 8 + 16);
        stt[b][0] = f32x2{s0[0], s0[1]}; stt[b][1] = f32x2{s0[2], s0[3]};
        stt[b][2] = f32x2{s1[0], s1[1]}; stt[b][3] = f32x2{s1[2], s1[3]};
      }
#pragma unroll
      for (int nb = 0; nb < 2; nb++) {
        const float sre = ((LAS float*)(lds + L_SU))[(nb * 2 + 0) * 16 + i16];
        const float sim = ((LAS float*)(lds + L_SU))[(nb * 2 + 1) * 16 + i16];
        cpx h = {0.f, 0.f};
#pragma unroll
        for (int k = 0; k < 8; k++) {
          const int b = k >> 2, rg = k & 3;
          const float m = stt[b][rg][0], rr = stt[b][rg][1];
          u[nb][k].r = fmaf(-m, sre, au[b][nb][0][rg]) * rr;
          u[nb][k].i = fmaf(-m, sim, au[b][nb][1][rg]) * rr;
          h = cfma(a1[nb], h, u[nb][k]);
        }
        *(LAS f32x2*)(lds + L_E + ((nb * 16 + i16) * 16 + 4 * w + q) * 8) = f32x2{h.r, h.i};
      }
    }
    cnt_arrive(lds, C_E);
    cnt_wait(lds, C_E, 4);
    cpx cl[2];
    {
      cpx c[2] = {{0.f, 0.f}, {0.f, 0.f}};
      LAS f32x4* e4a = (LAS f32x4*)(lds + L_E + i16 * 128);
      LAS f32x4* e4b = (LAS f32x4*)(lds + L_E + (16 + i16) * 128);
#pragma unroll 1
      for (int k = 0; k < 2 * w; k++) {
        f32x4 ea = e4a[k], eb = e4b[k];
        c[0] = cfma(a8[0], c[0], cpx{ea[0], ea[1]}); c[1] = cfma(a8[1], c[1], cpx{eb[0], eb[1]});
        c[0] = cfma(a8[0], c[0], cpx{ea[2], ea[3]}); c[1] = cfma(a8[1], c[1], cpx{eb[2], eb[3]});
      }
      cpx tot[2];
#pragma unroll
      for (int nb = 0; nb < 2; nb++) {
        LAS f32x4* e4 = nb ? e4b : e4a;
        f32x4 e0 = e4[2 * w], e1 = e4[2 * w + 1];
        cpx c1 = cfma(a8[nb], c[nb], cpx{e0[0], e0[1]});
        cpx c2 = cfma(a8[nb], c1, cpx{e0[2], e0[3]});
        cpx c3 = cfma(a8[nb], c2, cpx{e1[0], e1[1]});
        tot[nb] = cfma(a8[nb], c3, cpx{e1[2], e1[3]});
        cl[nb] = csel(q & 2, csel(q & 1, c3, c2), csel(q & 1, c1, c[nb]));
      }
      if (w == 3 && j < 15) {
        const cpx t = (q & 2) ? tot[1] : tot[0];
        const float v = (q & 1) ? t.i : t.r;
        const u64 g = mk_gran(epoch, v);
        GAS u64* gp = (GAS u64*)(p.ws + WS_GRAN + (size_t)(j * 16 + s) * 512) + (((q >> 1) * 16 + i16) * 2 + (q & 1));
        __hip_atomic_store(gp, g, RLX_AGENT);
      }
    }
    STAMP(4);
    const unsigned cwa = opaque(ldsb + L_CST + w * 4096 + (l >> 2) * 64 + (((l & 3) ^ ((-(l >> 4)) & 3)) * 16));
    const unsigned cra = opaque(ldsb + L_CST + w * 4096 + i16 * 64 + ((q ^ ((-(i16 >> 2)) & 3)) * 16));
    unsigned wdum = 0;
    CLOAD(0)
    {
      const unsigned wv2 = (unsigned)((l >> 5) * 1024 + (l & 31) * 16);
#pragma unroll 1
      for (int k = 0; k < NWARM; k++)
        asm volatile("global_load_dword %0, %1, %2" : "+v"(wdum) : "v"(wv2), "s"(p.w2 + (size_t)((k * 32 + r) * 4 + w) * 2048 + (xcd & 1) * 512) : "memory");
    }
    CWAIT(0, NWARM)
    CXPOSE(0)
    if (w < 2) {
      const char* gb = p.ws + WS_GRAN + (size_t)s * 512 + w * 256;
      unsigned spins = 0;
      if (j > 0) {
        GAS u64* pp = (GAS u64*)(gb + (size_t)(l < j ? l : 0) * 8192 + 248);
        while (!wall((__hip_atomic_load(pp, RLX_AGENT) >> 24) == (epoch & 0xffffffffffull))) {
          if (++spins > (1u << 20)) break;
          __builtin_amdgcn_s_sleep(2);
        }
      }
      v4u gr[4];
      for (;;) {
#pragma unroll
        for (int m = 0; m < 4; m++) {
          const int pn = (4 * m + q) < j ? 4 * m + q : 0;
          gld16_sc1<0>(gr[m], (unsigned)(pn * 8192 + i16 * 16), gb);
        }
        wait_vm4<0>(gr[0], gr[1], gr[2], gr[3]);
        bool ok = true;
#pragma unroll
        for (int m = 0; m < 4; m++) {
          const bool need = (4 * m + q) < j;
          const bool good = (gr[m].y == thi) && ((gr[m].x >> 24) == tlo) && (gr[m].w == thi) && ((gr[m].z >> 24) == tlo);
          ok = ok && (good || !need);
        }
        if (wall(ok)) break;
        if (++spins > (1u << 20)) break;
        __builtin_amdgcn_s_sleep(2);
      }
      const cpx b128 = csel(w != 0, a128[1], a128[0]);
      const cpx a256 = cmul(b128, b128);
      const cpx a512 = cmul(a256, a256);
      cpx acc = {0.f, 0.f};
#pragma unroll
      for (int m = 0; m < 4; m++) {
        const bool need = (4 * m + q) < j;
        cpx e = {__builtin_bit_cast(float, gr[m].x << 8), __builtin_bit_cast(float, gr[m].z << 8)};
        acc = csel(need, cfma(a512, acc, e), acc);
      }
      const int dd = (j - 1 - q) & 3;
      cpx one = {1.f, 0.f};
      cpx fz = cmul(csel(dd & 1, b128, one), csel(dd & 2, a256, one));
      acc = cmul(acc, fz);
      *(LAS f32x2*)(lds + L_HIN + ((w * 16 + i16) * 4 + q) * 8) = f32x2{acc.r, acc.i};
      cnt_arrive(lds, C_HIN);
    }
    cnt_wait(lds, C_HIN, 2);
    STAMP(5);
    {
      LAS char* ht = lds + L_HT + w * 2048;
      unsigned hpk[2][2][2][2];
#pragma unroll
      for (int nb = 0; nb < 2; nb++) {
        const f32x4 h01 = *(LAS f32x4*)(lds + L_HIN + (nb * 16 + i16) * 32);
        const f32x4 h23 = *(LAS f32x4*)(lds + L_HIN + (nb * 16 + i16) * 32 + 16);
        const cpx acc = {(h01[0] + h01[2]) + (h23[0] + h23[2]), (h01[1] + h01[3]) + (h23[1] + h23[3])};
        cpx h = cfma(pw[nb], acc, cl[nb]);
        float orr[8], oi[8];
#pragma unroll
        for (int k = 0; k < 8; k++) {
          h = cfma(a1[nb], h, u[nb][k]);
          orr[k] = cc[nb].r * h.r - cc[nb].i * h.i;
          oi[k] = -(cc[nb].r * h.i + cc[nb].i * h.r);
        }
#pragma unroll
        for (int b = 0; b < 2; b++) {
          hpk[nb][0][b][0] = pk_bf16(orr[4 * b], orr[4 * b + 1]); hpk[nb][0][b][1] = pk_bf16(orr[4 * b + 2], orr[4 * b + 3]);
          hpk[nb][1][b][0] = pk_bf16(oi[4 * b], oi[4 * b + 1]);   hpk[nb][1][b][1] = pk_bf16(oi[4 * b + 2], oi[4 * b + 3]);
        }
      }
      const unsigned zy = zb + Y_OFF + (unsigned)(4 * (s >> 1) + (s & 1)) * 4096u + (unsigned)(2 * (w & 1)) * 1024u;
#pragma unroll
      for (int b = 0; b < 2; b++) {
#pragma unroll
        for (int nb = 0; nb < 2; nb++)
#pragma unroll
          for (int part = 0; part < 2; part++)
            *(LAS v2u*)(ht + (part * 32 + nb * 16 + i16) * 32 + q * 8) = v2u{hpk[nb][part][b][0], hpk[nb][part][b][1]};
#pragma unroll
        for (int part = 0; part < 2; part++) {
          LAS char* rp = ht + (part * 32 + 8 * q + ta) * 32 + tp * 8;
          v4u v = lds_tr(rp, rp + 4 * 32);
          st16(rs, (unsigned)WS_Z + zy + (unsigned)part * 8192u + b * 1024u, v);
        }
      }
    }
    STAMP(6);
    asm volatile("s_waitcnt vmcnt(0)" : "+v"(wdum) :: "memory");
    if (cnt_arrive(lds, C_HS) == 3 && l == 0)
      __hip_atomic_store((GAS u64*)(p.ws + WS_FLAG) + (j * 16 + s), epoch, RLX_AGENT);
    STAMP(7);
    CLOAD(4)
    CWAIT(4, 0)
    CXPOSE(4)
    cnt_arrive(lds, C_CX);
    STAMP(8);
    poll_flags(p.ws + WS_FLAG + (size_t)(j * 16 + 4 * w) * 8, l, epoch);
    STAMP(9);
    f32x4 acc[4][4];
#pragma unroll
    for (int a = 0; a < 4; a++)
#pragma unroll
      for (int b = 0; b < 4; b++) acc[a][b] = f32x4{0, 0, 0, 0};
    v4u af[3][4];
    const char* ay = ab + Y_OFF + (size_t)(8 * w) * 4096;
#define YL(K, i) gld16_sc1<(i) * 1024>(af[(K) % 3][i], avoff, ay + ((((K) + 2 * rot2) & 7) * 4096))
#define YST(K, tb)                                                                                  \
  {                                                                                                 \
    _Pragma("unroll") for (int nb = 0; nb < 4; nb++) acc[nb][tb] = mfma16(cf[K][nb], af[(K) % 3][tb], acc[nb][tb]); \
    SB0();                                                                                          \
    if ((K) + 2 < 8) YL((K) + 2, tb);                                                               \
    SB0();                                                                                          \
  }
#define YS(K)                                                                                       \
  {                                                                                                 \
    wait_vm4<((K) < 7 ? 4 : 0)>(af[(K) % 3][0], af[(K) % 3][1], af[(K) % 3][2], af[(K) % 3][3]);    \
    YST(K, 0) YST(K, 1) YST(K, 2) YST(K, 3)                                                         \
  }
    YL(0, 0); YL(0, 1); YL(0, 2); YL(0, 3); YL(1, 0); YL(1, 1); YL(1, 2); YL(1, 3);
    YS(0) YS(1) YS(2) YS(3) YS(4) YS(5) YS(6) YS(7)
#undef YL
#undef YS
#undef YST
    STAMP(10);
    cnt_wait(lds, C_CX, 4);
    cnt_wait(lds, C_B2, 4);
    {
      LAS char* yb = lds + ((w & 1) ? L_YB1 : L_YB0);
      if (w < 2) cnt_wait(lds, C_Y1 + (w & 1), 1);
#pragma unroll
      for (int tb = 0; tb < 4; tb++) {
        const int tk = 32 * (tb >> 1) + 8 * (i16 >> 2) + 4 * (tb & 1) + (i16 & 3);
#pragma unroll
        for (int nb = 0; nb < 4; nb++) {
          LAS f32x4* d = (LAS f32x4*)(yb + tk * 256 + (((4 * nb + q) ^ i16) & 15) * 16);
          if (w < 2) *d = *d + acc[nb][tb]; else *d = acc[nb][tb];
        }
      }
      if (w >= 2) cnt_arrive(lds, C_Y1 + (w & 1));
    }
    STAMP(11);
  } else {
    v4u xf[2][16];
    f32x4 ah[7][2];
#pragma unroll
    for (int mb = 0; mb < 7; mb++) { ah[mb][0] = f32x4{0, 0, 0, 0}; ah[mb][1] = f32x4{0, 0, 0, 0}; }
    unsigned pt[7];
#pragma unroll
    for (int mb = 0; mb < 7; mb++) pt[mb] = opaque(ldsb + L_W1A + tro + (unsigned)(((mb + odd) ^ trkey) * 32));
    const int m1 = w < 3 ? w + 4 : 6;
    const unsigned hq0 = opaque(ldsb + L_W1A + tro + (unsigned)(((w + odd) ^ trkey) * 32));
    const unsigned hq1 = opaque(ldsb + L_W1A + tro + (unsigned)(((m1 + odd) ^ trkey) * 32));
    f32x4 sh0 = {0, 0, 0, 0}, sh1 = {0, 0, 0, 0};
    {
      LAS char* px0 = lp(opaque(ldsb + xo + sw0)); LAS char* px1 = lp(opaque(ldsb + xo + sw1));
#define L1IT(I, NW)                                                                                   \
  {                                                                                                   \
    wait_vm<NW>();                                                                                    \
    __builtin_amdgcn_s_barrier();                                                                     \
    asm volatile("" ::: "memory");                                                                    \
    xf[0][2 * (I)] = lds_rd16(px0 + ((I) & 3) * STG); xf[1][2 * (I)] = lds_rd16(px0 + ((I) & 3) * STG + 3072); \
    xf[0][2 * (I) + 1] = lds_rd16(px1 + ((I) & 3) * STG); xf[1][2 * (I) + 1] = lds_rd16(px1 + ((I) & 3) * STG + 3072); \
    SB0();                                                                                            \
    if ((I) < 3 || (I) == 7) {                                                                        \
      if ((I) < 6) { PALL((I) + 2) }                                                                  \
      W1P(I, 0); W1P(I, 1); W1P(I, 2); W1P(I, 3);                                                     \
    } else {                                                                                          \
      constexpr int c_ = (I) - 3;                                                                     \
      v4u wa[7], wb_[7], h0[2], h1[2];                                                                \
      _Pragma("unroll") for (int mb = 0; mb < 7; mb++) wa[mb] = lds_tr(lp(pt[mb]) + c_ * 16384, lp(pt[mb]) + c_ * 16384 + 1024); \
      SB0();                                                                                          \
      if ((I) < 6) { PIECE(0, (I) + 2) PIECE(1, (I) + 2) PIECE(2, (I) + 2) }                          \
      SB0();                                                                                          \
      _Pragma("unroll") for (int mb = 0; mb < 7; mb++) {                                              \
        ah[mb][0] = mfma16(wa[mb], xf[0][2 * c_], ah[mb][0]);                                         \
        ah[mb][1] = mfma16(wa[mb], xf[1][2 * c_], ah[mb][1]);                                         \
        wb_[mb] = lds_tr(lp(pt[mb]) + c_ * 16384 + 8192, lp(pt[mb]) + c_ * 16384 + 9216);             \
        if ((I) < 6) { if (mb == 1) PIECE(3, (I) + 2) if (mb == 3) PIECE(4, (I) + 2) if (mb == 5) PIECE(5, (I) + 2) } \
        SB0();                                                                                        \
      }                                                                                               \
      _Pragma("unroll") for (int mb = 0; mb < 7; mb++) {                                              \
        ah[mb][0] = mfma16(wb_[mb], xf[0][2 * c_ + 1], ah[mb][0]);                                    \
        ah[mb][1] = mfma16(wb_[mb], xf[1][2 * c_ + 1], ah[mb][1]);                                    \
        if (mb == 0) { h0[0] = lds_tr(lp(hq0) + c_ * 16384, lp(hq0) + c_ * 16384 + 1024); h1[0] = lds_tr(lp(hq1) + c_ * 16384, lp(hq1) + c_ * 16384 + 1024); } \
        if (mb == 1) { h0[1] = lds_tr(lp(hq0) + c_ * 16384 + 8192, lp(hq0) + c_ * 16384 + 9216); h1[1] = lds_tr(lp(hq1) + c_ * 16384 + 8192, lp(hq1) + c_ * 16384 + 9216); } \
        if (mb == 0) W1P(I, 0); if (mb == 2) W1P(I, 1); if (mb == 4) W1P(I, 2); if (mb == 6) W1P(I, 3); \
        SB0();                                                                                        \
      }                                                                                               \
      sh0 = mfma16(h0[0], ones, sh0); sh1 = mfma16(h1[0], ones, sh1);                                 \
      sh0 = mfma16(h0[1], ones, sh0); sh1 = mfma16(h1[1], ones, sh1);                                 \
    }                                                                                                 \
    wait_lgkm();                                                                                      \
  }
      L1IT(0, 6) L1IT(1, 10) L1IT(2, 14) STAMP(15); L1IT(3, 14) L1IT(4, 14) L1IT(5, 14) L1IT(6, 14) L1IT(7, 8)
#undef L1IT
    }
    STAMP(2);
    wait_vm<8>();
    bar();
    STAMP(3);
    const unsigned ring = ldsb + L_RING + w * RING;
    unsigned fv;
    {
      const int row = l >> 3, c16p = l & 7;
      fv = (unsigned)(row * 1024 + ct * 128 + ((((c16p >> 1) ^ ((row >> 1) & 3)) << 1) | (c16p & 1)) * 16);
    }
    const char* w2b = p.w2 + (size_t)(448 * w) * 1024;
#define WP(N, n) dma1(ring + ((N) & 1) * 14336 + (n) * 1024, w2b + (size_t)(((N) + rot2) & 3) * (112 * 1024) + (n) * 8192, fv)
    {
      v4u wd[7];
      static_for(std::make_integer_sequence<int, 7>{}, [&](auto NC) __attribute__((always_inline)) NOPK {
        constexpr int n = decltype(NC)::value, T2 = hv_t(n), m2 = hv_m(n);
        wd[n] = lds_tr(lp(pt[m2]) + (T2 & 7) * 8192, lp(pt[m2]) + (T2 & 7) * 8192 + 1024);
      });
      const unsigned zf = zb + (unsigned)s * 14336u;
      float nm[2], r2[2];
      v4u hs0, hs1;
      unsigned pkd[7][2][2];
      auto epi = [&](auto MB, auto BB) __attribute__((always_inline)) NOPK {
        constexpr int mb = decltype(MB)::value, b = decltype(BB)::value;
        const f32x4 sh = *(LAS f32x4*)(lds + L_SH + (mb * 16 + 4 * q) * 4);
        float y[4];
#pragma unroll
        for (int e = 0; e < 4; e++) {
          const float d = fmaf(sh[e], nm[b], ah[mb][b][e]);
          y[e] = d * __builtin_amdgcn_rcpf(1.f + __builtin_amdgcn_exp2f(d * r2[b]));
        }
        pkd[mb][b][0] = pk_bf16(y[0], y[1]); pkd[mb][b][1] = pk_bf16(y[2], y[3]);
        if constexpr ((mb & 1) == 1) {
          v4u v = {pkd[mb - 1][b][0], pkd[mb - 1][b][1], pkd[mb][b][0], pkd[mb][b][1]};
          st16(rs, (unsigned)WS_Z + zf + (unsigned)(mb >> 1) * 4096u + (unsigned)(2 * (w & 1) + b) * 1024u, v);
        }
      };
      static_for(std::make_integer_sequence<int, 56>{}, [&](auto NC) __attribute__((always_inline)) NOPK {
        constexpr int n = decltype(NC)::value, T = hv_t(n), mb = hv_m(n), sl = n % 7;
        if constexpr (n == 9) {
          wait_vm<0>();
          cnt_arrive(lds, C_W);
          cnt_wait(lds, C_W, 4);
        }
        if constexpr (n < 32 && (n & 3) == 0) {
          hs0 = lds_tr(lp(hq0) + (n / 4) * 8192, lp(hq0) + (n / 4) * 8192 + 1024);
          hs1 = lds_tr(lp(hq1) + (n / 4) * 8192, lp(hq1) + (n / 4) * 8192 + 1024);
        }
        if constexpr (n == 32) {
          if (i16 == 0) {
            *(LAS f32x4*)(lds + L_SH + (w * 16 + 4 * q) * 4) = sh0;
            if (w < 3) *(LAS f32x4*)(lds + L_SH + (m1 * 16 + 4 * q) * 4) = sh1;
          }
          cnt_arrive(lds, C_SH);
          cnt_wait(lds, C_SH, 4);
          const f32x2 s0 = *(LAS f32x2*)(lds + L_STAT + (w * 32 + i16) * 8), s1 = *(LAS f32x2*)(lds + L_STAT + (w * 32 + 16 + i16) * 8);
          nm[0] = -s0[0]; nm[1] = -s1[0]; r2[0] = s0[1] * -1.44269504088896f; r2[1] = s1[1] * -1.44269504088896f;
        }
        ah[mb][0] = mfma16(wd[sl], xf[0][T], ah[mb][0]);
        ah[mb][1] = mfma16(wd[sl], xf[1][T], ah[mb][1]);
        if constexpr (n < 32 && (n & 3) == 3) { sh0 = mfma16(hs0, ones, sh0); sh1 = mfma16(hs1, ones, sh1); }
        if constexpr (n + 7 < 56) {
          constexpr int T2 = hv_t(n + 7), m2 = hv_m(n + 7);
          wd[sl] = lds_tr(lp(pt[m2]) + (T2 & 7) * 8192, lp(pt[m2]) + (T2 & 7) * 8192 + 1024);
        }
        if constexpr (n >= 32 && (n - 32) % 3 == 1) {
          constexpr int u = (n - 32) / 3;
          epi(std::integral_constant<int, (u >> 2) * 2 + (u & 1)>{}, std::integral_constant<int, (u >> 1) & 1>{});
        }
        SB0();
      });
      STAMP(1);
      cnt_arrive(lds, C_B2);
      epi(std::integral_constant<int, 4>{}, std::integral_constant<int, 0>{});
      epi(std::integral_constant<int, 5>{}, std::integral_constant<int, 0>{});
      epi(std::integral_constant<int, 4>{}, std::integral_constant<int, 1>{});
      epi(std::integral_constant<int, 5>{}, std::integral_constant<int, 1>{});
      epi(std::integral_constant<int, 6>{}, std::integral_constant<int, 0>{});
      epi(std::integral_constant<int, 6>{}, std::integral_constant<int, 1>{});
      v4u v = {pkd[6][0][0], pkd[6][0][1], pkd[6][1][0], pkd[6][1][1]};
      st16(rs, (unsigned)WS_Z + zf + 12288u + (unsigned)(w & 1) * 1024u, v);
    }
    STAMP(4);
    wait_vm<0>();
    if (cnt_arrive(lds, C_FS) == 3 && l == 0)
      __hip_atomic_store((GAS u64*)(p.ws + WS_FLAG) + (256 + j * 16 + s), epoch, RLX_AGENT);
    STAMP(5);
    cnt_wait(lds, C_B2, 4);
#pragma unroll
    for (int n = 0; n < 14; n++) WP(0, n);
    poll_flags(p.ws + WS_FLAG + (size_t)(256 + j * 16 + 4 * w) * 8, l, epoch);
    STAMP(6);
    f32x4 acc[4][4];
#pragma unroll
    for (int a = 0; a < 4; a++)
#pragma unroll
      for (int b = 0; b < 4; b++) acc[a][b] = f32x4{0, 0, 0, 0};
    v4u af[4][4];
    const char* afb = ab + (size_t)(4 * w) * 14336;
    unsigned ftr[4];
#pragma unroll
    for (int nb = 0; nb < 4; nb++) {
      const int row = 4 * q + ta;
      ftr[nb] = opaque(ring + (unsigned)(row * 128 + ((nb ^ ((row >> 1) & 3)) * 32) + tp * 8));
    }
#define ALD(U, i) gld16_sc1<(i) * 1024>(af[(U) & 3][i], avoff, afb + (size_t)((((U) >> 2) + rot2) & 3) * 14336 + ((U) & 3) * 4096)
#define ALDS(U, V, t)                             \
  {                                                                                                 \
    if ((V) < 16) {                                                                                 \
      if (0 * usz(U) / usz(V) == (t)) ALD(V, 0);                                                    \
      if (1 * usz(U) / usz(V) == (t)) ALD(V, 1);                                                    \
      if (usz(V) == 4 && 2 * usz(U) / usz(V) == (t)) ALD(V, 2);                                     \
      if (usz(V) == 4 && 3 * usz(U) / usz(V) == (t)) ALD(V, 3);                                     \
    }                                                                                               \
  }
#define WRD(U)                                           \
  {                                                                                                 \
    _Pragma("unroll") for (int nb = 0; nb < 4; nb++) {                                              \
      LAS char* b_ = lp(ftr[nb]) + (((U) >> 2) & 1) * 14336 + ((U) & 3) * 4096;                     \
      if (((U) & 3) == 3) { v2u t_ = lds_tr1(b_); wf[(U) & 1][nb].x = t_.x; wf[(U) & 1][nb].y = t_.y; } \
      else wf[(U) & 1][nb] = lds_tr(b_, b_ + 2048);                                                 \
    }                                                                                               \
  }
#define FSTEP(U, t)                                                                                 \
  {                                                                                                 \
    if (((U) & 3) < 3) {                                                                            \
      _Pragma("unroll") for (int nb = 0; nb < 4; nb++)                                              \
          acc[nb][t] = mfma16(wf[(U) & 1][nb], af[(U) & 3][t], acc[nb][t]);                         \
    } else {                                                                                        \
      _Pragma("unroll") for (int nb = 0; nb < 4; nb++) {                                            \
        const v2u wh_ = {wf[(U) & 1][nb].x, wf[(U) & 1][nb].y};                                     \
        acc[nb][(2 * (t)) & 3] = mfma16h(wh_, v2u{af[3][t].x, af[3][t].y}, acc[nb][(2 * (t)) & 3]); \
        acc[nb][(2 * (t) + 1) & 3] = mfma16h(wh_, v2u{af[3][t].z, af[3][t].w}, acc[nb][(2 * (t) + 1) & 3]); \
      }                                                                                             \
    }                                                                                               \
    SB0();                                                                                          \
    ALDS(U, (U) + FD, t)                                                                            \
    if ((U) < 12) WP(((U) >> 2) + 1, 4 * ((U) & 3) + (t));                                          \
    if ((t) == usz(U) - 2 && (U) < 15) {                                                            \
      if ((U) + 1 >= 4) wait_vm<f_cnt(false, (U) + 1, U, (t) + 1)>();                               \
      WRD((U) + 1)                                                                                  \
    }                                                                                               \
    SB0();                                                                                          \
  }
#define FU(U)                                                                                       \
  {                                                                                                 \
    if (((U) & 3) < 3) {                                                                            \
      wait_vm4<f_cnt(true, U, U, 0)>(af[(U) & 3][0], af[(U) & 3][1], af[(U) & 3][2], af[(U) & 3][3]); \
      FSTEP(U, 0) FSTEP(U, 1) FSTEP(U, 2) FSTEP(U, 3)                                               \
    } else {                                                                                        \
      asm volatile("s_waitcnt vmcnt(%2)" : "+v"(af[3][0]), "+v"(af[3][1]) : "i"(f_cnt(true, U, U, 0)) : "memory"); \
      FSTEP(U, 0) FSTEP(U, 1)                                                                       \
    }                                                                                               \
  }
    v4u wf[2][4];
    ALD(0, 0); ALD(0, 1); ALD(0, 2); ALD(0, 3);
    ALD(1, 0); ALD(1, 1); ALD(1, 2); ALD(1, 3);
    ALD(2, 0); ALD(2, 1); ALD(2, 2); ALD(2, 3);
    static_assert(FD == 3, "window");
    WRD(0)
    FU(0) STAMP(7); FU(1) FU(2) FU(3) STAMP(8); FU(4) FU(5) FU(6) FU(7) STAMP(9); FU(8) FU(9) FU(10) FU(11) STAMP(14); FU(12) FU(13) FU(14) FU(15)
#undef FU
#undef FSTEP
#undef WRD
#undef ALD
#undef ALDS
#undef WP
    STAMP(10);
    {
      LAS char* dst = lp(ring);
#pragma unroll
      for (int tb = 0; tb < 4; tb++) {
        const int tk = 32 * (tb >> 1) + 8 * (i16 >> 2) + 4 * (tb & 1) + (i16 & 3);
#pragma unroll
        for (int nb = 0; nb < 4; nb++)
          *(LAS f32x4*)(dst + tk * 256 + (((4 * nb + q) ^ i16) & 15) * 16) = acc[nb][tb];
      }
    }
    STAMP(11);
  }
  {
    const int ec = tid & 15, et = tid >> 4;
    v2u xe[2];
    const v2u dk = *(const GAS v2u*)(p.dskip + (64 * ct + 4 * ec) * 2);
#pragma unroll
    for (int n = 0; n < 2; n++)
      xe[n] = *(const GAS v2u*)(p.x + (size_t)(64 * hp2 + et + 32 * n) * 1024 + (64 * ct + 4 * ec) * 2);
    bar();
    STAMP(12);
    if (j == 15 && s == 0 && tid == 0)
      __hip_atomic_store((GAS u64*)(p.ws + WS_EPOCH), epoch, RLX_AGENT);
    const float dkf[4] = {bf2f(dk.x & 0xffff), bf2f(dk.x >> 16), bf2f(dk.y & 0xffff), bf2f(dk.y >> 16)};
#pragma unroll
    for (int n = 0; n < 2; n++) {
      const int tk = et + 32 * n;
      const int key = (tk & 3) | (((tk >> 3) & 3) << 2);
      const int o = tk * 256 + ((ec ^ key) & 15) * 16;
      LAS char* fb = lds + L_RING + o;
      const f32x4 f = (*(LAS f32x4*)(fb) + *(LAS f32x4*)(fb + RING)) + (*(LAS f32x4*)(fb + 2 * RING) + *(LAS f32x4*)(fb + 3 * RING));
      const f32x4 y = *(LAS f32x4*)(lds + L_YB0 + o) + *(LAS f32x4*)(lds + L_YB1 + o);
      const int tkp = 64 * (hp2 & 1) + tk;
      const f32x2 se = *(LAS f32x2*)(lds + L_STAT + ((tkp >> 5) * 32 + ((tkp >> 2) & 1) * 16 + key) * 8);
      const float xv[4] = {bf2f(xe[n].x & 0xffff), bf2f(xe[n].x >> 16), bf2f(xe[n].y & 0xffff), bf2f(xe[n].y >> 16)};
      f32x4 o4;
#pragma unroll
      for (int e = 0; e < 4; e++) {
        const float xn = (xv[e] - se[0]) * se[1];
        const float sv = fmaf(xn, dkf[e], y[e]);
        const float fe = f[e] * se[1];
        const float g = fe * __builtin_amdgcn_rcpf(1.f + __builtin_amdgcn_exp2f(fe * -1.44269504088896f));
        o4[e] = fmaf(sv, g, xv[e]);
      }
      *(GAS f32x4*)(p.out + (size_t)(64 * hp2 + tk) * 512 + 64 * ct + 4 * ec) = o4;
    }
  }
  STAMP(13);
  STAMP_OUT();
}

extern "C" void kernel_launch(void* const* d_in, const int* in_sizes, int n_in, void* d_out, int out_size, void* d_ws,
                              size_t ws_size, hipStream_t stream) {
  Params p;
  p.x = (const char*)d_in[0]; p.lre = (const char*)d_in[1]; p.lim = (const char*)d_in[2];
  p.bre = (const char*)d_in[3]; p.bim = (const char*)d_in[4]; p.cre = (const char*)d_in[5]; p.cim = (const char*)d_in[6];
  p.logdt = (const char*)d_in[7]; p.dskip = (const char*)d_in[8]; p.w1 = (const char*)d_in[10]; p.w2 = (const char*)d_in[11];
  p.out = (float*)d_out; p.ws = (char*)d_ws;
  (void)hipFuncSetAttribute((const void*)k_fused, hipFuncAttributeMaxDynamicSharedMemorySize, LDS_BYTES);
  k_fused<<<256, 512, LDS_BYTES, stream>>>(p);
}
```

```cpp
#include <hip/hip_runtime.h>
#include <hip/hip_bf16.h>
#include <type_traits>
#include <utility>

typedef unsigned v4u __attribute__((ext_vector_type(4)));
typedef unsigned v2u __attribute__((ext_vector_type(2)));
typedef float f32x4 __attribute__((ext_vector_type(4)));
typedef float f32x2 __attribute__((ext_vector_type(2)));
typedef short bf16x8 __attribute__((ext_vector_type(8)));
typedef short s16x4 __attribute__((ext_vector_type(4)));
typedef __bf16 bf16x2_t __attribute__((ext_vector_type(2)));
typedef unsigned long long u64;
#define LAS __attribute__((address_space(3)))
#define GAS __attribute__((address_space(1)))
#if defined(__HIP_DEVICE_COMPILE__)
#define NOPK __attribute__((target("no-packed-fp32-ops")))
#else
#define NOPK
#endif
#define DI __device__ __forceinline__ NOPK
#define RLX_AGENT __ATOMIC_RELAXED, __HIP_MEMORY_SCOPE_AGENT
#define RLX_WG __ATOMIC_RELAXED, __HIP_MEMORY_SCOPE_WORKGROUP

static constexpr unsigned HP_BYTES = 16 * 14336 + 32 * 4096;
static constexpr unsigned Y_OFF = 16 * 14336;
static constexpr size_t WS_Z = 0;
static constexpr size_t WS_Z_BYTES = (size_t)32 * HP_BYTES;
static constexpr size_t WS_GRAN = WS_Z + WS_Z_BYTES;
static constexpr size_t WS_SG = WS_GRAN + 16 * 16 * 512;
static constexpr size_t WS_FLAG = WS_SG + 2048 * 16;
static constexpr size_t WS_EPOCH = WS_FLAG + 2 * 16 * 16 * 8;
static constexpr size_t WS_END = WS_EPOCH + 128;

static constexpr int LDS_BYTES = 160 * 1024;
static constexpr int STG = 24576;
static constexpr int L_W1A = 96 * 1024;
static constexpr int L_W1B = 32 * 1024;
static constexpr int L_CST = 0;
static constexpr int L_YB1 = L_CST;
static constexpr int L_HT = 16 * 1024;
static constexpr int L_E = 24 * 1024;
static constexpr int L_KEEP = 28 * 1024;
static constexpr int L_YB0 = 32 * 1024;
static constexpr int L_RING = 48 * 1024;
static constexpr int L_STAT = L_KEEP;
static constexpr int L_SU = L_KEEP + 1024;
static constexpr int L_SH = L_KEEP + 1280;
static constexpr int L_CNT = L_KEEP + 1792;
static constexpr int RING = 28672;
static constexpr int L_HIN = L_KEEP + 2048;
enum { C_W = 9, C_SH = 10, C_B2 = 11, C_E = 0, C_CX = 1, C_HIN = 2, C_HS = 4, C_FS = 5, C_Y1 = 7 };
#define ROT 1
#define CIT 3
#define NOWARM 0
#define ALGKM 0
#define LATEC 1
#define VM0 0
#define CPACE 2
#define CODEPF 38
#define APRIO 2
#define NWARM 7
#define ROT2 1

DI bool wall(bool x) { return __builtin_amdgcn_ballot_w64(!x) == 0ull; }
DI f32x4 mfma16(v4u a, v4u b, f32x4 c) {
  return __builtin_amdgcn_mfma_f32_16x16x32_bf16(__builtin_bit_cast(bf16x8, a), __builtin_bit_cast(bf16x8, b), c, 0, 0, 0);
}
DI unsigned pk_bf16(float lo, float hi) {
  f32x2 v = {lo, hi};
  return __builtin_bit_cast(unsigned, __builtin_convertvector(v, bf16x2_t));
}
DI unsigned pk_bf16v(f32x2 v) { return __builtin_bit_cast(unsigned, __builtin_convertvector(v, bf16x2_t)); }
DI float bf2f(unsigned u) { return __builtin_bit_cast(float, u << 16); }
DI unsigned opaque(unsigned v) { asm volatile("" : "+v"(v)); return v; }
DI LAS char* lp(unsigned a) { return (LAS char*)(size_t)a; }

template <int IMM> DI void gld16(v4u& d, unsigned voff, const void* sb) {
  asm volatile("global_load_dwordx4 %0, %1, %2 offset:%3" : "=v"(d) : "v"(voff), "s"(sb), "i"(IMM) : "memory");
}
template <int IMM> DI void gld16_sc1(v4u& d, unsigned voff, const void* sb) {
  asm volatile("global_load_dwordx4 %0, %1, %2 offset:%3 sc1" : "=v"(d) : "v"(voff), "s"(sb), "i"(IMM) : "memory");
}
template <int IMM> DI void gld_u16(unsigned& d, unsigned voff, const void* sb) {
  asm volatile("global_load_ushort %0, %1, %2 offset:%3" : "=v"(d) : "v"(voff), "s"(sb), "i"(IMM) : "memory");
}
DI void gld8_sc1(v2u& d, unsigned voff, const void* sb) {
  asm volatile("global_load_dwordx2 %0, %1, %2 sc1" : "=v"(d) : "v"(voff), "s"(sb) : "memory");
}
DI void dma4(unsigned lds, const void* sb, unsigned v0, unsigned v1, unsigned v2, unsigned v3) {
  asm volatile(
      "s_mov_b32 m0, %0\n\ts_nop 0\n\t"
      "global_load_lds_dwordx4 %2, %1\n\t"
      "global_load_lds_dwordx4 %3, %1 offset:1024\n\t"
      "global_load_lds_dwordx4 %4, %1 offset:2048\n\t"
      "global_load_lds_dwordx4 %5, %1 offset:3072"
      :: "s"(lds), "s"(sb), "v"(v0), "v"(v1), "v"(v2), "v"(v3) : "memory");
}
template <int N> DI void wait_vm() { asm volatile("s_waitcnt vmcnt(%0)" :: "i"(N) : "memory"); }
template <int N> DI void wait_vm4(v4u& a, v4u& b, v4u& c, v4u& d) {
  asm volatile("s_waitcnt vmcnt(%4)" : "+v"(a), "+v"(b), "+v"(c), "+v"(d) : "i"(N) : "memory");
}
DI void wait_lgkm() { asm volatile("s_waitcnt lgkmcnt(0)" ::: "memory"); }
DI void bar() { wait_lgkm(); __builtin_amdgcn_s_barrier(); asm volatile("" ::: "memory"); }

DI v4u lds_rd16(LAS char* p) { return *(LAS v4u*)p; }
DI v4u lds_tr(LAS char* p0, LAS char* p1) {
  s16x4 a = __builtin_amdgcn_ds_read_tr16_b64_v4i16((LAS s16x4*)p0);
  s16x4 b = __builtin_amdgcn_ds_read_tr16_b64_v4i16((LAS s16x4*)p1);
  bf16x8 f = __builtin_shufflevector(a, b, 0, 1, 2, 3, 4, 5, 6, 7);
  return __builtin_bit_cast(v4u, f);
}
DI unsigned cnt_arrive(LAS char* lds, int c) {
  wait_lgkm();
  unsigned old = 0;
  if ((threadIdx.x & 63) == 0) old = __hip_atomic_fetch_add((LAS unsigned*)(lds + L_CNT) + c, 1u, RLX_WG);
  return __builtin_amdgcn_readfirstlane(old);
}
DI void cnt_wait(LAS char* lds, int c, unsigned target) {
  unsigned spins = 0;
  while ((unsigned)__builtin_amdgcn_readfirstlane(__hip_atomic_load((LAS unsigned*)(lds + L_CNT) + c, RLX_WG)) < target) {
    if (++spins > (1u << 24)) break;
    __builtin_amdgcn_s_sleep(1);
  }
  asm volatile("" ::: "memory");
}
struct cpx { float r, i; };
DI cpx cmul(cpx a, cpx b) { return {a.r * b.r - a.i * b.i, a.r * b.i + a.i * b.r}; }
DI cpx cfma(cpx a, cpx h, cpx u) {
  return {fmaf(a.r, h.r, fmaf(-a.i, h.i, u.r)), fmaf(a.r, h.i, fmaf(a.i, h.r, u.i))};
}
DI float fma1(float a, float b, float c) { float d; asm("v_fma_f32 %0, %1, %2, %3" : "=v"(d) : "v"(a), "v"(b), "v"(c)); return d; }
DI float fms1(float a, float b, float c) { float d; asm("v_fma_f32 %0, -%1, %2, %3" : "=v"(d) : "v"(a), "v"(b), "v"(c)); return d; }
DI cpx cfma_s(cpx a, cpx h, cpx u) {
  return {fma1(a.r, h.r, fms1(a.i, h.i, u.r)), fma1(a.r, h.i, fma1(a.i, h.r, u.i))};
}
DI cpx csel(bool c, cpx a, cpx b) { return {c ? a.r : b.r, c ? a.i : b.i}; }

#define STAMP(i)
#define STAMP_DECL
#define STAMP_OUT()

struct Params {
  const char *x, *lre, *lim, *bre, *bim, *cre, *cim, *logdt, *dskip, *w1, *w2;
  float* out;
  char* ws;
};

DI void st16(__amdgpu_buffer_rsrc_t rs, unsigned off, v4u v) { __builtin_amdgcn_raw_buffer_store_b128(v, rs, (int)off, 0, 16); }
DI void st8(__amdgpu_buffer_rsrc_t rs, unsigned off, v2u v) { __builtin_amdgcn_raw_buffer_store_b64(v, rs, (int)off, 0, 16); }

DI void poll_flags(const char* fl, int l, u64 epoch) {
  GAS u64* fp = (GAS u64*)fl + (l & 3);
  unsigned spins = 0;
  while (!wall(__hip_atomic_load(fp, RLX_AGENT) == epoch)) {
    if (++spins > (1u << 20)) break;
    __builtin_amdgcn_s_sleep(4);
  }
  asm volatile("" ::: "memory");
}

DI f32x4 mfma16h(v2u a, v2u b, f32x4 c) {
  return __builtin_amdgcn_mfma_f32_16x16x16bf16_1k(__builtin_bit_cast(s16x4, a), __builtin_bit_cast(s16x4, b), c, 0, 0, 0);
}
DI v2u lds_tr1(LAS char* p0) { return __builtin_bit_cast(v2u, __builtin_amdgcn_ds_read_tr16_b64_v4i16((LAS s16x4*)p0)); }
DI void dma2(unsigned lds, const void* sb, unsigned v0, unsigned v1) {
  asm volatile(
      "s_mov_b32 m0, %0\n\ts_nop 0\n\t"
      "global_load_lds_dwordx4 %2, %1\n\t"
      "global_load_lds_dwordx4 %3, %1 offset:1024"
      :: "s"(lds), "s"(sb), "v"(v0), "v"(v1) : "memory");
}
DI void dma_stage(unsigned lds, const void* xb, const void* ub, const void* wb, unsigned vx0, unsigned vx1, unsigned vu, unsigned vw0, unsigned vw1) {
  asm volatile(
      "s_mov_b32 m0, %0\n\ts_nop 0\n\t"
      "global_load_lds_dwordx4 %5, %2\n\t"
      "global_load_lds_dwordx4 %6, %2 offset:1024\n\t"
      "global_load_lds_dwordx4 %7, %3 offset:2048\n\t"
      "global_load_lds_dwordx4 %8, %4 offset:3072\n\t"
      "s_mov_b32 m0, %1\n\ts_nop 0\n\t"
      "global_load_lds_dwordx4 %9, %4"
      :: "s"(lds), "s"(lds + 4096u), "s"(xb), "s"(ub), "s"(wb), "v"(vx0), "v"(vx1), "v"(vu), "v"(vw0), "v"(vw1) : "memory");
}
template <int CTRL> DI float dpp_add(float v) {
  return v + __builtin_bit_cast(float, __builtin_amdgcn_update_dpp(0, __builtin_bit_cast(int, v), CTRL, 0xf, 0xf, true));
}
DI float wave_sum(float v) {
  v = dpp_add<0x111>(v); v = dpp_add<0x112>(v); v = dpp_add<0x114>(v); v = dpp_add<0x118>(v);
  v = v + __builtin_bit_cast(float, __builtin_amdgcn_update_dpp(0, __builtin_bit_cast(int, v), 0x142, 0xa, 0xf, false));
  v = v + __builtin_bit_cast(float, __builtin_amdgcn_update_dpp(0, __builtin_bit_cast(int, v), 0x143, 0xc, 0xf, false));
  return __builtin_bit_cast(float, __builtin_amdgcn_readlane(__builtin_bit_cast(int, v), 63));
}
DI u64 mk_gran(u64 epoch, float v) {
  unsigned vb = __builtin_bit_cast(unsigned, v);
  vb = (vb + 0x80u) >> 8;
  return (epoch << 24) | (u64)(vb & 0xffffffu);
}
DI void dma1(unsigned lds, const void* sb, unsigned v) {
  asm volatile("s_mov_b32 m0, %0\n\ts_nop 0\n\tglobal_load_lds_dwordx4 %2, %1" :: "s"(lds), "s"(sb), "v"(v) : "memory");
}
#define FD 3
constexpr int usz(int u) { return (u & 3) == 3 ? 2 : 4; }
constexpr int f_cnt(bool kindA, int u, int at, int st) {
  int n = 0; bool seen = false;
  for (int a = 0; a < FD; a++) { if (seen) n += usz(a); if (kindA && a == u) seen = true; }
  for (int v = 0; v <= at; v++) {
    const int steps = v < at ? usz(v) : st;
    for (int t = 0; t < steps; t++) {
      if (v + FD < 16) {
        const int np = usz(v + FD), ns = usz(v);
        for (int i = 0; i < np; i++)
          if (i * ns / np == t) { if (seen) n++; if (kindA && v + FD == u && i == np - 1) seen = true; }
      }
      if (v + 4 < 16) { if (seen) n++; if (!kindA && v + 4 == u && t == usz(v) - 1) seen = true; }
    }
  }
  return n;
}
constexpr int hv_t(int n) { return n < 32 ? 8 + n / 4 : 8 + (n - 32) / 3; }
constexpr int hv_m(int n) { return n < 32 ? n % 4 : 4 + (n - 32) % 3; }
template <int... I, class F> DI void static_for(std::integer_sequence<int, I...>, F f) { (f(std::integral_constant<int, I>{}), ...); }
DI void delay_us(int us) { const u64 t0 = __builtin_amdgcn_s_memrealtime(); while (__builtin_amdgcn_s_memrealtime() - t0 < (u64)(us * 100)) __builtin_amdgcn_s_sleep(1); __builtin_amdgcn_s_waitcnt(0xC07F); }
#define DLY 2
#define SB0() __builtin_amdgcn_sched_barrier(0)

__global__ __launch_bounds__(512, 2) NOPK void k_fused(Params p) {
  extern __shared__ __attribute__((aligned(16))) char smem[];
  LAS char* lds = (LAS char*)smem;
  STAMP_DECL
  STAMP(0);
  const int tid = threadIdx.x, l = tid & 63;
  const int w8 = __builtin_amdgcn_readfirstlane(tid >> 6);
  const int role = w8 >> 2, w = w8 & 3;
  const int bid = blockIdx.x, xcd = bid & 7, r = bid >> 3;
  const int j = 4 * (xcd >> 1) + (r >> 3);
  const int s = 8 * (xcd & 1) + (r & 7);
  const int hp2 = 8 * (xcd >> 1) + (r >> 2);
  const int ct = 4 * (xcd & 1) + (r & 3);
  const int odd = s & 1;
  const int i16 = l & 15, q = l >> 4;
  const int ta = i16 >> 2, tp = i16 & 3;
  const unsigned ldsb = (unsigned)(size_t)lds;
  const __amdgpu_buffer_rsrc_t rs = __builtin_amdgcn_make_buffer_rsrc((void*)p.ws, 0, (int)WS_END, 0x00020000);
  const v4u ones = {0x3F803F80u, 0x3F803F80u, 0x3F803F80u, 0x3F803F80u};

  const int rot = ROT ? ((2 * (r >> 3) + (r & 7)) & 7) : 0;
  u64 ep0;
  asm volatile("s_load_dwordx2 %0, %1, 0x0" : "=s"(ep0) : "s"(p.ws + WS_EPOCH) : "memory");
  unsigned plr[2], pli[2], pdt[2];
  v4u xr[2];
  unsigned cdum = 0;
  if (role == 0) {
    const char* rb = p.x + (size_t)(128 * j + 8 * s + 2 * w) * 1024;
    gld16<0>(xr[0], (unsigned)l * 16, rb); gld16<1024>(xr[1], (unsigned)l * 16, rb);
    const unsigned co = (unsigned)(32 * s + i16) * 2;
    gld_u16<0>(plr[0], co, p.lre); gld_u16<32>(plr[1], co, p.lre);
    gld_u16<0>(pli[0], co, p.lim); gld_u16<32>(pli[1], co, p.lim);
    gld_u16<0>(pdt[0], co, p.logdt); gld_u16<32>(pdt[1], co, p.logdt);
    {
      u64 pc;
      asm volatile("s_getpc_b64 %0" : "=s"(pc));
      const int ci = r * 4 + w;
      const char* cb = (const char*)(pc & ~255ull) + (ci < CODEPF ? ci : 0) * 1024;
      asm volatile("global_load_dword %0, %1, %2" : "=v"(cdum) : "v"((unsigned)l * 16), "s"(cb) : "memory");
    }
  }
  unsigned vx0, vx1, vu, vwA, vwB;
  {
    const int r8 = l >> 3, c8 = l & 7;
    const int tok0 = 8 * (r8 >> 2) + (r8 & 3);
    vx0 = (unsigned)(tok0 * 1024 + ((c8 ^ (r8 >> 1)) & 7) * 16);
    vx1 = (unsigned)((tok0 + 16) * 1024 + ((c8 ^ (4 + (r8 >> 1))) & 7) * 16);
    const int ch = 8 * w + r8, iu = 8 * (w & 1) + r8;
    vu = (unsigned)(ch * 1024 + ((c8 ^ (iu >> 1)) & 7) * 16);
    const int winoff = 448 * (s >> 1) + (odd ? 192 : 0);
    vwA = (unsigned)((16 * w + q) * 3584 + winoff + ((((i16 >> 1) ^ q) << 1) | (i16 & 1)) * 16);
    vwB = (unsigned)((16 * w + 8 + q) * 3584 + winoff + ((((i16 >> 1) ^ (q | 4)) << 1) | (i16 & 1)) * 16);
  }
  const char* xb = p.x + (size_t)(128 * j + 32 * (w >> 1) + 4 * (w & 1)) * 1024;
  const char* ub0 = p.bre + (size_t)(32 * s) * 1024;
  const char* ub1 = p.bim + (size_t)(32 * s) * 1024;
#define PIECE(N, I)                                                                                  \
  {                                                                                                  \
    constexpr int h_ = (N) / 3, n_ = (N) % 3;                                                        \
    const int kq_ = ((I) + rot) & 7;                                                                 \
    const unsigned d_ = ldsb + w * 3072 + ((I) & 3) * STG + h_ * 12288 + n_ * 1024;                  \
    if (n_ == 0) dma1(d_, xb + h_ * 65536 + kq_ * 128, vx0);                                         \
    if (n_ == 1) dma1(d_, xb + h_ * 65536 + kq_ * 128, vx1);                                         \
    if (n_ == 2) dma1(d_, (h_ ? ub1 : ub0) + kq_ * 128, vu);                                         \
  }
#define PALL(I) PIECE(0, I) PIECE(1, I) PIECE(2, I) PIECE(3, I) PIECE(4, I) PIECE(5, I)
#define W1P(C, n) dma1(ldsb + L_W1A + ((C) & 3) * 16384 + (16 * w + 4 * (n)) * 256,          \
                       p.w1 + (size_t)((((C) + rot) & 7) * 64 + ((n) & 1) * 4) * 3584, ((n) & 2) ? vwB : vwA)
  if (role == 1) {
    PALL(0)
    __builtin_amdgcn_s_barrier();
    PALL(1)
  } else {
    __builtin_amdgcn_s_barrier();
  }
  asm volatile("s_waitcnt lgkmcnt(0)" : "+s"(ep0) :: "memory");
  if (DLY == 1) delay_us(3);
  const u64 epoch = ep0 + 1ull;
  const unsigned tlo = (unsigned)(epoch & 0xffu), thi = (unsigned)(epoch >> 8);

  const unsigned sw0 = (unsigned)((i16 & 7) * 128 + (((0 + q) ^ (i16 >> 1)) & 7) * 16);
  const unsigned sw1 = (unsigned)((i16 & 7) * 128 + (((4 + q) ^ (i16 >> 1)) & 7) * 16);
  const unsigned xo = (unsigned)(2 * w * 3072 + (i16 >> 3) * 1024);
  const unsigned tro = (unsigned)((8 * q + ta) * 256 + 8 * tp);
  const int trkey = ta | ((q & 1) << 2);
  const unsigned zb = (unsigned)(2 * j + (w >> 1)) * HP_BYTES + (unsigned)l * 16u;
  const char* ab = p.ws + WS_Z + (size_t)hp2 * HP_BYTES;
  const unsigned avoff = (unsigned)l * 16;
  const int rot2 = ROT2 == 1 ? (hp2 & 3) : ROT2 == 2 ? (ct & 3) : 0;

  if (role == 0) {
    if (APRIO) __builtin_amdgcn_s_setprio(APRIO);
    asm volatile("s_waitcnt vmcnt(%8)" : "+v"(plr[0]), "+v"(plr[1]), "+v"(pli[0]), "+v"(pli[1]), "+v"(pdt[0]), "+v"(pdt[1]), "+v"(xr[0]), "+v"(xr[1]) : "i"(CODEPF ? 1 : 0) : "memory");
#pragma unroll
    for (int n = 0; n < 2; n++) {
      float v[8];
#pragma unroll
      for (int e = 0; e < 4; e++) { v[2 * e] = bf2f(xr[n][e] & 0xffffu); v[2 * e + 1] = __builtin_bit_cast(float, xr[n][e] & 0xffff0000u); }
      const float mean = wave_sum(((v[0] + v[1]) + (v[2] + v[3])) + ((v[4] + v[5]) + (v[6] + v[7]))) * (1.f / 512.f);
      float sq = 0.f;
#pragma unroll
      for (int e = 0; e < 8; e++) { const float d = v[e] - mean; sq = fmaf(d, d, sq); }
      const float rstd = rsqrtf(wave_sum(sq) * (1.f / 512.f) + 1e-5f);
      if (l < 2)
        __hip_atomic_store((GAS u64*)(p.ws + WS_SG) + (size_t)(128 * j + 8 * s + 2 * w + n) * 2 + l, mk_gran(epoch, l ? rstd : mean), RLX_AGENT);
    }
    cpx a1[2], cc[2], a8[2], pw[2], a128[2];
#pragma unroll
    for (int nb = 0; nb < 2; nb++) {
      const float lr = bf2f(plr[nb]), li = bf2f(pli[nb]);
      const float dtf = expf(bf2f(pdt[nb]));
      const float dt = bf2f(pk_bf16(dtf, 0.f) & 0xffffu);
      const float e = expf(dt * lr);
      float sn, cs;
      sincosf(dt * li, &sn, &cs);
      cpx a = {e * cs, e * sn};
      a1[nb] = a;
      const float nr = a.r - 1.f, ni = a.i, den = lr * lr + li * li;
      cc[nb] = {2.f * (nr * lr + ni * li) / den, 2.f * (ni * lr - nr * li) / den};
      cpx t = cmul(a, a); t = cmul(t, t); t = cmul(t, t);
      a8[nb] = t;
      cpx a16 = cmul(t, t), a32 = cmul(a16, a16), a64 = cmul(a32, a32);
      a128[nb] = cmul(a64, a64);
      cpx one = {1.f, 0.f};
      cpx pq = cmul(csel(q & 1, t, one), csel(q & 2, a16, one));
      cpx pwv = cmul(csel(w & 1, a32, one), csel(w & 2, a64, one));
      pw[nb] = cmul(pq, pwv);
    }
#pragma unroll
    for (int nb = 0; nb < 2; nb++)
      asm volatile("" : "+v"(a1[nb].r), "+v"(a1[nb].i), "+v"(cc[nb].r), "+v"(cc[nb].i), "+v"(a8[nb].r), "+v"(a8[nb].i), "+v"(pw[nb].r),
                   "+v"(pw[nb].i), "+v"(a128[nb].r), "+v"(a128[nb].i));
    STAMP(1);
    v4u cf[8][4];
    const unsigned cvoff = (unsigned)((64 * ct + (l >> 2)) * 1024 + (l & 3) * 16);
#define CLOAD(K0)                                                                         \
  _Pragma("unroll") for (int k = K0; k < K0 + 4; k++) {                                   \
    const int kr = (k + 2 * rot2) & 7;                                                    \
    const int sidx = 4 * w + 2 * (kr >> 2) + (kr & 1);                                    \
    const char* cb = (((kr >> 1) & 1) ? p.cim : p.cre) + sidx * 64;                       \
    _Pragma("unroll") for (int n = 0; n < 4; n++) gld16<0>(cf[k][n], cvoff, cb + n * 16384); \
    if (CPACE) __builtin_amdgcn_s_sleep(CPACE);                                           \
  }
#define CWAIT(K0, N)                                                                      \
  asm volatile("s_waitcnt vmcnt(%16)" : "+v"(cf[K0][0]), "+v"(cf[K0][1]), "+v"(cf[K0][2]), "+v"(cf[K0][3]), "+v"(cf[K0 + 1][0]),   \
               "+v"(cf[K0 + 1][1]), "+v"(cf[K0 + 1][2]), "+v"(cf[K0 + 1][3]), "+v"(cf[K0 + 2][0]), "+v"(cf[K0 + 2][1]),          \
               "+v"(cf[K0 + 2][2]), "+v"(cf[K0 + 2][3]), "+v"(cf[K0 + 3][0]), "+v"(cf[K0 + 3][1]), "+v"(cf[K0 + 3][2]),          \
               "+v"(cf[K0 + 3][3]) : "i"(N) : "memory");
#define CXPOSE(K0)                                                                        \
  _Pragma("unroll") for (int k = K0; k < K0 + 4; k++) {                                   \
    _Pragma("unroll") for (int n = 0; n < 4; n++) *(LAS v4u*)(lp(cwa) + n * 1024) = cf[k][n]; \
    _Pragma("unroll") for (int n = 0; n < 4; n++) cf[k][n] = lds_rd16(lp(cra) + n * 1024);  \
  }
    v2u sg;
    const unsigned sgoff = (unsigned)l * 8;
    const char* sgb = p.ws + WS_SG + (size_t)(128 * j + 32 * w) * 16;
    f32x4 au[2][2][2], asu;
#pragma unroll
    for (int b = 0; b < 2; b++)
#pragma unroll
      for (int nb = 0; nb < 2; nb++) { au[b][nb][0] = f32x4{0, 0, 0, 0}; au[b][nb][1] = f32x4{0, 0, 0, 0}; }
    asu = f32x4{0, 0, 0, 0};
    {
      const unsigned uo = (unsigned)((i16 >> 3) * 3072 + 2048);
      const int gs = (w & 1) * 2 + (w >> 1);
      unsigned cur = 0;
      v4u xd[2][2], wd[2][4], sd[2];
#pragma unroll
      for (int t = 0; t < 2; t++) xd[t][0] = xd[t][1] = wd[t][0] = wd[t][1] = wd[t][2] = wd[t][3] = sd[t] = v4u{0, 0, 0, 0};
#define A_STEP(RD, T, PX, PU, PS)                                                                     \
  {                                                                                                   \
    _Pragma("unroll") for (int g = 0; g < 4; g++) {                                                   \
      au[0][g & 1][g >> 1] = mfma16(xd[T][0], wd[T][g], au[0][g & 1][g >> 1]);                        \
      au[1][g & 1][g >> 1] = mfma16(xd[T][1], wd[T][g], au[1][g & 1][g >> 1]);                        \
      if (RD) wd[T][g] = lds_rd16(PU + g * 6144);                                                     \
      SB0();                                                                                          \
    }                                                                                                 \
    asu = mfma16(ones, sd[T], asu);                                                                   \
    if (RD) { xd[T][0] = lds_rd16(PX); xd[T][1] = lds_rd16(PX + 3072); sd[T] = lds_rd16(PS); }        \
    SB0();                                                                                            \
  }
#pragma unroll 1
      for (int it = 0; it < 8; it++) {
        const unsigned b0 = ldsb + cur;
        LAS char* px0 = lp(opaque(b0 + xo + sw0)); LAS char* px1 = lp(opaque(b0 + xo + sw1));
        LAS char* pu0 = lp(opaque(b0 + uo + sw0)); LAS char* pu1 = lp(opaque(b0 + uo + sw1));
        LAS char* ps0 = lp(opaque(b0 + uo + sw0 + gs * 6144)); LAS char* ps1 = lp(opaque(b0 + uo + sw1 + gs * 6144));
        cur = cur >= 3 * STG ? 0 : cur + STG;
        if (ALGKM) wait_lgkm();
        asm volatile("" ::: "memory");
        __builtin_amdgcn_s_barrier();
        asm volatile("" ::: "memory");
        SB0();
        A_STEP(true, 0, px0, pu0, ps0)
        A_STEP(true, 1, px1, pu1, ps1)
      }
      gld8_sc1(sg, sgoff, sgb);
      A_STEP(false, 0, lds, lds, lds)
      A_STEP(false, 1, lds, lds, lds)
#undef A_STEP
    }
    STAMP(2);
    {
      asm volatile("s_waitcnt vmcnt(0)" : "+v"(sg), "+v"(cdum) :: "memory");
      if (tid < 16) ((LAS unsigned*)(lds + L_CNT))[tid] = 0u;
      unsigned spins = 0;
      while (!wall(sg.y == thi && (sg.x >> 24) == tlo)) {
        if (++spins > (1u << 22)) break;
        __builtin_amdgcn_s_sleep(1);
        gld8_sc1(sg, sgoff, sgb);
        asm volatile("s_waitcnt vmcnt(0)" : "+v"(sg), "+v"(cdum) :: "memory");
      }
      const int tk = l >> 1;
      ((LAS unsigned*)(lds + L_STAT))[(w * 32 + ((tk >> 2) & 1) * 16 + ((tk & 3) | ((tk >> 3) << 2))) * 2 + (l & 1)] = sg.x << 8;
      if (q == 0) ((LAS float*)(lds + L_SU))[w * 16 + i16] = asu[0];
    }
    bar();
    if (DLY == 4) delay_us(3);
    STAMP(3);
    cpx u[2][8];
    {
      f32x2 stt[2][4];
#pragma unroll
      for (int b = 0; b < 2; b++) {
        f32x4 s0 = *(LAS f32x4*)(lds + L_STAT + (w * 32 + b * 16 + 4 * q) * 8);
        f32x4 s1 = *(LAS f32x4*)(lds + L_STAT + (w * 32 + b * 16 + 4 * q) * 8 + 16);
        stt[b][0] = f32x2{s0[0], s0[1]}; stt[b][1] = f32x2{s0[2], s0[3]};
        stt[b][2] = f32x2{s1[0], s1[1]}; stt[b][3] = f32x2{s1[2], s1[3]};
      }
#pragma unroll
      for (int nb = 0; nb < 2; nb++) {
        const float sre = ((LAS float*)(lds + L_SU))[(nb * 2 + 0) * 16 + i16];
        const float sim = ((LAS float*)(lds + L_SU))[(nb * 2 + 1) * 16 + i16];
        cpx h = {0.f, 0.f};
#pragma unroll
        for (int k = 0; k < 8; k++) {
          const int b = k >> 2, rg = k & 3;
          const float m = stt[b][rg][0], rr = stt[b][rg][1];
          u[nb][k].r = fmaf(-m, sre, au[b][nb][0][rg]) * rr;
          u[nb][k].i = fmaf(-m, sim, au[b][nb][1][rg]) * rr;
          h = cfma(a1[nb], h, u[nb][k]);
        }
        *(LAS f32x2*)(lds + L_E + ((nb * 16 + i16) * 16 + 4 * w + q) * 8) = f32x2{h.r, h.i};
      }
    }
    cnt_arrive(lds, C_E);
    cnt_wait(lds, C_E, 4);
    cpx cl[2];
    {
      cpx c[2] = {{0.f, 0.f}, {0.f, 0.f}};
      LAS f32x4* e4a = (LAS f32x4*)(lds + L_E + i16 * 128);
      LAS f32x4* e4b = (LAS f32x4*)(lds + L_E + (16 + i16) * 128);
#pragma unroll 1
      for (int k = 0; k < 2 * w; k++) {
        f32x4 ea = e4a[k], eb = e4b[k];
        c[0] = cfma(a8[0], c[0], cpx{ea[0], ea[1]}); c[1] = cfma(a8[1], c[1], cpx{eb[0], eb[1]});
        c[0] = cfma(a8[0], c[0], cpx{ea[2], ea[3]}); c[1] = cfma(a8[1], c[1], cpx{eb[2], eb[3]});
      }
      cpx tot[2];
#pragma unroll
      for (int nb = 0; nb < 2; nb++) {
        LAS f32x4* e4 = nb ? e4b : e4a;
        f32x4 e0 = e4[2 * w], e1 = e4[2 * w + 1];
        cpx c1 = cfma(a8[nb], c[nb], cpx{e0[0], e0[1]});
        cpx c2 = cfma(a8[nb], c1, cpx{e0[2], e0[3]});
        cpx c3 = cfma(a8[nb], c2, cpx{e1[0], e1[1]});
        tot[nb] = cfma(a8[nb], c3, cpx{e1[2], e1[3]});
        cl[nb] = csel(q & 2, csel(q & 1, c3, c2), csel(q & 1, c1, c[nb]));
      }
      if (w == 3 && j < 15) {
        const cpx t = (q & 2) ? tot[1] : tot[0];
        const float v = (q & 1) ? t.i : t.r;
        const u64 g = mk_gran(epoch, v);
        GAS u64* gp = (GAS u64*)(p.ws + WS_GRAN + (size_t)(j * 16 + s) * 512) + (((q >> 1) * 16 + i16) * 2 + (q & 1));
        __hip_atomic_store(gp, g, RLX_AGENT);
      }
    }
    STAMP(4);
    const unsigned cwa = opaque(ldsb + L_CST + w * 4096 + (l >> 2) * 64 + (((l & 3) ^ ((-(l >> 4)) & 3)) * 16));
    const unsigned cra = opaque(ldsb + L_CST + w * 4096 + i16 * 64 + ((q ^ ((-(i16 >> 2)) & 3)) * 16));
    unsigned wdum = 0;
    CLOAD(0)
    {
      const unsigned wv2 = (unsigned)((l >> 5) * 1024 + (l & 31) * 16);
#pragma unroll 1
      for (int k = 0; k < NWARM; k++)
        asm volatile("global_load_dword %0, %1, %2" : "+v"(wdum) : "v"(wv2), "s"(p.w2 + (size_t)((k * 32 + r) * 4 + w) * 2048 + (xcd & 1) * 512) : "memory");
    }
    CWAIT(0, NWARM)
    CXPOSE(0)
    if (w < 2) {
      const char* gb = p.ws + WS_GRAN + (size_t)s * 512 + w * 256;
      unsigned spins = 0;
      if (j > 0) {
        GAS u64* pp = (GAS u64*)(gb + (size_t)(l < j ? l : 0) * 8192 + 248);
        while (!wall((__hip_atomic_load(pp, RLX_AGENT) >> 24) == (epoch & 0xffffffffffull))) {
          if (++spins > (1u << 20)) break;
          __builtin_amdgcn_s_sleep(2);
        }
      }
      v4u gr[4];
      for (;;) {
#pragma unroll
        for (int m = 0; m < 4; m++) {
          const int pn = (4 * m + q) < j ? 4 * m + q : 0;
          gld16_sc1<0>(gr[m], (unsigned)(pn * 8192 + i16 * 16), gb);
        }
        wait_vm4<0>(gr[0], gr[1], gr[2], gr[3]);
        bool ok = true;
#pragma unroll
        for (int m = 0; m < 4; m++) {
          const bool need = (4 * m + q) < j;
          const bool good = (gr[m].y == thi) && ((gr[m].x >> 24) == tlo) && (gr[m].w == thi) && ((gr[m].z >> 24) == tlo);
          ok = ok && (good || !need);
        }
        if (wall(ok)) break;
        if (++spins > (1u << 20)) break;
        __builtin_amdgcn_s_sleep(2);
      }
      const cpx b128 = csel(w != 0, a128[1], a128[0]);
      const cpx a256 = cmul(b128, b128);
      const cpx a512 = cmul(a256, a256);
      cpx acc = {0.f, 0.f};
#pragma unroll
      for (int m = 0; m < 4; m++) {
        const bool need = (4 * m + q) < j;
        cpx e = {__builtin_bit_cast(float, gr[m].x << 8), __builtin_bit_cast(float, gr[m].z << 8)};
        acc = csel(need, cfma(a512, acc, e), acc);
      }
      const int dd = (j - 1 - q) & 3;
      cpx one = {1.f, 0.f};
      cpx fz = cmul(csel(dd & 1, b128, one), csel(dd & 2, a256, one));
      acc = cmul(acc, fz);
      *(LAS f32x2*)(lds + L_HIN + ((w * 16 + i16) * 4 + q) * 8) = f32x2{acc.r, acc.i};
      cnt_arrive(lds, C_HIN);
    }
    cnt_wait(lds, C_HIN, 2);
    STAMP(5);
    {
      LAS char* ht = lds + L_HT + w * 2048;
      unsigned hpk[2][2][2][2];
#pragma unroll
      for (int nb = 0; nb < 2; nb++) {
        const f32x4 h01 = *(LAS f32x4*)(lds + L_HIN + (nb * 16 + i16) * 32);
        const f32x4 h23 = *(LAS f32x4*)(lds + L_HIN + (nb * 16 + i16) * 32 + 16);
        const cpx acc = {(h01[0] + h01[2]) + (h23[0] + h23[2]), (h01[1] + h01[3]) + (h23[1] + h23[3])};
        cpx h = cfma(pw[nb], acc, cl[nb]);
        float orr[8], oi[8];
#pragma unroll
        for (int k = 0; k < 8; k++) {
          h = cfma(a1[nb], h, u[nb][k]);
          orr[k] = cc[nb].r * h.r - cc[nb].i * h.i;
          oi[k] = -(cc[nb].r * h.i + cc[nb].i * h.r);
        }
#pragma unroll
        for (int b = 0; b < 2; b++) {
          hpk[nb][0][b][0] = pk_bf16(orr[4 * b], orr[4 * b + 1]); hpk[nb][0][b][1] = pk_bf16(orr[4 * b + 2], orr[4 * b + 3]);
          hpk[nb][1][b][0] = pk_bf16(oi[4 * b], oi[4 * b + 1]);   hpk[nb][1][b][1] = pk_bf16(oi[4 * b + 2], oi[4 * b + 3]);
        }
      }
      const unsigned zy = zb + Y_OFF + (unsigned)(4 * (s >> 1) + (s & 1)) * 4096u + (unsigned)(2 * (w & 1)) * 1024u;
#pragma unroll
      for (int b = 0; b < 2; b++) {
#pragma unroll
        for (int nb = 0; nb < 2; nb++)
#pragma unroll
          for (int part = 0; part < 2; part++)
            *(LAS v2u*)(ht + (part * 32 + nb * 16 + i16) * 32 + q * 8) = v2u{hpk[nb][part][b][0], hpk[nb][part][b][1]};
#pragma unroll
        for (int part = 0; part < 2; part++) {
          LAS char* rp = ht + (part * 32 + 8 * q + ta) * 32 + tp * 8;
          v4u v = lds_tr(rp, rp + 4 * 32);
          st16(rs, (unsigned)WS_Z + zy + (unsigned)part * 8192u + b * 1024u, v);
        }
      }
    }
    STAMP(6);
    asm volatile("s_waitcnt vmcnt(0)" : "+v"(wdum) :: "memory");
    if (cnt_arrive(lds, C_HS) == 3 && l == 0)
      __hip_atomic_store((GAS u64*)(p.ws + WS_FLAG) + (j * 16 + s), epoch, RLX_AGENT);
    STAMP(7);
    CLOAD(4)
    CWAIT(4, 0)
    CXPOSE(4)
    cnt_arrive(lds, C_CX);
    STAMP(8);
    poll_flags(p.ws + WS_FLAG + (size_t)(j * 16 + 4 * w) * 8, l, epoch);
    STAMP(9);
    f32x4 acc[4][4];
#pragma unroll
    for (int a = 0; a < 4; a++)
#pragma unroll
      for (int b = 0; b < 4; b++) acc[a][b] = f32x4{0, 0, 0, 0};
    v4u af[3][4];
    const char* ay = ab + Y_OFF + (size_t)(8 * w) * 4096;
#define YL(K, i) gld16_sc1<(i) * 1024>(af[(K) % 3][i], avoff, ay + ((((K) + 2 * rot2) & 7) * 4096))
#define YST(K, tb)                                                                                  \
  {                                                                                                 \
    _Pragma("unroll") for (int nb = 0; nb < 4; nb++) acc[nb][tb] = mfma16(cf[K][nb], af[(K) % 3][tb], acc[nb][tb]); \
    SB0();                                                                                          \
    if ((K) + 2 < 8) YL((K) + 2, tb);                                                               \
    SB0();                                                                                          \
  }
#define YS(K)                                                                                       \
  {                                                                                                 \
    wait_vm4<((K) < 7 ? 4 : 0)>(af[(K) % 3][0], af[(K) % 3][1], af[(K) % 3][2], af[(K) % 3][3]);    \
    YST(K, 0) YST(K, 1) YST(K, 2) YST(K, 3)                                                         \
  }
    YL(0, 0); YL(0, 1); YL(0, 2); YL(0, 3); YL(1, 0); YL(1, 1); YL(1, 2); YL(1, 3);
    YS(0) YS(1) YS(2) YS(3) YS(4) YS(5) YS(6) YS(7)
#undef YL
#undef YS
#undef YST
    STAMP(10);
    cnt_wait(lds, C_CX, 4);
    cnt_wait(lds, C_B2, 4);
    {
      LAS char* yb = lds + ((w & 1) ? L_YB1 : L_YB0);
      if (w < 2) cnt_wait(lds, C_Y1 + (w & 1), 1);
#pragma unroll
      for (int tb = 0; tb < 4; tb++) {
        const int tk = 32 * (tb >> 1) + 8 * (i16 >> 2) + 4 * (tb & 1) + (i16 & 3);
#pragma unroll
        for (int nb = 0; nb < 4; nb++) {
          LAS f32x4* d = (LAS f32x4*)(yb + tk * 256 + (((4 * nb + q) ^ i16) & 15) * 16);
          if (w < 2) *d = *d + acc[nb][tb]; else *d = acc[nb][tb];
        }
      }
      if (w >= 2) cnt_arrive(lds, C_Y1 + (w & 1));
    }
    STAMP(11);
  } else {
    v4u xf[2][16];
    f32x4 ah[7][2];
#pragma unroll
    for (int mb = 0; mb < 7; mb++) { ah[mb][0] = f32x4{0, 0, 0, 0}; ah[mb][1] = f32x4{0, 0, 0, 0}; }
    unsigned pt[7];
#pragma unroll
    for (int mb = 0; mb < 7; mb++) pt[mb] = opaque(ldsb + L_W1A + tro + (unsigned)(((mb + odd) ^ trkey) * 32));
    const int m1 = w < 3 ? w + 4 : 6;
    const unsigned hq0 = opaque(ldsb + L_W1A + tro + (unsigned)(((w + odd) ^ trkey) * 32));
    const unsigned hq1 = opaque(ldsb + L_W1A + tro + (unsigned)(((m1 + odd) ^ trkey) * 32));
    f32x4 sh0 = {0, 0, 0, 0}, sh1 = {0, 0, 0, 0};
    {
      LAS char* px0 = lp(opaque(ldsb + xo + sw0)); LAS char* px1 = lp(opaque(ldsb + xo + sw1));
#define L1IT(I, NW)                                                                                   \
  {                                                                                                   \
    wait_vm<NW>();                                                                                    \
    __builtin_amdgcn_s_barrier();                                                                     \
    asm volatile("" ::: "memory");                                                                    \
    xf[0][2 * (I)] = lds_rd16(px0 + ((I) & 3) * STG); xf[1][2 * (I)] = lds_rd16(px0 + ((I) & 3) * STG + 3072); \
    xf[0][2 * (I) + 1] = lds_rd16(px1 + ((I) & 3) * STG); xf[1][2 * (I) + 1] = lds_rd16(px1 + ((I) & 3) * STG + 3072); \
    SB0();                                                                                            \
    if ((I) < 3 || (I) == 7) {                                                                        \
      if ((I) < 6) { PALL((I) + 2) }                                                                  \
      W1P(I, 0); W1P(I, 1); W1P(I, 2); W1P(I, 3);                                                     \
    } else {                                                                                          \
      constexpr int c_ = (I) - 3;                                                                     \
      v4u wa[7], wb_[7], h0[2], h1[2];                                                                \
      _Pragma("unroll") for (int mb = 0; mb < 7; mb++) wa[mb] = lds_tr(lp(pt[mb]) + c_ * 16384, lp(pt[mb]) + c_ * 16384 + 1024); \
      SB0();                                                                                          \
      if ((I) < 6) { PIECE(0, (I) + 2) PIECE(1, (I) + 2) PIECE(2, (I) + 2) }                          \
      SB0();                                                                                          \
      _Pragma("unroll") for (int mb = 0; mb < 7; mb++) {                                              \
        ah[mb][0] = mfma16(wa[mb], xf[0][2 * c_], ah[mb][0]);                                         \
        ah[mb][1] = mfma16(wa[mb], xf[1][2 * c_], ah[mb][1]);                                         \
        wb_[mb] = lds_tr(lp(pt[mb]) + c_ * 16384 + 8192, lp(pt[mb]) + c_ * 16384 + 9216);             \
        if ((I) < 6) { if (mb == 1) PIECE(3, (I) + 2) if (mb == 3) PIECE(4, (I) + 2) if (mb == 5) PIECE(5, (I) + 2) } \
        SB0();                                                                                        \
      }                                                                                               \
      _Pragma("unroll") for (int mb = 0; mb < 7; mb++) {                                              \
        ah[mb][0] = mfma16(wb_[mb], xf[0][2 * c_ + 1], ah[mb][0]);                                    \
        ah[mb][1] = mfma16(wb_[mb], xf[1][2 * c_ + 1], ah[mb][1]);                                    \
        if (mb == 0) { h0[0] = lds_tr(lp(hq0) + c_ * 16384, lp(hq0) + c_ * 16384 + 1024); h1[0] = lds_tr(lp(hq1) + c_ * 16384, lp(hq1) + c_ * 16384 + 1024); } \
        if (mb == 1) { h0[1] = lds_tr(lp(hq0) + c_ * 16384 + 8192, lp(hq0) + c_ * 16384 + 9216); h1[1] = lds_tr(lp(hq1) + c_ * 16384 + 8192, lp(hq1) + c_ * 16384 + 9216); } \
        if (mb == 0) W1P(I, 0); if (mb == 2) W1P(I, 1); if (mb == 4) W1P(I, 2); if (mb == 6) W1P(I, 3); \
        SB0();                                                                                        \
      }                                                                                               \
      sh0 = mfma16(h0[0], ones, sh0); sh1 = mfma16(h1[0], ones, sh1);                                 \
      sh0 = mfma16(h0[1], ones, sh0); sh1 = mfma16(h1[1], ones, sh1);                                 \
    }                                                                                                 \
    wait_lgkm();                                                                                      \
  }
      L1IT(0, 6) L1IT(1, 10) L1IT(2, 14) STAMP(15); L1IT(3, 14) L1IT(4, 14) L1IT(5, 14) L1IT(6, 14) L1IT(7, 8)
#undef L1IT
    }
    STAMP(2);
    wait_vm<8>();
    bar();
    if (DLY == 4) delay_us(3);
    STAMP(3);
    const unsigned ring = ldsb + L_RING + w * RING;
    unsigned fv;
    {
      const int row = l >> 3, c16p = l & 7;
      fv = (unsigned)(row * 1024 + ct * 128 + ((((c16p >> 1) ^ ((row >> 1) & 3)) << 1) | (c16p & 1)) * 16);
    }
    const char* w2b = p.w2 + (size_t)(448 * w) * 1024;
#define WP(N, n) dma1(ring + ((N) & 1) * 14336 + (n) * 1024, w2b + (size_t)(((N) + rot2) & 3) * (112 * 1024) + (n) * 8192, fv)
    {
      v4u wd[7];
      static_for(std::make_integer_sequence<int, 7>{}, [&](auto NC) __attribute__((always_inline)) NOPK {
        constexpr int n = decltype(NC)::value, T2 = hv_t(n), m2 = hv_m(n);
        wd[n] = lds_tr(lp(pt[m2]) + (T2 & 7) * 8192, lp(pt[m2]) + (T2 & 7) * 8192 + 1024);
      });
      const unsigned zf = zb + (unsigned)s * 14336u;
      float nm[2], r2[2];
      v4u hs0, hs1;
      unsigned pkd[7][2][2];
      auto epi = [&](auto MB, auto BB) __attribute__((always_inline)) NOPK {
        constexpr int mb = decltype(MB)::value, b = decltype(BB)::value;
        const f32x4 sh = *(LAS f32x4*)(lds + L_SH + (mb * 16 + 4 * q) * 4);
        float y[4];
#pragma unroll
        for (int e = 0; e < 4; e++) {
          const float d = fmaf(sh[e], nm[b], ah[mb][b][e]);
          y[e] = d * __builtin_amdgcn_rcpf(1.f + __builtin_amdgcn_exp2f(d * r2[b]));
        }
        pkd[mb][b][0] = pk_bf16(y[0], y[1]); pkd[mb][b][1] = pk_bf16(y[2], y[3]);
        if constexpr ((mb & 1) == 1) {
          v4u v = {pkd[mb - 1][b][0], pkd[mb - 1][b][1], pkd[mb][b][0], pkd[mb][b][1]};
          st16(rs, (unsigned)WS_Z + zf + (unsigned)(mb >> 1) * 4096u + (unsigned)(2 * (w & 1) + b) * 1024u, v);
        }
      };
      static_for(std::make_integer_sequence<int, 56>{}, [&](auto NC) __attribute__((always_inline)) NOPK {
        constexpr int n = decltype(NC)::value, T = hv_t(n), mb = hv_m(n), sl = n % 7;
        if constexpr (n == 9) {
          wait_vm<0>();
          cnt_arrive(lds, C_W);
          cnt_wait(lds, C_W, 4);
        }
        if constexpr (n < 32 && (n & 3) == 0) {
          hs0 = lds_tr(lp(hq0) + (n / 4) * 8192, lp(hq0) + (n / 4) * 8192 + 1024);
          hs1 = lds_tr(lp(hq1) + (n / 4) * 8192, lp(hq1) + (n / 4) * 8192 + 1024);
        }
        if constexpr (n == 32) {
          if (i16 == 0) {
            *(LAS f32x4*)(lds + L_SH + (w * 16 + 4 * q) * 4) = sh0;
            if (w < 3) *(LAS f32x4*)(lds + L_SH + (m1 * 16 + 4 * q) * 4) = sh1;
          }
          cnt_arrive(lds, C_SH);
          cnt_wait(lds, C_SH, 4);
          const f32x2 s0 = *(LAS f32x2*)(lds + L_STAT + (w * 32 + i16) * 8), s1 = *(LAS f32x2*)(lds + L_STAT + (w * 32 + 16 + i16) * 8);
          nm[0] = -s0[0]; nm[1] = -s1[0]; r2[0] = s0[1] * -1.44269504088896f; r2[1] = s1[1] * -1.44269504088896f;
        }
        ah[mb][0] = mfma16(wd[sl], xf[0][T], ah[mb][0]);
        ah[mb][1] = mfma16(wd[sl], xf[1][T], ah[mb][1]);
        if constexpr (n < 32 && (n & 3) == 3) { sh0 = mfma16(hs0, ones, sh0); sh1 = mfma16(hs1, ones, sh1); }
        if constexpr (n + 7 < 56) {
          constexpr int T2 = hv_t(n + 7), m2 = hv_m(n + 7);
          wd[sl] = lds_tr(lp(pt[m2]) + (T2 & 7) * 8192, lp(pt[m2]) + (T2 & 7) * 8192 + 1024);
        }
        if constexpr (n >= 32 && (n - 32) % 3 == 1) {
          constexpr int u = (n - 32) / 3;
          epi(std::integral_constant<int, (u >> 2) * 2 + (u & 1)>{}, std::integral_constant<int, (u >> 1) & 1>{});
        }
        SB0();
      });
      STAMP(1);
      cnt_arrive(lds, C_B2);
      epi(std::integral_constant<int, 4>{}, std::integral_constant<int, 0>{});
      epi(std::integral_constant<int, 5>{}, std::integral_constant<int, 0>{});
      epi(std::integral_constant<int, 4>{}, std::integral_constant<int, 1>{});
      epi(std::integral_constant<int, 5>{}, std::integral_constant<int, 1>{});
      epi(std::integral_constant<int, 6>{}, std::integral_constant<int, 0>{});
      epi(std::integral_constant<int, 6>{}, std::integral_constant<int, 1>{});
      v4u v = {pkd[6][0][0], pkd[6][0][1], pkd[6][1][0], pkd[6][1][1]};
      st16(rs, (unsigned)WS_Z + zf + 12288u + (unsigned)(w & 1) * 1024u, v);
    }
    STAMP(4);
    if (DLY == 3) delay_us(3);
    wait_vm<0>();
    if (cnt_arrive(lds, C_FS) == 3 && l == 0)
      __hip_atomic_store((GAS u64*)(p.ws + WS_FLAG) + (256 + j * 16 + s), epoch, RLX_AGENT);
    STAMP(5);
    if (DLY == 2) delay_us(3);
    cnt_wait(lds, C_B2, 4);
#pragma unroll
    for (int n = 0; n < 14; n++) WP(0, n);
    poll_flags(p.ws + WS_FLAG + (size_t)(256 + j * 16 + 4 * w) * 8, l, epoch);
    STAMP(6);
    f32x4 acc[4][4];
#pragma unroll
    for (int a = 0; a < 4; a++)
#pragma unroll
      for (int b = 0; b < 4; b++) acc[a][b] = f32x4{0, 0, 0, 0};
    v4u af[4][4];
    const char* afb = ab + (size_t)(4 * w) * 14336;
    unsigned ftr[4];
#pragma unroll
    for (int nb = 0; nb < 4; nb++) {
      const int row = 4 * q + ta;
      ftr[nb] = opaque(ring + (unsigned)(row * 128 + ((nb ^ ((row >> 1) & 3)) * 32) + tp * 8));
    }
#define ALD(U, i) gld16_sc1<(i) * 1024>(af[(U) & 3][i], avoff, afb + (size_t)((((U) >> 2) + rot2) & 3) * 14336 + ((U) & 3) * 4096)
#define ALDS(U, V, t)                             \
  {                                                                                                 \
    if ((V) < 16) {                                                                                 \
      if (0 * usz(U) / usz(V) == (t)) ALD(V, 0);                                                    \
      if (1 * usz(U) / usz(V) == (t)) ALD(V, 1);                                                    \
      if (usz(V) == 4 && 2 * usz(U) / usz(V) == (t)) ALD(V, 2);                                     \
      if (usz(V) == 4 && 3 * usz(U) / usz(V) == (t)) ALD(V, 3);                                     \
    }                                                                                               \
  }
#define WRD(U)                                           \
  {                                                                                                 \
    _Pragma("unroll") for (int nb = 0; nb < 4; nb++) {                                              \
      LAS char* b_ = lp(ftr[nb]) + (((U) >> 2) & 1) * 14336 + ((U) & 3) * 4096;                     \
      if (((U) & 3) == 3) { v2u t_ = lds_tr1(b_); wf[(U) & 1][nb].x = t_.x; wf[(U) & 1][nb].y = t_.y; } \
      else wf[(U) & 1][nb] = lds_tr(b_, b_ + 2048);                                                 \
    }                                                                                               \
  }
#define FSTEP(U, t)                                                                                 \
  {                                                                                                 \
    if (((U) & 3) < 3) {                                                                            \
      _Pragma("unroll") for (int nb = 0; nb < 4; nb++)                                              \
          acc[nb][t] = mfma16(wf[(U) & 1][nb], af[(U) & 3][t], acc[nb][t]);                         \
    } else {                                                                                        \
      _Pragma("unroll") for (int nb = 0; nb < 4; nb++) {                                            \
        const v2u wh_ = {wf[(U) & 1][nb].x, wf[(U) & 1][nb].y};                                     \
        acc[nb][(2 * (t)) & 3] = mfma16h(wh_, v2u{af[3][t].x, af[3][t].y}, acc[nb][(2 * (t)) & 3]); \
        acc[nb][(2 * (t) + 1) & 3] = mfma16h(wh_, v2u{af[3][t].z, af[3][t].w}, acc[nb][(2 * (t) + 1) & 3]); \
      }                                                                                             \
    }                                                                                               \
    SB0();                                                                                          \
    ALDS(U, (U) + FD, t)                                                                            \
    if ((U) < 12) WP(((U) >> 2) + 1, 4 * ((U) & 3) + (t));                                          \
    if ((t) == usz(U) - 2 && (U) < 15) {                                                            \
      if ((U) + 1 >= 4) wait_vm<f_cnt(false, (U) + 1, U, (t) + 1)>();                               \
      WRD((U) + 1)                                                                                  \
    }                                                                                               \
    SB0();                                                                                          \
  }
#define FU(U)                                                                                       \
  {                                                                                                 \
    if (((U) & 3) < 3) {                                                                            \
      wait_vm4<f_cnt(true, U, U, 0)>(af[(U) & 3][0], af[(U) & 3][1], af[(U) & 3][2], af[(U) & 3][3]); \
      FSTEP(U, 0) FSTEP(U, 1) FSTEP(U, 2) FSTEP(U, 3)                                               \
    } else {                                                                                        \
      asm volatile("s_waitcnt vmcnt(%2)" : "+v"(af[3][0]), "+v"(af[3][1]) : "i"(f_cnt(true, U, U, 0)) : "memory"); \
      FSTEP(U, 0) FSTEP(U, 1)                                                                       \
    }                                                                                               \
  }
    v4u wf[2][4];
    ALD(0, 0); ALD(0, 1); ALD(0, 2); ALD(0, 3);
    ALD(1, 0); ALD(1, 1); ALD(1, 2); ALD(1, 3);
    ALD(2, 0); ALD(2, 1); ALD(2, 2); ALD(2, 3);
    static_assert(FD == 3, "window");
    WRD(0)
    if (DLY == 5) delay_us(3);
    FU(0) STAMP(7); FU(1) FU(2) FU(3) STAMP(8); FU(4) FU(5) FU(6) FU(7) STAMP(9); FU(8) FU(9) FU(10) FU(11) STAMP(14); FU(12) FU(13) FU(14) FU(15)
#undef FU
#undef FSTEP
#undef WRD
#undef ALD
#undef ALDS
#undef WP
    STAMP(10);
    {
      LAS char* dst = lp(ring);
#pragma unroll
      for (int tb = 0; tb < 4; tb++) {
        const int tk = 32 * (tb >> 1) + 8 * (i16 >> 2) + 4 * (tb & 1) + (i16 & 3);
#pragma unroll
        for (int nb = 0; nb < 4; nb++)
          *(LAS f32x4*)(dst + tk * 256 + (((4 * nb + q) ^ i16) & 15) * 16) = acc[nb][tb];
      }
    }
    STAMP(11);
  }
  {
    const int ec = tid & 15, et = tid >> 4;
    v2u xe[2];
    const v2u dk = *(const GAS v2u*)(p.dskip + (64 * ct + 4 * ec) * 2);
#pragma unroll
    for (int n = 0; n < 2; n++)
      xe[n] = *(const GAS v2u*)(p.x + (size_t)(64 * hp2 + et + 32 * n) * 1024 + (64 * ct + 4 * ec) * 2);
    bar();
    STAMP(12);
    if (j == 15 && s == 0 && tid == 0)
      __hip_atomic_store((GAS u64*)(p.ws + WS_EPOCH), epoch, RLX_AGENT);
    const float dkf[4] = {bf2f(dk.x & 0xffff), bf2f(dk.x >> 16), bf2f(dk.y & 0xffff), bf2f(dk.y >> 16)};
#pragma unroll
    for (int n = 0; n < 2; n++) {
      const int tk = et + 32 * n;
      const int key = (tk & 3) | (((tk >> 3) & 3) << 2);
      const int o = tk * 256 + ((ec ^ key) & 15) * 16;
      LAS char* fb = lds + L_RING + o;
      const f32x4 f = (*(LAS f32x4*)(fb) + *(LAS f32x4*)(fb + RING)) + (*(LAS f32x4*)(fb + 2 * RING) + *(LAS f32x4*)(fb + 3 * RING));
      const f32x4 y = *(LAS f32x4*)(lds + L_YB0 + o) + *(LAS f32x4*)(lds + L_YB1 + o);
      const int tkp = 64 * (hp2 & 1) + tk;
      const f32x2 se = *(LAS f32x2*)(lds + L_STAT + ((tkp >> 5) * 32 + ((tkp >> 2) & 1) * 16 + key) * 8);
      const float xv[4] = {bf2f(xe[n].x & 0xffff), bf2f(xe[n].x >> 16), bf2f(xe[n].y & 0xffff), bf2f(xe[n].y >> 16)};
      f32x4 o4;
#pragma unroll
      for (int e = 0; e < 4; e++) {
        const float xn = (xv[e] - se[0]) * se[1];
        const float sv = fmaf(xn, dkf[e], y[e]);
        const float fe = f[e] * se[1];
        const float g = fe * __builtin_amdgcn_rcpf(1.f + __builtin_amdgcn_exp2f(fe * -1.44269504088896f));
        o4[e] = fmaf(sv, g, xv[e]);
      }
      *(GAS f32x4*)(p.out + (size_t)(64 * hp2 + tk) * 512 + 64 * ct + 4 * ec) = o4;
    }
  }
  STAMP(13);
  STAMP_OUT();
}

extern "C" void kernel_launch(void* const* d_in, const int* in_sizes, int n_in, void* d_out, int out_size, void* d_ws,
                              size_t ws_size, hipStream_t stream) {
  Params p;
  p.x = (const char*)d_in[0]; p.lre = (const char*)d_in[1]; p.lim = (const char*)d_in[2];
  p.bre = (const char*)d_in[3]; p.bim = (const char*)d_in[4]; p.cre = (const char*)d_in[5]; p.cim = (const char*)d_in[6];
  p.logdt = (const char*)d_in[7]; p.dskip = (const char*)d_in[8]; p.w1 = (const char*)d_in[10]; p.w2 = (const char*)d_in[11];
  p.out = (float*)d_out; p.ws = (char*)d_ws;
  (void)hipFuncSetAttribute((const void*)k_fused, hipFuncAttributeMaxDynamicSharedMemorySize, LDS_BYTES);
  k_fused<<<256, 512, LDS_BYTES, stream>>>(p);
}
```

```cpp
#include <hip/hip_runtime.h>
#include <hip/hip_bf16.h>
#include <type_traits>
#include <utility>

typedef unsigned v4u __attribute__((ext_vector_type(4)));
typedef unsigned v2u __attribute__((ext_vector_type(2)));
typedef float f32x4 __attribute__((ext_vector_type(4)));
typedef float f32x2 __attribute__((ext_vector_type(2)));
typedef short bf16x8 __attribute__((ext_vector_type(8)));
typedef short s16x4 __attribute__((ext_vector_type(4)));
typedef __bf16 bf16x2_t __attribute__((ext_vector_type(2)));
typedef unsigned long long u64;
#define LAS __attribute__((address_space(3)))
#define GAS __attribute__((address_space(1)))
#if defined(__HIP_DEVICE_COMPILE__)
#define NOPK __attribute__((target("no-packed-fp32-ops")))
#else
#define NOPK
#endif
#define DI __device__ __forceinline__ NOPK
#define RLX_AGENT __ATOMIC_RELAXED, __HIP_MEMORY_SCOPE_AGENT
#define RLX_WG __ATOMIC_RELAXED, __HIP_MEMORY_SCOPE_WORKGROUP

static constexpr unsigned HP_BYTES = 16 * 14336 + 32 * 4096;
static constexpr unsigned Y_OFF = 16 * 14336;
static constexpr size_t WS_Z = 0;
static constexpr size_t WS_Z_BYTES = (size_t)32 * HP_BYTES;
static constexpr size_t WS_GRAN = WS_Z + WS_Z_BYTES;
static constexpr size_t WS_SG = WS_GRAN + 16 * 16 * 512;
static constexpr size_t WS_FLAG = WS_SG + 2048 * 16;
static constexpr size_t WS_EPOCH = WS_FLAG + 2 * 16 * 16 * 8;
static constexpr size_t WS_END = WS_EPOCH + 128;

static constexpr int LDS_BYTES = 160 * 1024;
static constexpr int STG = 24576;
static constexpr int L_W1A = 96 * 1024;
static constexpr int L_CS = 32 * 1024;
static constexpr int L_YB1 = 0;
static constexpr int L_HT = 16 * 1024;
static constexpr int L_E = 24 * 1024;
static constexpr int L_KEEP = 28 * 1024;
static constexpr int L_YB0 = 32 * 1024;
static constexpr int L_RING = 48 * 1024;
static constexpr int L_STAT = L_KEEP;
static constexpr int L_SU = L_KEEP + 1024;
static constexpr int L_SH = L_KEEP + 1280;
static constexpr int L_CNT = L_KEEP + 1792;
static constexpr int RING = 28672;
static constexpr int L_HIN = L_KEEP + 2048;
enum { C_W = 9, C_SH = 10, C_B2 = 11, C_E = 0, C_CX = 1, C_HIN = 2, C_HS = 4, C_FS = 5, C_Y1 = 7 };
#define ROT 1
#define CIT 3
#define NOWARM 0
#define ALGKM 0
#define LATEC 1
#define VM0 0
#define CPACE 2
#define CODEPF 38
#define APRIO 2
#define NWARM 7
#define ROT2 1

DI bool wall(bool x) { return __builtin_amdgcn_ballot_w64(!x) == 0ull; }
DI f32x4 mfma16(v4u a, v4u b, f32x4 c) {
  return __builtin_amdgcn_mfma_f32_16x16x32_bf16(__builtin_bit_cast(bf16x8, a), __builtin_bit_cast(bf16x8, b), c, 0, 0, 0);
}
DI unsigned pk_bf16(float lo, float hi) {
  f32x2 v = {lo, hi};
  return __builtin_bit_cast(unsigned, __builtin_convertvector(v, bf16x2_t));
}
DI unsigned pk_bf16v(f32x2 v) { return __builtin_bit_cast(unsigned, __builtin_convertvector(v, bf16x2_t)); }
DI float bf2f(unsigned u) { return __builtin_bit_cast(float, u << 16); }
DI unsigned opaque(unsigned v) { asm volatile("" : "+v"(v)); return v; }
DI LAS char* lp(unsigned a) { return (LAS char*)(size_t)a; }

template <int IMM> DI void gld16(v4u& d, unsigned voff, const void* sb) {
  asm volatile("global_load_dwordx4 %0, %1, %2 offset:%3" : "=v"(d) : "v"(voff), "s"(sb), "i"(IMM) : "memory");
}
template <int IMM> DI void gld16_sc1(v4u& d, unsigned voff, const void* sb) {
  asm volatile("global_load_dwordx4 %0, %1, %2 offset:%3 sc1" : "=v"(d) : "v"(voff), "s"(sb), "i"(IMM) : "memory");
}
template <int IMM> DI void gld_u16(unsigned& d, unsigned voff, const void* sb) {
  asm volatile("global_load_ushort %0, %1, %2 offset:%3" : "=v"(d) : "v"(voff), "s"(sb), "i"(IMM) : "memory");
}
DI void gld8_sc1(v2u& d, unsigned voff, const void* sb) {
  asm volatile("global_load_dwordx2 %0, %1, %2 sc1" : "=v"(d) : "v"(voff), "s"(sb) : "memory");
}
DI void dma4(unsigned lds, const void* sb, unsigned v0, unsigned v1, unsigned v2, unsigned v3) {
  asm volatile(
      "s_mov_b32 m0, %0\n\ts_nop 0\n\t"
      "global_load_lds_dwordx4 %2, %1\n\t"
      "global_load_lds_dwordx4 %3, %1 offset:1024\n\t"
      "global_load_lds_dwordx4 %4, %1 offset:2048\n\t"
      "global_load_lds_dwordx4 %5, %1 offset:3072"
      :: "s"(lds), "s"(sb), "v"(v0), "v"(v1), "v"(v2), "v"(v3) : "memory");
}
template <int N> DI void wait_vm() { asm volatile("s_waitcnt vmcnt(%0)" :: "i"(N) : "memory"); }
template <int N> DI void wait_vm4(v4u& a, v4u& b, v4u& c, v4u& d) {
  asm volatile("s_waitcnt vmcnt(%4)" : "+v"(a), "+v"(b), "+v"(c), "+v"(d) : "i"(N) : "memory");
}
DI void wait_lgkm() { asm volatile("s_waitcnt lgkmcnt(0)" ::: "memory"); }
DI void bar() { wait_lgkm(); __builtin_amdgcn_s_barrier(); asm volatile("" ::: "memory"); }

DI v4u lds_rd16(LAS char* p) { return *(LAS v4u*)p; }
DI v4u lds_tr(LAS char* p0, LAS char* p1) {
  s16x4 a = __builtin_amdgcn_ds_read_tr16_b64_v4i16((LAS s16x4*)p0);
  s16x4 b = __builtin_amdgcn_ds_read_tr16_b64_v4i16((LAS s16x4*)p1);
  bf16x8 f = __builtin_shufflevector(a, b, 0, 1, 2, 3, 4, 5, 6, 7);
  return __builtin_bit_cast(v4u, f);
}
DI unsigned cnt_arrive(LAS char* lds, int c) {
  wait_lgkm();
  unsigned old = 0;
  if ((threadIdx.x & 63) == 0) old = __hip_atomic_fetch_add((LAS unsigned*)(lds + L_CNT) + c, 1u, RLX_WG);
  return __builtin_amdgcn_readfirstlane(old);
}
DI void cnt_wait(LAS char* lds, int c, unsigned target) {
  unsigned spins = 0;
  while ((unsigned)__builtin_amdgcn_readfirstlane(__hip_atomic_load((LAS unsigned*)(lds + L_CNT) + c, RLX_WG)) < target) {
    if (++spins > (1u << 24)) break;
    __builtin_amdgcn_s_sleep(1);
  }
  asm volatile("" ::: "memory");
}
struct cpx { float r, i; };
DI cpx cmul(cpx a, cpx b) { return {a.r * b.r - a.i * b.i, a.r * b.i + a.i * b.r}; }
DI cpx cfma(cpx a, cpx h, cpx u) {
  return {fmaf(a.r, h.r, fmaf(-a.i, h.i, u.r)), fmaf(a.r, h.i, fmaf(a.i, h.r, u.i))};
}
DI float fma1(float a, float b, float c) { float d; asm("v_fma_f32 %0, %1, %2, %3" : "=v"(d) : "v"(a), "v"(b), "v"(c)); return d; }
DI float fms1(float a, float b, float c) { float d; asm("v_fma_f32 %0, -%1, %2, %3" : "=v"(d) : "v"(a), "v"(b), "v"(c)); return d; }
DI cpx cfma_s(cpx a, cpx h, cpx u) {
  return {fma1(a.r, h.r, fms1(a.i, h.i, u.r)), fma1(a.r, h.i, fma1(a.i, h.r, u.i))};
}
DI cpx csel(bool c, cpx a, cpx b) { return {c ? a.r : b.r, c ? a.i : b.i}; }

#define STAMP(i)
#define STAMP_DECL
#define STAMP_OUT()

struct Params {
  const char *x, *lre, *lim, *bre, *bim, *cre, *cim, *logdt, *dskip, *w1, *w2;
  float* out;
  char* ws;
};

DI void st16(__amdgpu_buffer_rsrc_t rs, unsigned off, v4u v) { __builtin_amdgcn_raw_buffer_store_b128(v, rs, (int)off, 0, 16); }
DI void st8(__amdgpu_buffer_rsrc_t rs, unsigned off, v2u v) { __builtin_amdgcn_raw_buffer_store_b64(v, rs, (int)off, 0, 16); }

DI void poll_flags(const char* fl, int l, u64 epoch) {
  GAS u64* fp = (GAS u64*)fl + (l & 3);
  unsigned spins = 0;
  while (!wall(__hip_atomic_load(fp, RLX_AGENT) == epoch)) {
    if (++spins > (1u << 20)) break;
    __builtin_amdgcn_s_sleep(4);
  }
  asm volatile("" ::: "memory");
}

DI f32x4 mfma16h(v2u a, v2u b, f32x4 c) {
  return __builtin_amdgcn_mfma_f32_16x16x16bf16_1k(__builtin_bit_cast(s16x4, a), __builtin_bit_cast(s16x4, b), c, 0, 0, 0);
}
DI v2u lds_tr1(LAS char* p0) { return __builtin_bit_cast(v2u, __builtin_amdgcn_ds_read_tr16_b64_v4i16((LAS s16x4*)p0)); }
DI void dma2(unsigned lds, const void* sb, unsigned v0, unsigned v1) {
  asm volatile(
      "s_mov_b32 m0, %0\n\ts_nop 0\n\t"
      "global_load_lds_dwordx4 %2, %1\n\t"
      "global_load_lds_dwordx4 %3, %1 offset:1024"
      :: "s"(lds), "s"(sb), "v"(v0), "v"(v1) : "memory");
}
DI void dma_stage(unsigned lds, const void* xb, const void* ub, const void* wb, unsigned vx0, unsigned vx1, unsigned vu, unsigned vw0, unsigned vw1) {
  asm volatile(
      "s_mov_b32 m0, %0\n\ts_nop 0\n\t"
      "global_load_lds_dwordx4 %5, %2\n\t"
      "global_load_lds_dwordx4 %6, %2 offset:1024\n\t"
      "global_load_lds_dwordx4 %7, %3 offset:2048\n\t"
      "global_load_lds_dwordx4 %8, %4 offset:3072\n\t"
      "s_mov_b32 m0, %1\n\ts_nop 0\n\t"
      "global_load_lds_dwordx4 %9, %4"
      :: "s"(lds), "s"(lds + 4096u), "s"(xb), "s"(ub), "s"(wb), "v"(vx0), "v"(vx1), "v"(vu), "v"(vw0), "v"(vw1) : "memory");
}
template <int CTRL> DI float dpp_add(float v) {
  return v + __builtin_bit_cast(float, __builtin_amdgcn_update_dpp(0, __builtin_bit_cast(int, v), CTRL, 0xf, 0xf, true));
}
DI float wave_sum(float v) {
  v = dpp_add<0x111>(v); v = dpp_add<0x112>(v); v = dpp_add<0x114>(v); v = dpp_add<0x118>(v);
  v = v + __builtin_bit_cast(float, __builtin_amdgcn_update_dpp(0, __builtin_bit_cast(int, v), 0x142, 0xa, 0xf, false));
  v = v + __builtin_bit_cast(float, __builtin_amdgcn_update_dpp(0, __builtin_bit_cast(int, v), 0x143, 0xc, 0xf, false));
  return __builtin_bit_cast(float, __builtin_amdgcn_readlane(__builtin_bit_cast(int, v), 63));
}
DI u64 mk_gran(u64 epoch, float v) {
  unsigned vb = __builtin_bit_cast(unsigned, v);
  vb = (vb + 0x80u) >> 8;
  return (epoch << 24) | (u64)(vb & 0xffffffu);
}
DI void dma1(unsigned lds, const void* sb, unsigned v) {
  asm volatile("s_mov_b32 m0, %0\n\ts_nop 0\n\tglobal_load_lds_dwordx4 %2, %1" :: "s"(lds), "s"(sb), "v"(v) : "memory");
}
#define FD 3
constexpr int usz(int u) { return (u & 3) == 3 ? 2 : 4; }
constexpr int f_cnt(bool kindA, int u, int at, int st) {
  int n = 0; bool seen = false;
  for (int a = 0; a < FD; a++) { if (seen) n += usz(a); if (kindA && a == u) seen = true; }
  for (int v = 0; v <= at; v++) {
    const int steps = v < at ? usz(v) : st;
    for (int t = 0; t < steps; t++) {
      if (v + FD < 16) {
        const int np = usz(v + FD), ns = usz(v);
        for (int i = 0; i < np; i++)
          if (i * ns / np == t) { if (seen) n++; if (kindA && v + FD == u && i == np - 1) seen = true; }
      }
      if (v + 4 < 16) { if (seen) n++; if (!kindA && v + 4 == u && t == usz(v) - 1) seen = true; }
    }
  }
  return n;
}
constexpr int hv_t(int n) { return n < 32 ? 8 + n / 4 : 8 + (n - 32) / 3; }
constexpr int hv_m(int n) { return n < 32 ? n % 4 : 4 + (n - 32) % 3; }
template <int... I, class F> DI void static_for(std::integer_sequence<int, I...>, F f) { (f(std::integral_constant<int, I>{}), ...); }
#define SB0() __builtin_amdgcn_sched_barrier(0)

__global__ __launch_bounds__(512, 2) NOPK void k_fused(Params p) {
  extern __shared__ __attribute__((aligned(16))) char smem[];
  LAS char* lds = (LAS char*)smem;
  STAMP_DECL
  STAMP(0);
  const int tid = threadIdx.x, l = tid & 63;
  const int w8 = __builtin_amdgcn_readfirstlane(tid >> 6);
  const int role = w8 >> 2, w = w8 & 3;
  const int bid = blockIdx.x, xcd = bid & 7, r = bid >> 3;
  const int j = 4 * (xcd >> 1) + (r >> 3);
  const int s = 8 * (xcd & 1) + (r & 7);
  const int hp2 = 8 * (xcd >> 1) + (r >> 2);
  const int ct = 4 * (xcd & 1) + (r & 3);
  const int odd = s & 1;
  const int i16 = l & 15, q = l >> 4;
  const int ta = i16 >> 2, tp = i16 & 3;
  const unsigned ldsb = (unsigned)(size_t)lds;
  const __amdgpu_buffer_rsrc_t rs = __builtin_amdgcn_make_buffer_rsrc((void*)p.ws, 0, (int)WS_END, 0x00020000);
  const v4u ones = {0x3F803F80u, 0x3F803F80u, 0x3F803F80u, 0x3F803F80u};

  const int rot = ROT ? ((2 * (r >> 3) + (r & 7)) & 7) : 0;
  u64 ep0;
  asm volatile("s_load_dwordx2 %0, %1, 0x0" : "=s"(ep0) : "s"(p.ws + WS_EPOCH) : "memory");
  unsigned plr[2], pli[2], pdt[2];
  v4u xr[2];
  unsigned cdum = 0;
  if (role == 0) {
    const char* rb = p.x + (size_t)(128 * j + 8 * s + 2 * w) * 1024;
    gld16<0>(xr[0], (unsigned)l * 16, rb); gld16<1024>(xr[1], (unsigned)l * 16, rb);
    const unsigned co = (unsigned)(32 * s + i16) * 2;
    gld_u16<0>(plr[0], co, p.lre); gld_u16<32>(plr[1], co, p.lre);
    gld_u16<0>(pli[0], co, p.lim); gld_u16<32>(pli[1], co, p.lim);
    gld_u16<0>(pdt[0], co, p.logdt); gld_u16<32>(pdt[1], co, p.logdt);
    {
      u64 pc;
      asm volatile("s_getpc_b64 %0" : "=s"(pc));
      const int ci = r * 4 + w;
      const char* cb = (const char*)(pc & ~255ull) + (ci < CODEPF ? ci : 0) * 1024;
      asm volatile("global_load_dword %0, %1, %2" : "=v"(cdum) : "v"((unsigned)l * 16), "s"(cb) : "memory");
    }
  }
  unsigned vx0, vx1, vu, vwA, vwB;
  {
    const int r8 = l >> 3, c8 = l & 7;
    const int tok0 = 8 * (r8 >> 2) + (r8 & 3);
    vx0 = (unsigned)(tok0 * 1024 + ((c8 ^ (r8 >> 1)) & 7) * 16);
    vx1 = (unsigned)((tok0 + 16) * 1024 + ((c8 ^ (4 + (r8 >> 1))) & 7) * 16);
    const int ch = 8 * w + r8, iu = 8 * (w & 1) + r8;
    vu = (unsigned)(ch * 1024 + ((c8 ^ (iu >> 1)) & 7) * 16);
    const int winoff = 448 * (s >> 1) + (odd ? 192 : 0);
    vwA = (unsigned)((16 * w + q) * 3584 + winoff + ((((i16 >> 1) ^ q) << 1) | (i16 & 1)) * 16);
    vwB = (unsigned)((16 * w + 8 + q) * 3584 + winoff + ((((i16 >> 1) ^ (q | 4)) << 1) | (i16 & 1)) * 16);
  }
  const char* xb = p.x + (size_t)(128 * j + 32 * (w >> 1) + 4 * (w & 1)) * 1024;
  const char* ub0 = p.bre + (size_t)(32 * s) * 1024;
  const char* ub1 = p.bim + (size_t)(32 * s) * 1024;
#define PIECE(N, I)                                                                                  \
  {                                                                                                  \
    constexpr int h_ = (N) / 3, n_ = (N) % 3;                                                        \
    const int kq_ = ((I) + rot) & 7;                                                                 \
    const unsigned d_ = ldsb + w * 3072 + ((I) & 3) * STG + h_ * 12288 + n_ * 1024;                  \
    if (n_ == 0) dma1(d_, xb + h_ * 65536 + kq_ * 128, vx0);                                         \
    if (n_ == 1) dma1(d_, xb + h_ * 65536 + kq_ * 128, vx1);                                         \
    if (n_ == 2) dma1(d_, (h_ ? ub1 : ub0) + kq_ * 128, vu);                                         \
  }
#define PALL(I) PIECE(0, I) PIECE(1, I) PIECE(2, I) PIECE(3, I) PIECE(4, I) PIECE(5, I)
#define W1P(C, n) dma1(ldsb + L_W1A + ((C) & 3) * 16384 + (16 * w + 4 * (n)) * 256,          \
                       p.w1 + (size_t)((((C) + rot) & 7) * 64 + ((n) & 1) * 4) * 3584, ((n) & 2) ? vwB : vwA)
  if (role == 1) {
    PALL(0)
    __builtin_amdgcn_s_barrier();
    PALL(1)
  } else {
    __builtin_amdgcn_s_barrier();
  }
  asm volatile("s_waitcnt lgkmcnt(0)" : "+s"(ep0) :: "memory");
  const u64 epoch = ep0 + 1ull;
  const unsigned tlo = (unsigned)(epoch & 0xffu), thi = (unsigned)(epoch >> 8);

  const unsigned sw0 = (unsigned)((i16 & 7) * 128 + (((0 + q) ^ (i16 >> 1)) & 7) * 16);
  const unsigned sw1 = (unsigned)((i16 & 7) * 128 + (((4 + q) ^ (i16 >> 1)) & 7) * 16);
  const unsigned xo = (unsigned)(2 * w * 3072 + (i16 >> 3) * 1024);
  const unsigned tro = (unsigned)((8 * q + ta) * 256 + 8 * tp);
  const int trkey = ta | ((q & 1) << 2);
  const unsigned zb = (unsigned)(2 * j + (w >> 1)) * HP_BYTES + (unsigned)l * 16u;
  const char* ab = p.ws + WS_Z + (size_t)hp2 * HP_BYTES;
  const unsigned avoff = (unsigned)l * 16;
  const int rot2 = ROT2 == 1 ? (hp2 & 3) : ROT2 == 2 ? (ct & 3) : 0;

  if (role == 0) {
    if (APRIO) __builtin_amdgcn_s_setprio(APRIO);
    asm volatile("s_waitcnt vmcnt(%8)" : "+v"(plr[0]), "+v"(plr[1]), "+v"(pli[0]), "+v"(pli[1]), "+v"(pdt[0]), "+v"(pdt[1]), "+v"(xr[0]), "+v"(xr[1]) : "i"(CODEPF ? 1 : 0) : "memory");
#pragma unroll
    for (int n = 0; n < 2; n++) {
      float v[8];
#pragma unroll
      for (int e = 0; e < 4; e++) { v[2 * e] = bf2f(xr[n][e] & 0xffffu); v[2 * e + 1] = __builtin_bit_cast(float, xr[n][e] & 0xffff0000u); }
      const float mean = wave_sum(((v[0] + v[1]) + (v[2] + v[3])) + ((v[4] + v[5]) + (v[6] + v[7]))) * (1.f / 512.f);
      float sq = 0.f;
#pragma unroll
      for (int e = 0; e < 8; e++) { const float d = v[e] - mean; sq = fmaf(d, d, sq); }
      const float rstd = rsqrtf(wave_sum(sq) * (1.f / 512.f) + 1e-5f);
      if (l < 2)
        __hip_atomic_store((GAS u64*)(p.ws + WS_SG) + (size_t)(128 * j + 8 * s + 2 * w + n) * 2 + l, mk_gran(epoch, l ? rstd : mean), RLX_AGENT);
    }
    cpx a1[2], cc[2], a8[2], pw[2], a128[2];
#pragma unroll
    for (int nb = 0; nb < 2; nb++) {
      const float lr = bf2f(plr[nb]), li = bf2f(pli[nb]);
      const float dtf = expf(bf2f(pdt[nb]));
      const float dt = bf2f(pk_bf16(dtf, 0.f) & 0xffffu);
      const float e = expf(dt * lr);
      float sn, cs;
      sincosf(dt * li, &sn, &cs);
      cpx a = {e * cs, e * sn};
      a1[nb] = a;
      const float nr = a.r - 1.f, ni = a.i, den = lr * lr + li * li;
      cc[nb] = {2.f * (nr * lr + ni * li) / den, 2.f * (ni * lr - nr * li) / den};
      cpx t = cmul(a, a); t = cmul(t, t); t = cmul(t, t);
      a8[nb] = t;
      cpx a16 = cmul(t, t), a32 = cmul(a16, a16), a64 = cmul(a32, a32);
      a128[nb] = cmul(a64, a64);
      cpx one = {1.f, 0.f};
      cpx pq = cmul(csel(q & 1, t, one), csel(q & 2, a16, one));
      cpx pwv = cmul(csel(w & 1, a32, one), csel(w & 2, a64, one));
      pw[nb] = cmul(pq, pwv);
    }
#pragma unroll
    for (int nb = 0; nb < 2; nb++)
      asm volatile("" : "+v"(a1[nb].r), "+v"(a1[nb].i), "+v"(cc[nb].r), "+v"(cc[nb].i), "+v"(a8[nb].r), "+v"(a8[nb].i), "+v"(pw[nb].r),
                   "+v"(pw[nb].i), "+v"(a128[nb].r), "+v"(a128[nb].i));
    STAMP(1);
    v4u cf[8][4];
    const unsigned cvoff = (unsigned)((64 * ct + (l >> 2)) * 1024 + (((l & 3) ^ ((-(l >> 4)) & 3)) * 16));
    const unsigned cra = opaque(ldsb + L_CS + w * 16384 + i16 * 64 + ((q ^ ((-(i16 >> 2)) & 3)) * 16));
#define CDMA(K0)                                                                          \
  _Pragma("unroll") for (int k = K0; k < K0 + 4; k++) {                                   \
    const int kr = (k + 2 * rot2) & 7;                                                    \
    const int sidx = 4 * w + 2 * (kr >> 2) + (kr & 1);                                    \
    const char* cb = (((kr >> 1) & 1) ? p.cim : p.cre) + sidx * 64;                       \
    _Pragma("unroll") for (int n = 0; n < 4; n++) dma1(ldsb + L_CS + w * 16384 + (k & 3) * 4096 + n * 1024, cb + n * 16384, cvoff); \
  }
#define CRD(K0)                                                                           \
  _Pragma("unroll") for (int k = K0; k < K0 + 4; k++)                                     \
    _Pragma("unroll") for (int n = 0; n < 4; n++) cf[k][n] = lds_rd16(lp(cra) + (k & 3) * 4096 + n * 1024);
    v2u sg;
    const unsigned sgoff = (unsigned)l * 8;
    const char* sgb = p.ws + WS_SG + (size_t)(128 * j + 32 * w) * 16;
    f32x4 au[2][2][2], asu;
#pragma unroll
    for (int b = 0; b < 2; b++)
#pragma unroll
      for (int nb = 0; nb < 2; nb++) { au[b][nb][0] = f32x4{0, 0, 0, 0}; au[b][nb][1] = f32x4{0, 0, 0, 0}; }
    asu = f32x4{0, 0, 0, 0};
    {
      const unsigned uo = (unsigned)((i16 >> 3) * 3072 + 2048);
      const int gs = (w & 1) * 2 + (w >> 1);
      unsigned cur = 0;
      v4u xd[2][2], wd[2][4], sd[2];
#pragma unroll
      for (int t = 0; t < 2; t++) xd[t][0] = xd[t][1] = wd[t][0] = wd[t][1] = wd[t][2] = wd[t][3] = sd[t] = v4u{0, 0, 0, 0};
#define A_STEP(RD, T, PX, PU, PS)                                                                     \
  {                                                                                                   \
    _Pragma("unroll") for (int g = 0; g < 4; g++) {                                                   \
      au[0][g & 1][g >> 1] = mfma16(xd[T][0], wd[T][g], au[0][g & 1][g >> 1]);                        \
      au[1][g & 1][g >> 1] = mfma16(xd[T][1], wd[T][g], au[1][g & 1][g >> 1]);                        \
      if (RD) wd[T][g] = lds_rd16(PU + g * 6144);                                                     \
      SB0();                                                                                          \
    }                                                                                                 \
    asu = mfma16(ones, sd[T], asu);                                                                   \
    if (RD) { xd[T][0] = lds_rd16(PX); xd[T][1] = lds_rd16(PX + 3072); sd[T] = lds_rd16(PS); }        \
    SB0();                                                                                            \
  }
#pragma unroll 1
      for (int it = 0; it < 8; it++) {
        const unsigned b0 = ldsb + cur;
        LAS char* px0 = lp(opaque(b0 + xo + sw0)); LAS char* px1 = lp(opaque(b0 + xo + sw1));
        LAS char* pu0 = lp(opaque(b0 + uo + sw0)); LAS char* pu1 = lp(opaque(b0 + uo + sw1));
        LAS char* ps0 = lp(opaque(b0 + uo + sw0 + gs * 6144)); LAS char* ps1 = lp(opaque(b0 + uo + sw1 + gs * 6144));
        cur = cur >= 3 * STG ? 0 : cur + STG;
        if (ALGKM) wait_lgkm();
        asm volatile("" ::: "memory");
        __builtin_amdgcn_s_barrier();
        asm volatile("" ::: "memory");
        SB0();
        A_STEP(true, 0, px0, pu0, ps0)
        A_STEP(true, 1, px1, pu1, ps1)
      }
      gld8_sc1(sg, sgoff, sgb);
      A_STEP(false, 0, lds, lds, lds)
      A_STEP(false, 1, lds, lds, lds)
#undef A_STEP
    }
    STAMP(2);
    {
      asm volatile("s_waitcnt vmcnt(0)" : "+v"(sg), "+v"(cdum) :: "memory");
      if (tid < 16) ((LAS unsigned*)(lds + L_CNT))[tid] = 0u;
      unsigned spins = 0;
      while (!wall(sg.y == thi && (sg.x >> 24) == tlo)) {
        if (++spins > (1u << 22)) break;
        __builtin_amdgcn_s_sleep(1);
        gld8_sc1(sg, sgoff, sgb);
        asm volatile("s_waitcnt vmcnt(0)" : "+v"(sg), "+v"(cdum) :: "memory");
      }
      const int tk = l >> 1;
      ((LAS unsigned*)(lds + L_STAT))[(w * 32 + ((tk >> 2) & 1) * 16 + ((tk & 3) | ((tk >> 3) << 2))) * 2 + (l & 1)] = sg.x << 8;
      if (q == 0) ((LAS float*)(lds + L_SU))[w * 16 + i16] = asu[0];
    }
    bar();
    STAMP(3);
    CDMA(0)
    cpx u[2][8];
    {
      f32x2 stt[2][4];
#pragma unroll
      for (int b = 0; b < 2; b++) {
        f32x4 s0 = *(LAS f32x4*)(lds + L_STAT + (w * 32 + b * 16 + 4 * q) * 8);
        f32x4 s1 = *(LAS f32x4*)(lds + L_STAT + (w * 32 + b * 16 + 4 * q) * 8 + 16);
        stt[b][0] = f32x2{s0[0], s0[1]}; stt[b][1] = f32x2{s0[2], s0[3]};
        stt[b][2] = f32x2{s1[0], s1[1]}; stt[b][3] = f32x2{s1[2], s1[3]};
      }
#pragma unroll
      for (int nb = 0; nb < 2; nb++) {
        const float sre = ((LAS float*)(lds + L_SU))[(nb * 2 + 0) * 16 + i16];
        const float sim = ((LAS float*)(lds + L_SU))[(nb * 2 + 1) * 16 + i16];
        cpx h = {0.f, 0.f};
#pragma unroll
        for (int k = 0; k < 8; k++) {
          const int b = k >> 2, rg = k & 3;
          const float m = stt[b][rg][0], rr = stt[b][rg][1];
          u[nb][k].r = fmaf(-m, sre, au[b][nb][0][rg]) * rr;
          u[nb][k].i = fmaf(-m, sim, au[b][nb][1][rg]) * rr;
          h = cfma(a1[nb], h, u[nb][k]);
        }
        *(LAS f32x2*)(lds + L_E + ((nb * 16 + i16) * 16 + 4 * w + q) * 8) = f32x2{h.r, h.i};
      }
    }
    cnt_arrive(lds, C_E);
    cnt_wait(lds, C_E, 4);
    cpx cl[2];
    {
      cpx c[2] = {{0.f, 0.f}, {0.f, 0.f}};
      LAS f32x4* e4a = (LAS f32x4*)(lds + L_E + i16 * 128);
      LAS f32x4* e4b = (LAS f32x4*)(lds + L_E + (16 + i16) * 128);
#pragma unroll 1
      for (int k = 0; k < 2 * w; k++) {
        f32x4 ea = e4a[k], eb = e4b[k];
        c[0] = cfma(a8[0], c[0], cpx{ea[0], ea[1]}); c[1] = cfma(a8[1], c[1], cpx{eb[0], eb[1]});
        c[0] = cfma(a8[0], c[0], cpx{ea[2], ea[3]}); c[1] = cfma(a8[1], c[1], cpx{eb[2], eb[3]});
      }
      cpx tot[2];
#pragma unroll
      for (int nb = 0; nb < 2; nb++) {
        LAS f32x4* e4 = nb ? e4b : e4a;
        f32x4 e0 = e4[2 * w], e1 = e4[2 * w + 1];
        cpx c1 = cfma(a8[nb], c[nb], cpx{e0[0], e0[1]});
        cpx c2 = cfma(a8[nb], c1, cpx{e0[2], e0[3]});
        cpx c3 = cfma(a8[nb], c2, cpx{e1[0], e1[1]});
        tot[nb] = cfma(a8[nb], c3, cpx{e1[2], e1[3]});
        cl[nb] = csel(q & 2, csel(q & 1, c3, c2), csel(q & 1, c1, c[nb]));
      }
      if (w == 3 && j < 15) {
        const cpx t = (q & 2) ? tot[1] : tot[0];
        const float v = (q & 1) ? t.i : t.r;
        const u64 g = mk_gran(epoch, v);
        GAS u64* gp = (GAS u64*)(p.ws + WS_GRAN + (size_t)(j * 16 + s) * 512) + (((q >> 1) * 16 + i16) * 2 + (q & 1));
        __hip_atomic_store(gp, g, RLX_AGENT);
      }
    }
    STAMP(4);
    wait_vm<1>();
    if (w != 3 || j == 15) wait_vm<0>();
    CRD(0)
    wait_lgkm();
    CDMA(4)
    if (w < 2) {
      const char* gb = p.ws + WS_GRAN + (size_t)s * 512 + w * 256;
      unsigned spins = 0;
      if (j > 0) {
        GAS u64* pp = (GAS u64*)(gb + (size_t)(l < j ? l : 0) * 8192 + 248);
        while (!wall((__hip_atomic_load(pp, RLX_AGENT) >> 24) == (epoch & 0xffffffffffull))) {
          if (++spins > (1u << 20)) break;
          __builtin_amdgcn_s_sleep(2);
        }
      }
      v4u gr[4];
      for (;;) {
#pragma unroll
        for (int m = 0; m < 4; m++) {
          const int pn = (4 * m + q) < j ? 4 * m + q : 0;
          gld16_sc1<0>(gr[m], (unsigned)(pn * 8192 + i16 * 16), gb);
        }
        wait_vm4<0>(gr[0], gr[1], gr[2], gr[3]);
        bool ok = true;
#pragma unroll
        for (int m = 0; m < 4; m++) {
          const bool need = (4 * m + q) < j;
          const bool good = (gr[m].y == thi) && ((gr[m].x >> 24) == tlo) && (gr[m].w == thi) && ((gr[m].z >> 24) == tlo);
          ok = ok && (good || !need);
        }
        if (wall(ok)) break;
        if (++spins > (1u << 20)) break;
        __builtin_amdgcn_s_sleep(2);
      }
      const cpx b128 = csel(w != 0, a128[1], a128[0]);
      const cpx a256 = cmul(b128, b128);
      const cpx a512 = cmul(a256, a256);
      cpx acc = {0.f, 0.f};
#pragma unroll
      for (int m = 0; m < 4; m++) {
        const bool need = (4 * m + q) < j;
        cpx e = {__builtin_bit_cast(float, gr[m].x << 8), __builtin_bit_cast(float, gr[m].z << 8)};
        acc = csel(need, cfma(a512, acc, e), acc);
      }
      const int dd = (j - 1 - q) & 3;
      cpx one = {1.f, 0.f};
      cpx fz = cmul(csel(dd & 1, b128, one), csel(dd & 2, a256, one));
      acc = cmul(acc, fz);
      *(LAS f32x2*)(lds + L_HIN + ((w * 16 + i16) * 4 + q) * 8) = f32x2{acc.r, acc.i};
      cnt_arrive(lds, C_HIN);
    }
    cnt_wait(lds, C_HIN, 2);
    unsigned wdum = 0;
    STAMP(5);
    {
      LAS char* ht = lds + L_HT + w * 2048;
      unsigned hpk[2][2][2][2];
#pragma unroll
      for (int nb = 0; nb < 2; nb++) {
        const f32x4 h01 = *(LAS f32x4*)(lds + L_HIN + (nb * 16 + i16) * 32);
        const f32x4 h23 = *(LAS f32x4*)(lds + L_HIN + (nb * 16 + i16) * 32 + 16);
        const cpx acc = {(h01[0] + h01[2]) + (h23[0] + h23[2]), (h01[1] + h01[3]) + (h23[1] + h23[3])};
        cpx h = cfma(pw[nb], acc, cl[nb]);
        float orr[8], oi[8];
#pragma unroll
        for (int k = 0; k < 8; k++) {
          h = cfma(a1[nb], h, u[nb][k]);
          orr[k] = cc[nb].r * h.r - cc[nb].i * h.i;
          oi[k] = -(cc[nb].r * h.i + cc[nb].i * h.r);
        }
#pragma unroll
        for (int b = 0; b < 2; b++) {
          hpk[nb][0][b][0] = pk_bf16(orr[4 * b], orr[4 * b + 1]); hpk[nb][0][b][1] = pk_bf16(orr[4 * b + 2], orr[4 * b + 3]);
          hpk[nb][1][b][0] = pk_bf16(oi[4 * b], oi[4 * b + 1]);   hpk[nb][1][b][1] = pk_bf16(oi[4 * b + 2], oi[4 * b + 3]);
        }
      }
      const unsigned zy = zb + Y_OFF + (unsigned)(4 * (s >> 1) + (s & 1)) * 4096u + (unsigned)(2 * (w & 1)) * 1024u;
#pragma unroll
      for (int b = 0; b < 2; b++) {
#pragma unroll
        for (int nb = 0; nb < 2; nb++)
#pragma unroll
          for (int part = 0; part < 2; part++)
            *(LAS v2u*)(ht + (part * 32 + nb * 16 + i16) * 32 + q * 8) = v2u{hpk[nb][part][b][0], hpk[nb][part][b][1]};
#pragma unroll
        for (int part = 0; part < 2; part++) {
          LAS char* rp = ht + (part * 32 + 8 * q + ta) * 32 + tp * 8;
          v4u v = lds_tr(rp, rp + 4 * 32);
          st16(rs, (unsigned)WS_Z + zy + (unsigned)part * 8192u + b * 1024u, v);
        }
      }
    }
    STAMP(6);
    wait_vm<0>();
    if (cnt_arrive(lds, C_HS) == 3 && l == 0)
      __hip_atomic_store((GAS u64*)(p.ws + WS_FLAG) + (j * 16 + s), epoch, RLX_AGENT);
    STAMP(7);
    CRD(4)
    cnt_arrive(lds, C_CX);
    {
      const unsigned wv2 = (unsigned)((l >> 5) * 1024 + (l & 31) * 16);
#pragma unroll 1
      for (int k = 0; k < NWARM; k++)
        asm volatile("global_load_dword %0, %1, %2" : "+v"(wdum) : "v"(wv2), "s"(p.w2 + (size_t)((k * 32 + r) * 4 + w) * 2048 + (xcd & 1) * 512) : "memory");
    }
    STAMP(8);
    poll_flags(p.ws + WS_FLAG + (size_t)(j * 16 + 4 * w) * 8, l, epoch);
    asm volatile("s_waitcnt vmcnt(0)" : "+v"(wdum) :: "memory");
    STAMP(9);
    f32x4 acc[4][4];
#pragma unroll
    for (int a = 0; a < 4; a++)
#pragma unroll
      for (int b = 0; b < 4; b++) acc[a][b] = f32x4{0, 0, 0, 0};
    v4u af[3][4];
    const char* ay = ab + Y_OFF + (size_t)(8 * w) * 4096;
#define YL(K, i) gld16_sc1<(i) * 1024>(af[(K) % 3][i], avoff, ay + ((((K) + 2 * rot2) & 7) * 4096))
#define YST(K, tb)                                                                                  \
  {                                                                                                 \
    _Pragma("unroll") for (int nb = 0; nb < 4; nb++) acc[nb][tb] = mfma16(cf[K][nb], af[(K) % 3][tb], acc[nb][tb]); \
    SB0();                                                                                          \
    if ((K) + 2 < 8) YL((K) + 2, tb);                                                               \
    SB0();                                                                                          \
  }
#define YS(K)                                                                                       \
  {                                                                                                 \
    wait_vm4<((K) < 7 ? 4 : 0)>(af[(K) % 3][0], af[(K) % 3][1], af[(K) % 3][2], af[(K) % 3][3]);    \
    YST(K, 0) YST(K, 1) YST(K, 2) YST(K, 3)                                                         \
  }
    YL(0, 0); YL(0, 1); YL(0, 2); YL(0, 3); YL(1, 0); YL(1, 1); YL(1, 2); YL(1, 3);
    YS(0) YS(1) YS(2) YS(3) YS(4) YS(5) YS(6) YS(7)
#undef YL
#undef YS
#undef YST
    STAMP(10);
    cnt_wait(lds, C_CX, 4);
    cnt_wait(lds, C_B2, 4);
    {
      LAS char* yb = lds + ((w & 1) ? L_YB1 : L_YB0);
      if (w < 2) cnt_wait(lds, C_Y1 + (w & 1), 1);
#pragma unroll
      for (int tb = 0; tb < 4; tb++) {
        const int tk = 32 * (tb >> 1) + 8 * (i16 >> 2) + 4 * (tb & 1) + (i16 & 3);
#pragma unroll
        for (int nb = 0; nb < 4; nb++) {
          LAS f32x4* d = (LAS f32x4*)(yb + tk * 256 + (((4 * nb + q) ^ i16) & 15) * 16);
          if (w < 2) *d = *d + acc[nb][tb]; else *d = acc[nb][tb];
        }
      }
      if (w >= 2) cnt_arrive(lds, C_Y1 + (w & 1));
    }
    STAMP(11);
  } else {
    v4u xf[2][16];
    f32x4 ah[7][2];
#pragma unroll
    for (int mb = 0; mb < 7; mb++) { ah[mb][0] = f32x4{0, 0, 0, 0}; ah[mb][1] = f32x4{0, 0, 0, 0}; }
    unsigned pt[7];
#pragma unroll
    for (int mb = 0; mb < 7; mb++) pt[mb] = opaque(ldsb + L_W1A + tro + (unsigned)(((mb + odd) ^ trkey) * 32));
    const int m1 = w < 3 ? w + 4 : 6;
    const unsigned hq0 = opaque(ldsb + L_W1A + tro + (unsigned)(((w + odd) ^ trkey) * 32));
    const unsigned hq1 = opaque(ldsb + L_W1A + tro + (unsigned)(((m1 + odd) ^ trkey) * 32));
    f32x4 sh0 = {0, 0, 0, 0}, sh1 = {0, 0, 0, 0};
    {
      LAS char* px0 = lp(opaque(ldsb + xo + sw0)); LAS char* px1 = lp(opaque(ldsb + xo + sw1));
#define L1IT(I, NW)                                                                                   \
  {                                                                                                   \
    wait_vm<NW>();                                                                                    \
    __builtin_amdgcn_s_barrier();                                                                     \
    asm volatile("" ::: "memory");                                                                    \
    xf[0][2 * (I)] = lds_rd16(px0 + ((I) & 3) * STG); xf[1][2 * (I)] = lds_rd16(px0 + ((I) & 3) * STG + 3072); \
    xf[0][2 * (I) + 1] = lds_rd16(px1 + ((I) & 3) * STG); xf[1][2 * (I) + 1] = lds_rd16(px1 + ((I) & 3) * STG + 3072); \
    SB0();                                                                                            \
    if ((I) < 3 || (I) == 7) {                                                                        \
      if ((I) < 6) { PALL((I) + 2) }                                                                  \
      W1P(I, 0); W1P(I, 1); W1P(I, 2); W1P(I, 3);                                                     \
    } else {                                                                                          \
      constexpr int c_ = (I) - 3;                                                                     \
      v4u wa[7], wb_[7], h0[2], h1[2];                                                                \
      _Pragma("unroll") for (int mb = 0; mb < 7; mb++) wa[mb] = lds_tr(lp(pt[mb]) + c_ * 16384, lp(pt[mb]) + c_ * 16384 + 1024); \
      SB0();                                                                                          \
      if ((I) < 6) { PIECE(0, (I) + 2) PIECE(1, (I) + 2) PIECE(2, (I) + 2) }                          \
      SB0();                                                                                          \
      _Pragma("unroll") for (int mb = 0; mb < 7; mb++) {                                              \
        ah[mb][0] = mfma16(wa[mb], xf[0][2 * c_], ah[mb][0]);                                         \
        ah[mb][1] = mfma16(wa[mb], xf[1][2 * c_], ah[mb][1]);                                         \
        wb_[mb] = lds_tr(lp(pt[mb]) + c_ * 16384 + 8192, lp(pt[mb]) + c_ * 16384 + 9216);             \
        if ((I) < 6) { if (mb == 1) PIECE(3, (I) + 2) if (mb == 3) PIECE(4, (I) + 2) if (mb == 5) PIECE(5, (I) + 2) } \
        SB0();                                                                                        \
      }                                                                                               \
      _Pragma("unroll") for (int mb = 0; mb < 7; mb++) {                                              \
        ah[mb][0] = mfma16(wb_[mb], xf[0][2 * c_ + 1], ah[mb][0]);                                    \
        ah[mb][1] = mfma16(wb_[mb], xf[1][2 * c_ + 1], ah[mb][1]);                                    \
        if (mb == 0) { h0[0] = lds_tr(lp(hq0) + c_ * 16384, lp(hq0) + c_ * 16384 + 1024); h1[0] = lds_tr(lp(hq1) + c_ * 16384, lp(hq1) + c_ * 16384 + 1024); } \
        if (mb == 1) { h0[1] = lds_tr(lp(hq0) + c_ * 16384 + 8192, lp(hq0) + c_ * 16384 + 9216); h1[1] = lds_tr(lp(hq1) + c_ * 16384 + 8192, lp(hq1) + c_ * 16384 + 9216); } \
        if (mb == 0) W1P(I, 0); if (mb == 2) W1P(I, 1); if (mb == 4) W1P(I, 2); if (mb == 6) W1P(I, 3); \
        SB0();                                                                                        \
      }                                                                                               \
      sh0 = mfma16(h0[0], ones, sh0); sh1 = mfma16(h1[0], ones, sh1);                                 \
      sh0 = mfma16(h0[1], ones, sh0); sh1 = mfma16(h1[1], ones, sh1);                                 \
    }                                                                                                 \
    wait_lgkm();                                                                                      \
  }
      L1IT(0, 6) L1IT(1, 10) L1IT(2, 14) STAMP(15); L1IT(3, 14) L1IT(4, 14) L1IT(5, 14) L1IT(6, 14) L1IT(7, 8)
#undef L1IT
    }
    STAMP(2);
    wait_vm<8>();
    bar();
    STAMP(3);
    const unsigned ring = ldsb + L_RING + w * RING;
    unsigned fv;
    {
      const int row = l >> 3, c16p = l & 7;
      fv = (unsigned)(row * 1024 + ct * 128 + ((((c16p >> 1) ^ ((row >> 1) & 3)) << 1) | (c16p & 1)) * 16);
    }
    const char* w2b = p.w2 + (size_t)(448 * w) * 1024;
#define WP(N, n) dma1(ring + ((N) & 1) * 14336 + (n) * 1024, w2b + (size_t)(((N) + rot2) & 3) * (112 * 1024) + (n) * 8192, fv)
    {
      v4u wd[7];
      static_for(std::make_integer_sequence<int, 7>{}, [&](auto NC) __attribute__((always_inline)) NOPK {
        constexpr int n = decltype(NC)::value, T2 = hv_t(n), m2 = hv_m(n);
        wd[n] = lds_tr(lp(pt[m2]) + (T2 & 7) * 8192, lp(pt[m2]) + (T2 & 7) * 8192 + 1024);
      });
      const unsigned zf = zb + (unsigned)s * 14336u;
      float nm[2], r2[2];
      v4u hs0, hs1;
      unsigned pkd[7][2][2];
      auto epi = [&](auto MB, auto BB) __attribute__((always_inline)) NOPK {
        constexpr int mb = decltype(MB)::value, b = decltype(BB)::value;
        const f32x4 sh = *(LAS f32x4*)(lds + L_SH + (mb * 16 + 4 * q) * 4);
        float y[4];
#pragma unroll
        for (int e = 0; e < 4; e++) {
          const float d = fmaf(sh[e], nm[b], ah[mb][b][e]);
          y[e] = d * __builtin_amdgcn_rcpf(1.f + __builtin_amdgcn_exp2f(d * r2[b]));
        }
        pkd[mb][b][0] = pk_bf16(y[0], y[1]); pkd[mb][b][1] = pk_bf16(y[2], y[3]);
        if constexpr ((mb & 1) == 1) {
          v4u v = {pkd[mb - 1][b][0], pkd[mb - 1][b][1], pkd[mb][b][0], pkd[mb][b][1]};
          st16(rs, (unsigned)WS_Z + zf + (unsigned)(mb >> 1) * 4096u + (unsigned)(2 * (w & 1) + b) * 1024u, v);
        }
      };
      static_for(std::make_integer_sequence<int, 56>{}, [&](auto NC) __attribute__((always_inline)) NOPK {
        constexpr int n = decltype(NC)::value, T = hv_t(n), mb = hv_m(n), sl = n % 7;
        if constexpr (n == 9) {
          wait_vm<0>();
          cnt_arrive(lds, C_W);
          cnt_wait(lds, C_W, 4);
        }
        if constexpr (n < 32 && (n & 3) == 0) {
          hs0 = lds_tr(lp(hq0) + (n / 4) * 8192, lp(hq0) + (n / 4) * 8192 + 1024);
          hs1 = lds_tr(lp(hq1) + (n / 4) * 8192, lp(hq1) + (n / 4) * 8192 + 1024);
        }
        if constexpr (n == 32) {
          if (i16 == 0) {
            *(LAS f32x4*)(lds + L_SH + (w * 16 + 4 * q) * 4) = sh0;
            if (w < 3) *(LAS f32x4*)(lds + L_SH + (m1 * 16 + 4 * q) * 4) = sh1;
          }
          cnt_arrive(lds, C_SH);
          cnt_wait(lds, C_SH, 4);
          const f32x2 s0 = *(LAS f32x2*)(lds + L_STAT + (w * 32 + i16) * 8), s1 = *(LAS f32x2*)(lds + L_STAT + (w * 32 + 16 + i16) * 8);
          nm[0] = -s0[0]; nm[1] = -s1[0]; r2[0] = s0[1] * -1.44269504088896f; r2[1] = s1[1] * -1.44269504088896f;
        }
        ah[mb][0] = mfma16(wd[sl], xf[0][T], ah[mb][0]);
        ah[mb][1] = mfma16(wd[sl], xf[1][T], ah[mb][1]);
        if constexpr (n < 32 && (n & 3) == 3) { sh0 = mfma16(hs0, ones, sh0); sh1 = mfma16(hs1, ones, sh1); }
        if constexpr (n + 7 < 56) {
          constexpr int T2 = hv_t(n + 7), m2 = hv_m(n + 7);
          wd[sl] = lds_tr(lp(pt[m2]) + (T2 & 7) * 8192, lp(pt[m2]) + (T2 & 7) * 8192 + 1024);
        }
        if constexpr (n >= 32 && (n - 32) % 3 == 1) {
          constexpr int u = (n - 32) / 3;
          epi(std::integral_constant<int, (u >> 2) * 2 + (u & 1)>{}, std::integral_constant<int, (u >> 1) & 1>{});
        }
        SB0();
      });
      STAMP(1);
      cnt_arrive(lds, C_B2);
      epi(std::integral_constant<int, 4>{}, std::integral_constant<int, 0>{});
      epi(std::integral_constant<int, 5>{}, std::integral_constant<int, 0>{});
      epi(std::integral_constant<int, 4>{}, std::integral_constant<int, 1>{});
      epi(std::integral_constant<int, 5>{}, std::integral_constant<int, 1>{});
      epi(std::integral_constant<int, 6>{}, std::integral_constant<int, 0>{});
      epi(std::integral_constant<int, 6>{}, std::integral_constant<int, 1>{});
      v4u v = {pkd[6][0][0], pkd[6][0][1], pkd[6][1][0], pkd[6][1][1]};
      st16(rs, (unsigned)WS_Z + zf + 12288u + (unsigned)(w & 1) * 1024u, v);
    }
    STAMP(4);
    wait_vm<0>();
    if (cnt_arrive(lds, C_FS) == 3 && l == 0)
      __hip_atomic_store((GAS u64*)(p.ws + WS_FLAG) + (256 + j * 16 + s), epoch, RLX_AGENT);
    STAMP(5);
    cnt_wait(lds, C_B2, 4);
    cnt_wait(lds, C_CX, 4);
#pragma unroll
    for (int n = 0; n < 14; n++) WP(0, n);
    poll_flags(p.ws + WS_FLAG + (size_t)(256 + j * 16 + 4 * w) * 8, l, epoch);
    STAMP(6);
    f32x4 acc[4][4];
#pragma unroll
    for (int a = 0; a < 4; a++)
#pragma unroll
      for (int b = 0; b < 4; b++) acc[a][b] = f32x4{0, 0, 0, 0};
    v4u af[4][4];
    const char* afb = ab + (size_t)(4 * w) * 14336;
    unsigned ftr[4];
#pragma unroll
    for (int nb = 0; nb < 4; nb++) {
      const int row = 4 * q + ta;
      ftr[nb] = opaque(ring + (unsigned)(row * 128 + ((nb ^ ((row >> 1) & 3)) * 32) + tp * 8));
    }
#define ALD(U, i) gld16_sc1<(i) * 1024>(af[(U) & 3][i], avoff, afb + (size_t)((((U) >> 2) + rot2) & 3) * 14336 + ((U) & 3) * 4096)
#define ALDS(U, V, t)                             \
  {                                                                                                 \
    if ((V) < 16) {                                                                                 \
      if (0 * usz(U) / usz(V) == (t)) ALD(V, 0);                                                    \
      if (1 * usz(U) / usz(V) == (t)) ALD(V, 1);                                                    \
      if (usz(V) == 4 && 2 * usz(U) / usz(V) == (t)) ALD(V, 2);                                     \
      if (usz(V) == 4 && 3 * usz(U) / usz(V) == (t)) ALD(V, 3);                                     \
    }                                                                                               \
  }
#define WRD(U)                                           \
  {                                                                                                 \
    _Pragma("unroll") for (int nb = 0; nb < 4; nb++) {                                              \
      LAS char* b_ = lp(ftr[nb]) + (((U) >> 2) & 1) * 14336 + ((U) & 3) * 4096;                     \
      if (((U) & 3) == 3) { v2u t_ = lds_tr1(b_); wf[(U) & 1][nb].x = t_.x; wf[(U) & 1][nb].y = t_.y; } \
      else wf[(U) & 1][nb] = lds_tr(b_, b_ + 2048);                                                 \
    }                                                                                               \
  }
#define FSTEP(U, t)                                                                                 \
  {                                                                                                 \
    if (((U) & 3) < 3) {                                                                            \
      _Pragma("unroll") for (int nb = 0; nb < 4; nb++)                                              \
          acc[nb][t] = mfma16(wf[(U) & 1][nb], af[(U) & 3][t], acc[nb][t]);                         \
    } else {                                                                                        \
      _Pragma("unroll") for (int nb = 0; nb < 4; nb++) {                                            \
        const v2u wh_ = {wf[(U) & 1][nb].x, wf[(U) & 1][nb].y};                                     \
        acc[nb][(2 * (t)) & 3] = mfma16h(wh_, v2u{af[3][t].x, af[3][t].y}, acc[nb][(2 * (t)) & 3]); \
        acc[nb][(2 * (t) + 1) & 3] = mfma16h(wh_, v2u{af[3][t].z, af[3][t].w}, acc[nb][(2 * (t) + 1) & 3]); \
      }                                                                                             \
    }                                                                                               \
    SB0();                                                                                          \
    ALDS(U, (U) + FD, t)                                                                            \
    if ((U) < 12) WP(((U) >> 2) + 1, 4 * ((U) & 3) + (t));                                          \
    if ((t) == usz(U) - 2 && (U) < 15) {                                                            \
      if ((U) + 1 >= 4) wait_vm<f_cnt(false, (U) + 1, U, (t) + 1)>();                               \
      WRD((U) + 1)                                                                                  \
    }                                                                                               \
    SB0();                                                                                          \
  }
#define FU(U)                                                                                       \
  {                                                                                                 \
    if (((U) & 3) < 3) {                                                                            \
      wait_vm4<f_cnt(true, U, U, 0)>(af[(U) & 3][0], af[(U) & 3][1], af[(U) & 3][2], af[(U) & 3][3]); \
      FSTEP(U, 0) FSTEP(U, 1) FSTEP(U, 2) FSTEP(U, 3)                                               \
    } else {                                                                                        \
      asm volatile("s_waitcnt vmcnt(%2)" : "+v"(af[3][0]), "+v"(af[3][1]) : "i"(f_cnt(true, U, U, 0)) : "memory"); \
      FSTEP(U, 0) FSTEP(U, 1)                                                                       \
    }                                                                                               \
  }
    v4u wf[2][4];
    ALD(0, 0); ALD(0, 1); ALD(0, 2); ALD(0, 3);
    ALD(1, 0); ALD(1, 1); ALD(1, 2); ALD(1, 3);
    ALD(2, 0); ALD(2, 1); ALD(2, 2); ALD(2, 3);
    static_assert(FD == 3, "window");
    WRD(0)
    FU(0) STAMP(7); FU(1) FU(2) FU(3) STAMP(8); FU(4) FU(5) FU(6) FU(7) STAMP(9); FU(8) FU(9) FU(10) FU(11) STAMP(14); FU(12) FU(13) FU(14) FU(15)
#undef FU
#undef FSTEP
#undef WRD
#undef ALD
#undef ALDS
#undef WP
    STAMP(10);
    {
      LAS char* dst = lp(ring);
#pragma unroll
      for (int tb = 0; tb < 4; tb++) {
        const int tk = 32 * (tb >> 1) + 8 * (i16 >> 2) + 4 * (tb & 1) + (i16 & 3);
#pragma unroll
        for (int nb = 0; nb < 4; nb++)
          *(LAS f32x4*)(dst + tk * 256 + (((4 * nb + q) ^ i16) & 15) * 16) = acc[nb][tb];
      }
    }
    STAMP(11);
  }
  {
    const int ec = tid & 15, et = tid >> 4;
    v2u xe[2];
    const v2u dk = *(const GAS v2u*)(p.dskip + (64 * ct + 4 * ec) * 2);
#pragma unroll
    for (int n = 0; n < 2; n++)
      xe[n] = *(const GAS v2u*)(p.x + (size_t)(64 * hp2 + et + 32 * n) * 1024 + (64 * ct + 4 * ec) * 2);
    bar();
    STAMP(12);
    if (j == 15 && s == 0 && tid == 0)
      __hip_atomic_store((GAS u64*)(p.ws + WS_EPOCH), epoch, RLX_AGENT);
    const float dkf[4] = {bf2f(dk.x & 0xffff), bf2f(dk.x >> 16), bf2f(dk.y & 0xffff), bf2f(dk.y >> 16)};
#pragma unroll
    for (int n = 0; n < 2; n++) {
      const int tk = et + 32 * n;
      const int key = (tk & 3) | (((tk >> 3) & 3) << 2);
      const int o = tk * 256 + ((ec ^ key) & 15) * 16;
      LAS char* fb = lds + L_RING + o;
      const f32x4 f = (*(LAS f32x4*)(fb) + *(LAS f32x4*)(fb + RING)) + (*(LAS f32x4*)(fb + 2 * RING) + *(LAS f32x4*)(fb + 3 * RING));
      const f32x4 y = *(LAS f32x4*)(lds + L_YB0 + o) + *(LAS f32x4*)(lds + L_YB1 + o);
      const int tkp = 64 * (hp2 & 1) + tk;
      const f32x2 se = *(LAS f32x2*)(lds + L_STAT + ((tkp >> 5) * 32 + ((tkp >> 2) & 1) * 16 + key) * 8);
      const float xv[4] = {bf2f(xe[n].x & 0xffff), bf2f(xe[n].x >> 16), bf2f(xe[n].y & 0xffff), bf2f(xe[n].y >> 16)};
      f32x4 o4;
#pragma unroll
      for (int e = 0; e < 4; e++) {
        const float xn = (xv[e] - se[0]) * se[1];
        const float sv = fmaf(xn, dkf[e], y[e]);
        const float fe = f[e] * se[1];
        const float g = fe * __builtin_amdgcn_rcpf(1.f + __builtin_amdgcn_exp2f(fe * -1.44269504088896f));
        o4[e] = fmaf(sv, g, xv[e]);
      }
      *(GAS f32x4*)(p.out + (size_t)(64 * hp2 + tk) * 512 + 64 * ct + 4 * ec) = o4;
    }
  }
  STAMP(13);
  STAMP_OUT();
}

extern "C" void kernel_launch(void* const* d_in, const int* in_sizes, int n_in, void* d_out, int out_size, void* d_ws,
                              size_t ws_size, hipStream_t stream) {
  Params p;
  p.x = (const char*)d_in[0]; p.lre = (const char*)d_in[1]; p.lim = (const char*)d_in[2];
  p.bre = (const char*)d_in[3]; p.bim = (const char*)d_in[4]; p.cre = (const char*)d_in[5]; p.cim = (const char*)d_in[6];
  p.logdt = (const char*)d_in[7]; p.dskip = (const char*)d_in[8]; p.w1 = (const char*)d_in[10]; p.w2 = (const char*)d_in[11];
  p.out = (float*)d_out; p.ws = (char*)d_ws;
  (void)hipFuncSetAttribute((const void*)k_fused, hipFuncAttributeMaxDynamicSharedMemorySize, LDS_BYTES);
  k_fused<<<256, 512, LDS_BYTES, stream>>>(p);
}
```

```cpp
#include <hip/hip_runtime.h>
#include <hip/hip_bf16.h>
#include <type_traits>
#include <utility>

typedef unsigned v4u __attribute__((ext_vector_type(4)));
typedef unsigned v2u __attribute__((ext_vector_type(2)));
typedef float f32x4 __attribute__((ext_vector_type(4)));
typedef float f32x2 __attribute__((ext_vector_type(2)));
typedef short bf16x8 __attribute__((ext_vector_type(8)));
typedef short s16x4 __attribute__((ext_vector_type(4)));
typedef __bf16 bf16x2_t __attribute__((ext_vector_type(2)));
typedef unsigned long long u64;
#define LAS __attribute__((address_space(3)))
#define GAS __attribute__((address_space(1)))
#if defined(__HIP_DEVICE_COMPILE__)
#define NOPK __attribute__((target("no-packed-fp32-ops")))
#else
#define NOPK
#endif
#define DI __device__ __forceinline__ NOPK
#define RLX_AGENT __ATOMIC_RELAXED, __HIP_MEMORY_SCOPE_AGENT
#define RLX_WG __ATOMIC_RELAXED, __HIP_MEMORY_SCOPE_WORKGROUP

static constexpr unsigned HP_BYTES = 16 * 14336 + 32 * 4096;
static constexpr unsigned Y_OFF = 16 * 14336;
static constexpr size_t WS_Z = 0;
static constexpr size_t WS_Z_BYTES = (size_t)32 * HP_BYTES;
static constexpr size_t WS_GRAN = WS_Z + WS_Z_BYTES;
static constexpr size_t WS_SG = WS_GRAN + 16 * 16 * 512;
static constexpr size_t WS_FLAG = WS_SG + 2048 * 16;
static constexpr size_t WS_EPOCH = WS_FLAG + 2 * 16 * 16 * 8;
static constexpr size_t WS_END = WS_EPOCH + 128;

static constexpr int LDS_BYTES = 160 * 1024;
static constexpr int STG = 24576;
static constexpr int L_W1A = 96 * 1024;
static constexpr int L_W1B = 32 * 1024;
static constexpr int L_CST = 0;
static constexpr int L_YB1 = L_CST;
static constexpr int L_HT = 16 * 1024;
static constexpr int L_E = 24 * 1024;
static constexpr int L_KEEP = 28 * 1024;
static constexpr int L_YB0 = 32 * 1024;
static constexpr int L_RING = 48 * 1024;
static constexpr int L_STAT = L_KEEP;
static constexpr int L_SU = L_KEEP + 1024;
static constexpr int L_SH = L_KEEP + 1280;
static constexpr int L_CNT = L_KEEP + 1792;
static constexpr int RING = 28672;
static constexpr int L_HIN = L_KEEP + 2048;
enum { C_W = 9, C_SH = 10, C_B2 = 11, C_E = 0, C_CX = 1, C_HIN = 2, C_HS = 4, C_FS = 5, C_Y1 = 7 };
#define ROT 1
#define CIT 3
#define NOWARM 0
#define ALGKM 0
#define LATEC 1
#define VM0 0
#define CPACE 2
#define CODEPF 38
#define APRIO 2
#define NWARM 0
#define MWARM 1
#define ROT2 1

DI bool wall(bool x) { return __builtin_amdgcn_ballot_w64(!x) == 0ull; }
DI f32x4 mfma16(v4u a, v4u b, f32x4 c) {
  return __builtin_amdgcn_mfma_f32_16x16x32_bf16(__builtin_bit_cast(bf16x8, a), __builtin_bit_cast(bf16x8, b), c, 0, 0, 0);
}
DI unsigned pk_bf16(float lo, float hi) {
  f32x2 v = {lo, hi};
  return __builtin_bit_cast(unsigned, __builtin_convertvector(v, bf16x2_t));
}
DI unsigned pk_bf16v(f32x2 v) { return __builtin_bit_cast(unsigned, __builtin_convertvector(v, bf16x2_t)); }
DI float bf2f(unsigned u) { return __builtin_bit_cast(float, u << 16); }
DI unsigned opaque(unsigned v) { asm volatile("" : "+v"(v)); return v; }
DI LAS char* lp(unsigned a) { return (LAS char*)(size_t)a; }

template <int IMM> DI void gld16(v4u& d, unsigned voff, const void* sb) {
  asm volatile("global_load_dwordx4 %0, %1, %2 offset:%3" : "=v"(d) : "v"(voff), "s"(sb), "i"(IMM) : "memory");
}
template <int IMM> DI void gld16_sc1(v4u& d, unsigned voff, const void* sb) {
  asm volatile("global_load_dwordx4 %0, %1, %2 offset:%3 sc1" : "=v"(d) : "v"(voff), "s"(sb), "i"(IMM) : "memory");
}
template <int IMM> DI void gld_u16(unsigned& d, unsigned voff, const void* sb) {
  asm volatile("global_load_ushort %0, %1, %2 offset:%3" : "=v"(d) : "v"(voff), "s"(sb), "i"(IMM) : "memory");
}
DI void gld8_sc1(v2u& d, unsigned voff, const void* sb) {
  asm volatile("global_load_dwordx2 %0, %1, %2 sc1" : "=v"(d) : "v"(voff), "s"(sb) : "memory");
}
DI void dma4(unsigned lds, const void* sb, unsigned v0, unsigned v1, unsigned v2, unsigned v3) {
  asm volatile(
      "s_mov_b32 m0, %0\n\ts_nop 0\n\t"
      "global_load_lds_dwordx4 %2, %1\n\t"
      "global_load_lds_dwordx4 %3, %1 offset:1024\n\t"
      "global_load_lds_dwordx4 %4, %1 offset:2048\n\t"
      "global_load_lds_dwordx4 %5, %1 offset:3072"
      :: "s"(lds), "s"(sb), "v"(v0), "v"(v1), "v"(v2), "v"(v3) : "memory");
}
template <int N> DI void wait_vm() { asm volatile("s_waitcnt vmcnt(%0)" :: "i"(N) : "memory"); }
template <int N> DI void wait_vm4(v4u& a, v4u& b, v4u& c, v4u& d) {
  asm volatile("s_waitcnt vmcnt(%4)" : "+v"(a), "+v"(b), "+v"(c), "+v"(d) : "i"(N) : "memory");
}
DI void wait_lgkm() { asm volatile("s_waitcnt lgkmcnt(0)" ::: "memory"); }
DI void bar() { wait_lgkm(); __builtin_amdgcn_s_barrier(); asm volatile("" ::: "memory"); }

DI v4u lds_rd16(LAS char* p) { return *(LAS v4u*)p; }
DI v4u lds_tr(LAS char* p0, LAS char* p1) {
  s16x4 a = __builtin_amdgcn_ds_read_tr16_b64_v4i16((LAS s16x4*)p0);
  s16x4 b = __builtin_amdgcn_ds_read_tr16_b64_v4i16((LAS s16x4*)p1);
  bf16x8 f = __builtin_shufflevector(a, b, 0, 1, 2, 3, 4, 5, 6, 7);
  return __builtin_bit_cast(v4u, f);
}
DI unsigned cnt_arrive(LAS char* lds, int c) {
  wait_lgkm();
  unsigned old = 0;
  if ((threadIdx.x & 63) == 0) old = __hip_atomic_fetch_add((LAS unsigned*)(lds + L_CNT) + c, 1u, RLX_WG);
  return __builtin_amdgcn_readfirstlane(old);
}
DI void cnt_wait(LAS char* lds, int c, unsigned target) {
  unsigned spins = 0;
  while ((unsigned)__builtin_amdgcn_readfirstlane(__hip_atomic_load((LAS unsigned*)(lds + L_CNT) + c, RLX_WG)) < target) {
    if (++spins > (1u << 24)) break;
    __builtin_amdgcn_s_sleep(1);
  }
  asm volatile("" ::: "memory");
}
struct cpx { float r, i; };
DI cpx cmul(cpx a, cpx b) { return {a.r * b.r - a.i * b.i, a.r * b.i + a.i * b.r}; }
DI cpx cfma(cpx a, cpx h, cpx u) {
  return {fmaf(a.r, h.r, fmaf(-a.i, h.i, u.r)), fmaf(a.r, h.i, fmaf(a.i, h.r, u.i))};
}
DI float fma1(float a, float b, float c) { float d; asm("v_fma_f32 %0, %1, %2, %3" : "=v"(d) : "v"(a), "v"(b), "v"(c)); return d; }
DI float fms1(float a, float b, float c) { float d; asm("v_fma_f32 %0, -%1, %2, %3" : "=v"(d) : "v"(a), "v"(b), "v"(c)); return d; }
DI cpx cfma_s(cpx a, cpx h, cpx u) {
  return {fma1(a.r, h.r, fms1(a.i, h.i, u.r)), fma1(a.r, h.i, fma1(a.i, h.r, u.i))};
}
DI cpx csel(bool c, cpx a, cpx b) { return {c ? a.r : b.r, c ? a.i : b.i}; }

#define STAMP(i)
#define STAMP_DECL
#define STAMP_OUT()

struct Params {
  const char *x, *lre, *lim, *bre, *bim, *cre, *cim, *logdt, *dskip, *w1, *w2;
  float* out;
  char* ws;
};

DI void st16(__amdgpu_buffer_rsrc_t rs, unsigned off, v4u v) { __builtin_amdgcn_raw_buffer_store_b128(v, rs, (int)off, 0, 16); }
DI void st8(__amdgpu_buffer_rsrc_t rs, unsigned off, v2u v) { __builtin_amdgcn_raw_buffer_store_b64(v, rs, (int)off, 0, 16); }

DI void poll_flags(const char* fl, int l, u64 epoch) {
  GAS u64* fp = (GAS u64*)fl + (l & 3);
  unsigned spins = 0;
  while (!wall(__hip_atomic_load(fp, RLX_AGENT) == epoch)) {
    if (++spins > (1u << 20)) break;
    __builtin_amdgcn_s_sleep(4);
  }
  asm volatile("" ::: "memory");
}

DI f32x4 mfma16h(v2u a, v2u b, f32x4 c) {
  return __builtin_amdgcn_mfma_f32_16x16x16bf16_1k(__builtin_bit_cast(s16x4, a), __builtin_bit_cast(s16x4, b), c, 0, 0, 0);
}
DI v2u lds_tr1(LAS char* p0) { return __builtin_bit_cast(v2u, __builtin_amdgcn_ds_read_tr16_b64_v4i16((LAS s16x4*)p0)); }
DI void dma2(unsigned lds, const void* sb, unsigned v0, unsigned v1) {
  asm volatile(
      "s_mov_b32 m0, %0\n\ts_nop 0\n\t"
      "global_load_lds_dwordx4 %2, %1\n\t"
      "global_load_lds_dwordx4 %3, %1 offset:1024"
      :: "s"(lds), "s"(sb), "v"(v0), "v"(v1) : "memory");
}
DI void dma_stage(unsigned lds, const void* xb, const void* ub, const void* wb, unsigned vx0, unsigned vx1, unsigned vu, unsigned vw0, unsigned vw1) {
  asm volatile(
      "s_mov_b32 m0, %0\n\ts_nop 0\n\t"
      "global_load_lds_dwordx4 %5, %2\n\t"
      "global_load_lds_dwordx4 %6, %2 offset:1024\n\t"
      "global_load_lds_dwordx4 %7, %3 offset:2048\n\t"
      "global_load_lds_dwordx4 %8, %4 offset:3072\n\t"
      "s_mov_b32 m0, %1\n\ts_nop 0\n\t"
      "global_load_lds_dwordx4 %9, %4"
      :: "s"(lds), "s"(lds + 4096u), "s"(xb), "s"(ub), "s"(wb), "v"(vx0), "v"(vx1), "v"(vu), "v"(vw0), "v"(vw1) : "memory");
}
template <int CTRL> DI float dpp_add(float v) {
  return v + __builtin_bit_cast(float, __builtin_amdgcn_update_dpp(0, __builtin_bit_cast(int, v), CTRL, 0xf, 0xf, true));
}
DI float wave_sum(float v) {
  v = dpp_add<0x111>(v); v = dpp_add<0x112>(v); v = dpp_add<0x114>(v); v = dpp_add<0x118>(v);
  v = v + __builtin_bit_cast(float, __builtin_amdgcn_update_dpp(0, __builtin_bit_cast(int, v), 0x142, 0xa, 0xf, false));
  v = v + __builtin_bit_cast(float, __builtin_amdgcn_update_dpp(0, __builtin_bit_cast(int, v), 0x143, 0xc, 0xf, false));
  return __builtin_bit_cast(float, __builtin_amdgcn_readlane(__builtin_bit_cast(int, v), 63));
}
DI u64 mk_gran(u64 epoch, float v) {
  unsigned vb = __builtin_bit_cast(unsigned, v);
  vb = (vb + 0x80u) >> 8;
  return (epoch << 24) | (u64)(vb & 0xffffffu);
}
DI void dma1(unsigned lds, const void* sb, unsigned v) {
  asm volatile("s_mov_b32 m0, %0\n\ts_nop 0\n\tglobal_load_lds_dwordx4 %2, %1" :: "s"(lds), "s"(sb), "v"(v) : "memory");
}
#define FD 3
constexpr int usz(int u) { return (u & 3) == 3 ? 2 : 4; }
constexpr int f_cnt(bool kindA, int u, int at, int st) {
  int n = 0; bool seen = false;
  for (int a = 0; a < FD; a++) { if (seen) n += usz(a); if (kindA && a == u) seen = true; }
  for (int v = 0; v <= at; v++) {
    const int steps = v < at ? usz(v) : st;
    for (int t = 0; t < steps; t++) {
      if (v + FD < 16) {
        const int np = usz(v + FD), ns = usz(v);
        for (int i = 0; i < np; i++)
          if (i * ns / np == t) { if (seen) n++; if (kindA && v + FD == u && i == np - 1) seen = true; }
      }
      if (v + 4 < 16) { if (seen) n++; if (!kindA && v + 4 == u && t == usz(v) - 1) seen = true; }
    }
  }
  return n;
}
constexpr int hv_t(int n) { return n < 32 ? 8 + n / 4 : 8 + (n - 32) / 3; }
constexpr int hv_m(int n) { return n < 32 ? n % 4 : 4 + (n - 32) % 3; }
template <int... I, class F> DI void static_for(std::integer_sequence<int, I...>, F f) { (f(std::integral_constant<int, I>{}), ...); }
#define SB0() __builtin_amdgcn_sched_barrier(0)

__global__ __launch_bounds__(512, 2) NOPK void k_fused(Params p) {
  extern __shared__ __attribute__((aligned(16))) char smem[];
  LAS char* lds = (LAS char*)smem;
  STAMP_DECL
  STAMP(0);
  const int tid = threadIdx.x, l = tid & 63;
  const int w8 = __builtin_amdgcn_readfirstlane(tid >> 6);
  const int role = w8 >> 2, w = w8 & 3;
  const int bid = blockIdx.x, xcd = bid & 7, r = bid >> 3;
  const int j = 4 * (xcd >> 1) + (r >> 3);
  const int s = 8 * (xcd & 1) + (r & 7);
  const int hp2 = 8 * (xcd >> 1) + (r >> 2);
  const int ct = 4 * (xcd & 1) + (r & 3);
  const int odd = s & 1;
  const int i16 = l & 15, q = l >> 4;
  const int ta = i16 >> 2, tp = i16 & 3;
  const unsigned ldsb = (unsigned)(size_t)lds;
  const __amdgpu_buffer_rsrc_t rs = __builtin_amdgcn_make_buffer_rsrc((void*)p.ws, 0, (int)WS_END, 0x00020000);
  const v4u ones = {0x3F803F80u, 0x3F803F80u, 0x3F803F80u, 0x3F803F80u};

  const int rot = ROT ? ((2 * (r >> 3) + (r & 7)) & 7) : 0;
  u64 ep0;
  asm volatile("s_load_dwordx2 %0, %1, 0x0" : "=s"(ep0) : "s"(p.ws + WS_EPOCH) : "memory");
  unsigned plr[2], pli[2], pdt[2];
  v4u xr[2];
  unsigned cdum = 0;
  if (role == 0) {
    const char* rb = p.x + (size_t)(128 * j + 8 * s + 2 * w) * 1024;
    gld16<0>(xr[0], (unsigned)l * 16, rb); gld16<1024>(xr[1], (unsigned)l * 16, rb);
    const unsigned co = (unsigned)(32 * s + i16) * 2;
    gld_u16<0>(plr[0], co, p.lre); gld_u16<32>(plr[1], co, p.lre);
    gld_u16<0>(pli[0], co, p.lim); gld_u16<32>(pli[1], co, p.lim);
    gld_u16<0>(pdt[0], co, p.logdt); gld_u16<32>(pdt[1], co, p.logdt);
    {
      u64 pc;
      asm volatile("s_getpc_b64 %0" : "=s"(pc));
      const int ci = r * 4 + w;
      const char* cb = (const char*)(pc & ~255ull) + (ci < CODEPF ? ci : 0) * 1024;
      asm volatile("global_load_dword %0, %1, %2" : "=v"(cdum) : "v"((unsigned)l * 16), "s"(cb) : "memory");
    }
#pragma unroll
    for (int k = 0; k < 3; k++) {
      int row = bid * 11 + w + 4 * k;
      if (w + 4 * k >= 11) row = bid * 11;
      const char* src = row < 1792 ? p.w2 + (size_t)row * 1024 : (row < 2304 ? p.cre + (size_t)(row - 1792) * 1024 : p.cim + (size_t)(row - 2304) * 1024);
      asm volatile("global_load_dword %0, %1, %2" : "+v"(cdum) : "v"((unsigned)l * 16), "s"(src) : "memory");
    }
  }
  unsigned vx0, vx1, vu, vwA, vwB;
  {
    const int r8 = l >> 3, c8 = l & 7;
    const int tok0 = 8 * (r8 >> 2) + (r8 & 3);
    vx0 = (unsigned)(tok0 * 1024 + ((c8 ^ (r8 >> 1)) & 7) * 16);
    vx1 = (unsigned)((tok0 + 16) * 1024 + ((c8 ^ (4 + (r8 >> 1))) & 7) * 16);
    const int ch = 8 * w + r8, iu = 8 * (w & 1) + r8;
    vu = (unsigned)(ch * 1024 + ((c8 ^ (iu >> 1)) & 7) * 16);
    const int winoff = 448 * (s >> 1) + (odd ? 192 : 0);
    vwA = (unsigned)((16 * w + q) * 3584 + winoff + ((((i16 >> 1) ^ q) << 1) | (i16 & 1)) * 16);
    vwB = (unsigned)((16 * w + 8 + q) * 3584 + winoff + ((((i16 >> 1) ^ (q | 4)) << 1) | (i16 & 1)) * 16);
  }
  const char* xb = p.x + (size_t)(128 * j + 32 * (w >> 1) + 4 * (w & 1)) * 1024;
  const char* ub0 = p.bre + (size_t)(32 * s) * 1024;
  const char* ub1 = p.bim + (size_t)(32 * s) * 1024;
#define PIECE(N, I)                                                                                  \
  {                                                                                                  \
    constexpr int h_ = (N) / 3, n_ = (N) % 3;                                                        \
    const int kq_ = ((I) + rot) & 7;                                                                 \
    const unsigned d_ = ldsb + w * 3072 + ((I) & 3) * STG + h_ * 12288 + n_ * 1024;                  \
    if (n_ == 0) dma1(d_, xb + h_ * 65536 + kq_ * 128, vx0);                                         \
    if (n_ == 1) dma1(d_, xb + h_ * 65536 + kq_ * 128, vx1);                                         \
    if (n_ == 2) dma1(d_, (h_ ? ub1 : ub0) + kq_ * 128, vu);                                         \
  }
#define PALL(I) PIECE(0, I) PIECE(1, I) PIECE(2, I) PIECE(3, I) PIECE(4, I) PIECE(5, I)
#define W1P(C, n) dma1(ldsb + L_W1A + ((C) & 3) * 16384 + (16 * w + 4 * (n)) * 256,          \
                       p.w1 + (size_t)((((C) + rot) & 7) * 64 + ((n) & 1) * 4) * 3584, ((n) & 2) ? vwB : vwA)
  if (role == 1) {
    PALL(0)
    __builtin_amdgcn_s_barrier();
    PALL(1)
  } else {
    __builtin_amdgcn_s_barrier();
  }
  asm volatile("s_waitcnt lgkmcnt(0)" : "+s"(ep0) :: "memory");
  const u64 epoch = ep0 + 1ull;
  const unsigned tlo = (unsigned)(epoch & 0xffu), thi = (unsigned)(epoch >> 8);

  const unsigned sw0 = (unsigned)((i16 & 7) * 128 + (((0 + q) ^ (i16 >> 1)) & 7) * 16);
  const unsigned sw1 = (unsigned)((i16 & 7) * 128 + (((4 + q) ^ (i16 >> 1)) & 7) * 16);
  const unsigned xo = (unsigned)(2 * w * 3072 + (i16 >> 3) * 1024);
  const unsigned tro = (unsigned)((8 * q + ta) * 256 + 8 * tp);
  const int trkey = ta | ((q & 1) << 2);
  const unsigned zb = (unsigned)(2 * j + (w >> 1)) * HP_BYTES + (unsigned)l * 16u;
  const char* ab = p.ws + WS_Z + (size_t)hp2 * HP_BYTES;
  const unsigned avoff = (unsigned)l * 16;
  const int rot2 = ROT2 == 1 ? (hp2 & 3) : ROT2 == 2 ? (ct & 3) : 0;

  if (role == 0) {
    if (APRIO) __builtin_amdgcn_s_setprio(APRIO);
    asm volatile("s_waitcnt vmcnt(%8)" : "+v"(plr[0]), "+v"(plr[1]), "+v"(pli[0]), "+v"(pli[1]), "+v"(pdt[0]), "+v"(pdt[1]), "+v"(xr[0]), "+v"(xr[1]) : "i"((CODEPF ? 1 : 0) + (MWARM ? 3 : 0)) : "memory");
#pragma unroll
    for (int n = 0; n < 2; n++) {
      float v[8];
#pragma unroll
      for (int e = 0; e < 4; e++) { v[2 * e] = bf2f(xr[n][e] & 0xffffu); v[2 * e + 1] = __builtin_bit_cast(float, xr[n][e] & 0xffff0000u); }
      const float mean = wave_sum(((v[0] + v[1]) + (v[2] + v[3])) + ((v[4] + v[5]) + (v[6] + v[7]))) * (1.f / 512.f);
      float sq = 0.f;
#pragma unroll
      for (int e = 0; e < 8; e++) { const float d = v[e] - mean; sq = fmaf(d, d, sq); }
      const float rstd = rsqrtf(wave_sum(sq) * (1.f / 512.f) + 1e-5f);
      if (l < 2)
        __hip_atomic_store((GAS u64*)(p.ws + WS_SG) + (size_t)(128 * j + 8 * s + 2 * w + n) * 2 + l, mk_gran(epoch, l ? rstd : mean), RLX_AGENT);
    }
    cpx a1[2], cc[2], a8[2], pw[2], a128[2];
#pragma unroll
    for (int nb = 0; nb < 2; nb++) {
      const float lr = bf2f(plr[nb]), li = bf2f(pli[nb]);
      const float dtf = expf(bf2f(pdt[nb]));
      const float dt = bf2f(pk_bf16(dtf, 0.f) & 0xffffu);
      const float e = expf(dt * lr);
      float sn, cs;
      sincosf(dt * li, &sn, &cs);
      cpx a = {e * cs, e * sn};
      a1[nb] = a;
      const float nr = a.r - 1.f, ni = a.i, den = lr * lr + li * li;
      cc[nb] = {2.f * (nr * lr + ni * li) / den, 2.f * (ni * lr - nr * li) / den};
      cpx t = cmul(a, a); t = cmul(t, t); t = cmul(t, t);
      a8[nb] = t;
      cpx a16 = cmul(t, t), a32 = cmul(a16, a16), a64 = cmul(a32, a32);
      a128[nb] = cmul(a64, a64);
      cpx one = {1.f, 0.f};
      cpx pq = cmul(csel(q & 1, t, one), csel(q & 2, a16, one));
      cpx pwv = cmul(csel(w & 1, a32, one), csel(w & 2, a64, one));
      pw[nb] = cmul(pq, pwv);
    }
#pragma unroll
    for (int nb = 0; nb < 2; nb++)
      asm volatile("" : "+v"(a1[nb].r), "+v"(a1[nb].i), "+v"(cc[nb].r), "+v"(cc[nb].i), "+v"(a8[nb].r), "+v"(a8[nb].i), "+v"(pw[nb].r),
                   "+v"(pw[nb].i), "+v"(a128[nb].r), "+v"(a128[nb].i));
    STAMP(1);
    v4u cf[8][4];
    const unsigned cvoff = (unsigned)((64 * ct + (l >> 2)) * 1024 + (l & 3) * 16);
#define CLOAD(K0)                                                                         \
  _Pragma("unroll") for (int k = K0; k < K0 + 4; k++) {                                   \
    const int kr = (k + 2 * rot2) & 7;                                                    \
    const int sidx = 4 * w + 2 * (kr >> 2) + (kr & 1);                                    \
    const char* cb = (((kr >> 1) & 1) ? p.cim : p.cre) + sidx * 64;                       \
    _Pragma("unroll") for (int n = 0; n < 4; n++) gld16<0>(cf[k][n], cvoff, cb + n * 16384); \
    if (CPACE) __builtin_amdgcn_s_sleep(CPACE);                                           \
  }
#define CWAIT(K0, N)                                                                      \
  asm volatile("s_waitcnt vmcnt(%16)" : "+v"(cf[K0][0]), "+v"(cf[K0][1]), "+v"(cf[K0][2]), "+v"(cf[K0][3]), "+v"(cf[K0 + 1][0]),   \
               "+v"(cf[K0 + 1][1]), "+v"(cf[K0 + 1][2]), "+v"(cf[K0 + 1][3]), "+v"(cf[K0 + 2][0]), "+v"(cf[K0 + 2][1]),          \
               "+v"(cf[K0 + 2][2]), "+v"(cf[K0 + 2][3]), "+v"(cf[K0 + 3][0]), "+v"(cf[K0 + 3][1]), "+v"(cf[K0 + 3][2]),          \
               "+v"(cf[K0 + 3][3]) : "i"(N) : "memory");
#define CXPOSE(K0)                                                                        \
  _Pragma("unroll") for (int k = K0; k < K0 + 4; k++) {                                   \
    _Pragma("unroll") for (int n = 0; n < 4; n++) *(LAS v4u*)(lp(cwa) + n * 1024) = cf[k][n]; \
    _Pragma("unroll") for (int n = 0; n < 4; n++) cf[k][n] = lds_rd16(lp(cra) + n * 1024);  \
  }
    v2u sg;
    const unsigned sgoff = (unsigned)l * 8;
    const char* sgb = p.ws + WS_SG + (size_t)(128 * j + 32 * w) * 16;
    f32x4 au[2][2][2], asu;
#pragma unroll
    for (int b = 0; b < 2; b++)
#pragma unroll
      for (int nb = 0; nb < 2; nb++) { au[b][nb][0] = f32x4{0, 0, 0, 0}; au[b][nb][1] = f32x4{0, 0, 0, 0}; }
    asu = f32x4{0, 0, 0, 0};
    {
      const unsigned uo = (unsigned)((i16 >> 3) * 3072 + 2048);
      const int gs = (w & 1) * 2 + (w >> 1);
      unsigned cur = 0;
      v4u xd[2][2], wd[2][4], sd[2];
#pragma unroll
      for (int t = 0; t < 2; t++) xd[t][0] = xd[t][1] = wd[t][0] = wd[t][1] = wd[t][2] = wd[t][3] = sd[t] = v4u{0, 0, 0, 0};
#define A_STEP(RD, T, PX, PU, PS)                                                                     \
  {                                                                                                   \
    _Pragma("unroll") for (int g = 0; g < 4; g++) {                                                   \
      au[0][g & 1][g >> 1] = mfma16(xd[T][0], wd[T][g], au[0][g & 1][g >> 1]);                        \
      au[1][g & 1][g >> 1] = mfma16(xd[T][1], wd[T][g], au[1][g & 1][g >> 1]);                        \
      if (RD) wd[T][g] = lds_rd16(PU + g * 6144);                                                     \
      SB0();                                                                                          \
    }                                                                                                 \
    asu = mfma16(ones, sd[T], asu);                                                                   \
    if (RD) { xd[T][0] = lds_rd16(PX); xd[T][1] = lds_rd16(PX + 3072); sd[T] = lds_rd16(PS); }        \
    SB0();                                                                                            \
  }
#pragma unroll 1
      for (int it = 0; it < 8; it++) {
        const unsigned b0 = ldsb + cur;
        LAS char* px0 = lp(opaque(b0 + xo + sw0)); LAS char* px1 = lp(opaque(b0 + xo + sw1));
        LAS char* pu0 = lp(opaque(b0 + uo + sw0)); LAS char* pu1 = lp(opaque(b0 + uo + sw1));
        LAS char* ps0 = lp(opaque(b0 + uo + sw0 + gs * 6144)); LAS char* ps1 = lp(opaque(b0 + uo + sw1 + gs * 6144));
        cur = cur >= 3 * STG ? 0 : cur + STG;
        if (ALGKM) wait_lgkm();
        asm volatile("" ::: "memory");
        __builtin_amdgcn_s_barrier();
        asm volatile("" ::: "memory");
        SB0();
        A_STEP(true, 0, px0, pu0, ps0)
        A_STEP(true, 1, px1, pu1, ps1)
      }
      gld8_sc1(sg, sgoff, sgb);
      A_STEP(false, 0, lds, lds, lds)
      A_STEP(false, 1, lds, lds, lds)
#undef A_STEP
    }
    STAMP(2);
    {
      asm volatile("s_waitcnt vmcnt(0)" : "+v"(sg), "+v"(cdum) :: "memory");
      if (tid < 16) ((LAS unsigned*)(lds + L_CNT))[tid] = 0u;
      unsigned spins = 0;
      while (!wall(sg.y == thi && (sg.x >> 24) == tlo)) {
        if (++spins > (1u << 22)) break;
        __builtin_amdgcn_s_sleep(1);
        gld8_sc1(sg, sgoff, sgb);
        asm volatile("s_waitcnt vmcnt(0)" : "+v"(sg), "+v"(cdum) :: "memory");
      }
      const int tk = l >> 1;
      ((LAS unsigned*)(lds + L_STAT))[(w * 32 + ((tk >> 2) & 1) * 16 + ((tk & 3) | ((tk >> 3) << 2))) * 2 + (l & 1)] = sg.x << 8;
      if (q == 0) ((LAS float*)(lds + L_SU))[w * 16 + i16] = asu[0];
    }
    bar();
    STAMP(3);
    cpx u[2][8];
    {
      f32x2 stt[2][4];
#pragma unroll
      for (int b = 0; b < 2; b++) {
        f32x4 s0 = *(LAS f32x4*)(lds + L_STAT + (w * 32 + b * 16 + 4 * q) * 8);
        f32x4 s1 = *(LAS f32x4*)(lds + L_STAT + (w * 32 + b * 16 + 4 * q) * 8 + 16);
        stt[b][0] = f32x2{s0[0], s0[1]}; stt[b][1] = f32x2{s0[2], s0[3]};
        stt[b][2] = f32x2{s1[0], s1[1]}; stt[b][3] = f32x2{s1[2], s1[3]};
      }
#pragma unroll
      for (int nb = 0; nb < 2; nb++) {
        const float sre = ((LAS float*)(lds + L_SU))[(nb * 2 + 0) * 16 + i16];
        const float sim = ((LAS float*)(lds + L_SU))[(nb * 2 + 1) * 16 + i16];
        cpx h = {0.f, 0.f};
#pragma unroll
        for (int k = 0; k < 8; k++) {
          const int b = k >> 2, rg = k & 3;
          const float m = stt[b][rg][0], rr = stt[b][rg][1];
          u[nb][k].r = fmaf(-m, sre, au[b][nb][0][rg]) * rr;
          u[nb][k].i = fmaf(-m, sim, au[b][nb][1][rg]) * rr;
          h = cfma(a1[nb], h, u[nb][k]);
        }
        *(LAS f32x2*)(lds + L_E + ((nb * 16 + i16) * 16 + 4 * w + q) * 8) = f32x2{h.r, h.i};
      }
    }
    cnt_arrive(lds, C_E);
    cnt_wait(lds, C_E, 4);
    cpx cl[2];
    {
      cpx c[2] = {{0.f, 0.f}, {0.f, 0.f}};
      LAS f32x4* e4a = (LAS f32x4*)(lds + L_E + i16 * 128);
      LAS f32x4* e4b = (LAS f32x4*)(lds + L_E + (16 + i16) * 128);
#pragma unroll 1
      for (int k = 0; k < 2 * w; k++) {
        f32x4 ea = e4a[k], eb = e4b[k];
        c[0] = cfma(a8[0], c[0], cpx{ea[0], ea[1]}); c[1] = cfma(a8[1], c[1], cpx{eb[0], eb[1]});
        c[0] = cfma(a8[0], c[0], cpx{ea[2], ea[3]}); c[1] = cfma(a8[1], c[1], cpx{eb[2], eb[3]});
      }
      cpx tot[2];
#pragma unroll
      for (int nb = 0; nb < 2; nb++) {
        LAS f32x4* e4 = nb ? e4b : e4a;
        f32x4 e0 = e4[2 * w], e1 = e4[2 * w + 1];
        cpx c1 = cfma(a8[nb], c[nb], cpx{e0[0], e0[1]});
        cpx c2 = cfma(a8[nb], c1, cpx{e0[2], e0[3]});
        cpx c3 = cfma(a8[nb], c2, cpx{e1[0], e1[1]});
        tot[nb] = cfma(a8[nb], c3, cpx{e1[2], e1[3]});
        cl[nb] = csel(q & 2, csel(q & 1, c3, c2), csel(q & 1, c1, c[nb]));
      }
      if (w == 3 && j < 15) {
        const cpx t = (q & 2) ? tot[1] : tot[0];
        const float v = (q & 1) ? t.i : t.r;
        const u64 g = mk_gran(epoch, v);
        GAS u64* gp = (GAS u64*)(p.ws + WS_GRAN + (size_t)(j * 16 + s) * 512) + (((q >> 1) * 16 + i16) * 2 + (q & 1));
        __hip_atomic_store(gp, g, RLX_AGENT);
      }
    }
    STAMP(4);
    const unsigned cwa = opaque(ldsb + L_CST + w * 4096 + (l >> 2) * 64 + (((l & 3) ^ ((-(l >> 4)) & 3)) * 16));
    const unsigned cra = opaque(ldsb + L_CST + w * 4096 + i16 * 64 + ((q ^ ((-(i16 >> 2)) & 3)) * 16));
    unsigned wdum = 0;
    CLOAD(0)
    {
      const unsigned wv2 = (unsigned)((l >> 5) * 1024 + (l & 31) * 16);
#pragma unroll 1
      for (int k = 0; k < NWARM; k++)
        asm volatile("global_load_dword %0, %1, %2" : "+v"(wdum) : "v"(wv2), "s"(p.w2 + (size_t)((k * 32 + r) * 4 + w) * 2048 + (xcd & 1) * 512) : "memory");
    }
    CWAIT(0, NWARM)
    CXPOSE(0)
    if (w < 2) {
      const char* gb = p.ws + WS_GRAN + (size_t)s * 512 + w * 256;
      unsigned spins = 0;
      if (j > 0) {
        GAS u64* pp = (GAS u64*)(gb + (size_t)(l < j ? l : 0) * 8192 + 248);
        while (!wall((__hip_atomic_load(pp, RLX_AGENT) >> 24) == (epoch & 0xffffffffffull))) {
          if (++spins > (1u << 20)) break;
          __builtin_amdgcn_s_sleep(2);
        }
      }
      v4u gr[4];
      for (;;) {
#pragma unroll
        for (int m = 0; m < 4; m++) {
          const int pn = (4 * m + q) < j ? 4 * m + q : 0;
          gld16_sc1<0>(gr[m], (unsigned)(pn * 8192 + i16 * 16), gb);
        }
        wait_vm4<0>(gr[0], gr[1], gr[2], gr[3]);
        bool ok = true;
#pragma unroll
        for (int m = 0; m < 4; m++) {
          const bool need = (4 * m + q) < j;
          const bool good = (gr[m].y == thi) && ((gr[m].x >> 24) == tlo) && (gr[m].w == thi) && ((gr[m].z >> 24) == tlo);
          ok = ok && (good || !need);
        }
        if (wall(ok)) break;
        if (++spins > (1u << 20)) break;
        __builtin_amdgcn_s_sleep(2);
      }
      const cpx b128 = csel(w != 0, a128[1], a128[0]);
      const cpx a256 = cmul(b128, b128);
      const cpx a512 = cmul(a256, a256);
      cpx acc = {0.f, 0.f};
#pragma unroll
      for (int m = 0; m < 4; m++) {
        const bool need = (4 * m + q) < j;
        cpx e = {__builtin_bit_cast(float, gr[m].x << 8), __builtin_bit_cast(float, gr[m].z << 8)};
        acc = csel(need, cfma(a512, acc, e), acc);
      }
      const int dd = (j - 1 - q) & 3;
      cpx one = {1.f, 0.f};
      cpx fz = cmul(csel(dd & 1, b128, one), csel(dd & 2, a256, one));
      acc = cmul(acc, fz);
      *(LAS f32x2*)(lds + L_HIN + ((w * 16 + i16) * 4 + q) * 8) = f32x2{acc.r, acc.i};
      cnt_arrive(lds, C_HIN);
    }
    cnt_wait(lds, C_HIN, 2);
    STAMP(5);
    {
      LAS char* ht = lds + L_HT + w * 2048;
      unsigned hpk[2][2][2][2];
#pragma unroll
      for (int nb = 0; nb < 2; nb++) {
        const f32x4 h01 = *(LAS f32x4*)(lds + L_HIN + (nb * 16 + i16) * 32);
        const f32x4 h23 = *(LAS f32x4*)(lds + L_HIN + (nb * 16 + i16) * 32 + 16);
        const cpx acc = {(h01[0] + h01[2]) + (h23[0] + h23[2]), (h01[1] + h01[3]) + (h23[1] + h23[3])};
        cpx h = cfma(pw[nb], acc, cl[nb]);
        float orr[8], oi[8];
#pragma unroll
        for (int k = 0; k < 8; k++) {
          h = cfma(a1[nb], h, u[nb][k]);
          orr[k] = cc[nb].r * h.r - cc[nb].i * h.i;
          oi[k] = -(cc[nb].r * h.i + cc[nb].i * h.r);
        }
#pragma unroll
        for (int b = 0; b < 2; b++) {
          hpk[nb][0][b][0] = pk_bf16(orr[4 * b], orr[4 * b + 1]); hpk[nb][0][b][1] = pk_bf16(orr[4 * b + 2], orr[4 * b + 3]);
          hpk[nb][1][b][0] = pk_bf16(oi[4 * b], oi[4 * b + 1]);   hpk[nb][1][b][1] = pk_bf16(oi[4 * b + 2], oi[4 * b + 3]);
        }
      }
      const unsigned zy = zb + Y_OFF + (unsigned)(4 * (s >> 1) + (s & 1)) * 4096u + (unsigned)(2 * (w & 1)) * 1024u;
#pragma unroll
      for (int b = 0; b < 2; b++) {
#pragma unroll
        for (int nb = 0; nb < 2; nb++)
#pragma unroll
          for (int part = 0; part < 2; part++)
            *(LAS v2u*)(ht + (part * 32 + nb * 16 + i16) * 32 + q * 8) = v2u{hpk[nb][part][b][0], hpk[nb][part][b][1]};
#pragma unroll
        for (int part = 0; part < 2; part++) {
          LAS char* rp = ht + (part * 32 + 8 * q + ta) * 32 + tp * 8;
          v4u v = lds_tr(rp, rp + 4 * 32);
          st16(rs, (unsigned)WS_Z + zy + (unsigned)part * 8192u + b * 1024u, v);
        }
      }
    }
    STAMP(6);
    asm volatile("s_waitcnt vmcnt(0)" : "+v"(wdum) :: "memory");
    if (cnt_arrive(lds, C_HS) == 3 && l == 0)
      __hip_atomic_store((GAS u64*)(p.ws + WS_FLAG) + (j * 16 + s), epoch, RLX_AGENT);
    STAMP(7);
    CLOAD(4)
    CWAIT(4, 0)
    CXPOSE(4)
    cnt_arrive(lds, C_CX);
    STAMP(8);
    poll_flags(p.ws + WS_FLAG + (size_t)(j * 16 + 4 * w) * 8, l, epoch);
    STAMP(9);
    f32x4 acc[4][4];
#pragma unroll
    for (int a = 0; a < 4; a++)
#pragma unroll
      for (int b = 0; b < 4; b++) acc[a][b] = f32x4{0, 0, 0, 0};
    v4u af[3][4];
    const char* ay = ab + Y_OFF + (size_t)(8 * w) * 4096;
#define YL(K, i) gld16_sc1<(i) * 1024>(af[(K) % 3][i], avoff, ay + ((((K) + 2 * rot2) & 7) * 4096))
#define YST(K, tb)                                                                                  \
  {                                                                                                 \
    _Pragma("unroll") for (int nb = 0; nb < 4; nb++) acc[nb][tb] = mfma16(cf[K][nb], af[(K) % 3][tb], acc[nb][tb]); \
    SB0();                                                                                          \
    if ((K) + 2 < 8) YL((K) + 2, tb);                                                               \
    SB0();                                                                                          \
  }
#define YS(K)                                                                                       \
  {                                                                                                 \
    wait_vm4<((K) < 7 ? 4 : 0)>(af[(K) % 3][0], af[(K) % 3][1], af[(K) % 3][2], af[(K) % 3][3]);    \
    YST(K, 0) YST(K, 1) YST(K, 2) YST(K, 3)                                                         \
  }
    YL(0, 0); YL(0, 1); YL(0, 2); YL(0, 3); YL(1, 0); YL(1, 1); YL(1, 2); YL(1, 3);
    YS(0) YS(1) YS(2) YS(3) YS(4) YS(5) YS(6) YS(7)
#undef YL
#undef YS
#undef YST
    STAMP(10);
    cnt_wait(lds, C_CX, 4);
    cnt_wait(lds, C_B2, 4);
    {
      LAS char* yb = lds + ((w & 1) ? L_YB1 : L_YB0);
      if (w < 2) cnt_wait(lds, C_Y1 + (w & 1), 1);
#pragma unroll
      for (int tb = 0; tb < 4; tb++) {
        const int tk = 32 * (tb >> 1) + 8 * (i16 >> 2) + 4 * (tb & 1) + (i16 & 3);
#pragma unroll
        for (int nb = 0; nb < 4; nb++) {
          LAS f32x4* d = (LAS f32x4*)(yb + tk * 256 + (((4 * nb + q) ^ i16) & 15) * 16);
          if (w < 2) *d = *d + acc[nb][tb]; else *d = acc[nb][tb];
        }
      }
      if (w >= 2) cnt_arrive(lds, C_Y1 + (w & 1));
    }
    STAMP(11);
  } else {
    v4u xf[2][16];
    f32x4 ah[7][2];
#pragma unroll
    for (int mb = 0; mb < 7; mb++) { ah[mb][0] = f32x4{0, 0, 0, 0}; ah[mb][1] = f32x4{0, 0, 0, 0}; }
    unsigned pt[7];
#pragma unroll
    for (int mb = 0; mb < 7; mb++) pt[mb] = opaque(ldsb + L_W1A + tro + (unsigned)(((mb + odd) ^ trkey) * 32));
    const int m1 = w < 3 ? w + 4 : 6;
    const unsigned hq0 = opaque(ldsb + L_W1A + tro + (unsigned)(((w + odd) ^ trkey) * 32));
    const unsigned hq1 = opaque(ldsb + L_W1A + tro + (unsigned)(((m1 + odd) ^ trkey) * 32));
    f32x4 sh0 = {0, 0, 0, 0}, sh1 = {0, 0, 0, 0};
    {
      LAS char* px0 = lp(opaque(ldsb + xo + sw0)); LAS char* px1 = lp(opaque(ldsb + xo + sw1));
#define L1IT(I, NW)                                                                                   \
  {                                                                                                   \
    wait_vm<NW>();                                                                                    \
    __builtin_amdgcn_s_barrier();                                                                     \
    asm volatile("" ::: "memory");                                                                    \
    xf[0][2 * (I)] = lds_rd16(px0 + ((I) & 3) * STG); xf[1][2 * (I)] = lds_rd16(px0 + ((I) & 3) * STG + 3072); \
    xf[0][2 * (I) + 1] = lds_rd16(px1 + ((I) & 3) * STG); xf[1][2 * (I) + 1] = lds_rd16(px1 + ((I) & 3) * STG + 3072); \
    SB0();                                                                                            \
    if ((I) < 3 || (I) == 7) {                                                                        \
      if ((I) < 6) { PALL((I) + 2) }                                                                  \
      W1P(I, 0); W1P(I, 1); W1P(I, 2); W1P(I, 3);                                                     \
    } else {                                                                                          \
      constexpr int c_ = (I) - 3;                                                                     \
      v4u wa[7], wb_[7], h0[2], h1[2];                                                                \
      _Pragma("unroll") for (int mb = 0; mb < 7; mb++) wa[mb] = lds_tr(lp(pt[mb]) + c_ * 16384, lp(pt[mb]) + c_ * 16384 + 1024); \
      SB0();                                                                                          \
      if ((I) < 6) { PIECE(0, (I) + 2) PIECE(1, (I) + 2) PIECE(2, (I) + 2) }                          \
      SB0();                                                                                          \
      _Pragma("unroll") for (int mb = 0; mb < 7; mb++) {                                              \
        ah[mb][0] = mfma16(wa[mb], xf[0][2 * c_], ah[mb][0]);                                         \
        ah[mb][1] = mfma16(wa[mb], xf[1][2 * c_], ah[mb][1]);                                         \
        wb_[mb] = lds_tr(lp(pt[mb]) + c_ * 16384 + 8192, lp(pt[mb]) + c_ * 16384 + 9216);             \
        if ((I) < 6) { if (mb == 1) PIECE(3, (I) + 2) if (mb == 3) PIECE(4, (I) + 2) if (mb == 5) PIECE(5, (I) + 2) } \
        SB0();                                                                                        \
      }                                                                                               \
      _Pragma("unroll") for (int mb = 0; mb < 7; mb++) {                                              \
        ah[mb][0] = mfma16(wb_[mb], xf[0][2 * c_ + 1], ah[mb][0]);                                    \
        ah[mb][1] = mfma16(wb_[mb], xf[1][2 * c_ + 1], ah[mb][1]);                                    \
        if (mb == 0) { h0[0] = lds_tr(lp(hq0) + c_ * 16384, lp(hq0) + c_ * 16384 + 1024); h1[0] = lds_tr(lp(hq1) + c_ * 16384, lp(hq1) + c_ * 16384 + 1024); } \
        if (mb == 1) { h0[1] = lds_tr(lp(hq0) + c_ * 16384 + 8192, lp(hq0) + c_ * 16384 + 9216); h1[1] = lds_tr(lp(hq1) + c_ * 16384 + 8192, lp(hq1) + c_ * 16384 + 9216); } \
        if (mb == 0) W1P(I, 0); if (mb == 2) W1P(I, 1); if (mb == 4) W1P(I, 2); if (mb == 6) W1P(I, 3); \
        SB0();                                                                                        \
      }                                                                                               \
      sh0 = mfma16(h0[0], ones, sh0); sh1 = mfma16(h1[0], ones, sh1);                                 \
      sh0 = mfma16(h0[1], ones, sh0); sh1 = mfma16(h1[1], ones, sh1);                                 \
    }                                                                                                 \
    wait_lgkm();                                                                                      \
  }
      L1IT(0, 6) L1IT(1, 10) L1IT(2, 14) STAMP(15); L1IT(3, 14) L1IT(4, 14) L1IT(5, 14) L1IT(6, 14) L1IT(7, 8)
#undef L1IT
    }
    STAMP(2);
    wait_vm<8>();
    bar();
    STAMP(3);
    const unsigned ring = ldsb + L_RING + w * RING;
    unsigned fv;
    {
      const int row = l >> 3, c16p = l & 7;
      fv = (unsigned)(row * 1024 + ct * 128 + ((((c16p >> 1) ^ ((row >> 1) & 3)) << 1) | (c16p & 1)) * 16);
    }
    const char* w2b = p.w2 + (size_t)(448 * w) * 1024;
#define WP(N, n) dma1(ring + ((N) & 1) * 14336 + (n) * 1024, w2b + (size_t)(((N) + rot2) & 3) * (112 * 1024) + (n) * 8192, fv)
    {
      v4u wd[7];
      static_for(std::make_integer_sequence<int, 7>{}, [&](auto NC) __attribute__((always_inline)) NOPK {
        constexpr int n = decltype(NC)::value, T2 = hv_t(n), m2 = hv_m(n);
        wd[n] = lds_tr(lp(pt[m2]) + (T2 & 7) * 8192, lp(pt[m2]) + (T2 & 7) * 8192 + 1024);
      });
      const unsigned zf = zb + (unsigned)s * 14336u;
      float nm[2], r2[2];
      v4u hs0, hs1;
      unsigned pkd[7][2][2];
      auto epi = [&](auto MB, auto BB) __attribute__((always_inline)) NOPK {
        constexpr int mb = decltype(MB)::value, b = decltype(BB)::value;
        const f32x4 sh = *(LAS f32x4*)(lds + L_SH + (mb * 16 + 4 * q) * 4);
        float y[4];
#pragma unroll
        for (int e = 0; e < 4; e++) {
          const float d = fmaf(sh[e], nm[b], ah[mb][b][e]);
          y[e] = d * __builtin_amdgcn_rcpf(1.f + __builtin_amdgcn_exp2f(d * r2[b]));
        }
        pkd[mb][b][0] = pk_bf16(y[0], y[1]); pkd[mb][b][1] = pk_bf16(y[2], y[3]);
        if constexpr ((mb & 1) == 1) {
          v4u v = {pkd[mb - 1][b][0], pkd[mb - 1][b][1], pkd[mb][b][0], pkd[mb][b][1]};
          st16(rs, (unsigned)WS_Z + zf + (unsigned)(mb >> 1) * 4096u + (unsigned)(2 * (w & 1) + b) * 1024u, v);
        }
      };
      static_for(std::make_integer_sequence<int, 56>{}, [&](auto NC) __attribute__((always_inline)) NOPK {
        constexpr int n = decltype(NC)::value, T = hv_t(n), mb = hv_m(n), sl = n % 7;
        if constexpr (n == 9) {
          wait_vm<0>();
          cnt_arrive(lds, C_W);
          cnt_wait(lds, C_W, 4);
        }
        if constexpr (n < 32 && (n & 3) == 0) {
          hs0 = lds_tr(lp(hq0) + (n / 4) * 8192, lp(hq0) + (n / 4) * 8192 + 1024);
          hs1 = lds_tr(lp(hq1) + (n / 4) * 8192, lp(hq1) + (n / 4) * 8192 + 1024);
        }
        if constexpr (n == 32) {
          if (i16 == 0) {
            *(LAS f32x4*)(lds + L_SH + (w * 16 + 4 * q) * 4) = sh0;
            if (w < 3) *(LAS f32x4*)(lds + L_SH + (m1 * 16 + 4 * q) * 4) = sh1;
          }
          cnt_arrive(lds, C_SH);
          cnt_wait(lds, C_SH, 4);
          const f32x2 s0 = *(LAS f32x2*)(lds + L_STAT + (w * 32 + i16) * 8), s1 = *(LAS f32x2*)(lds + L_STAT + (w * 32 + 16 + i16) * 8);
          nm[0] = -s0[0]; nm[1] = -s1[0]; r2[0] = s0[1] * -1.44269504088896f; r2[1] = s1[1] * -1.44269504088896f;
        }
        ah[mb][0] = mfma16(wd[sl], xf[0][T], ah[mb][0]);
        ah[mb][1] = mfma16(wd[sl], xf[1][T], ah[mb][1]);
        if constexpr (n < 32 && (n & 3) == 3) { sh0 = mfma16(hs0, ones, sh0); sh1 = mfma16(hs1, ones, sh1); }
        if constexpr (n + 7 < 56) {
          constexpr int T2 = hv_t(n + 7), m2 = hv_m(n + 7);
          wd[sl] = lds_tr(lp(pt[m2]) + (T2 & 7) * 8192, lp(pt[m2]) + (T2 & 7) * 8192 + 1024);
        }
        if constexpr (n >= 32 && (n - 32) % 3 == 1) {
          constexpr int u = (n - 32) / 3;
          epi(std::integral_constant<int, (u >> 2) * 2 + (u & 1)>{}, std::integral_constant<int, (u >> 1) & 1>{});
        }
        SB0();
      });
      STAMP(1);
      cnt_arrive(lds, C_B2);
      epi(std::integral_constant<int, 4>{}, std::integral_constant<int, 0>{});
      epi(std::integral_constant<int, 5>{}, std::integral_constant<int, 0>{});
      epi(std::integral_constant<int, 4>{}, std::integral_constant<int, 1>{});
      epi(std::integral_constant<int, 5>{}, std::integral_constant<int, 1>{});
      epi(std::integral_constant<int, 6>{}, std::integral_constant<int, 0>{});
      epi(std::integral_constant<int, 6>{}, std::integral_constant<int, 1>{});
      v4u v = {pkd[6][0][0], pkd[6][0][1], pkd[6][1][0], pkd[6][1][1]};
      st16(rs, (unsigned)WS_Z + zf + 12288u + (unsigned)(w & 1) * 1024u, v);
    }
    STAMP(4);
    wait_vm<0>();
    if (cnt_arrive(lds, C_FS) == 3 && l == 0)
      __hip_atomic_store((GAS u64*)(p.ws + WS_FLAG) + (256 + j * 16 + s), epoch, RLX_AGENT);
    STAMP(5);
    cnt_wait(lds, C_B2, 4);
    cnt_wait(lds, C_CX, 4);
#pragma unroll
    for (int n = 0; n < 14; n++) WP(0, n);
    poll_flags(p.ws + WS_FLAG + (size_t)(256 + j * 16 + 4 * w) * 8, l, epoch);
    STAMP(6);
    f32x4 acc[4][4];
#pragma unroll
    for (int a = 0; a < 4; a++)
#pragma unroll
      for (int b = 0; b < 4; b++) acc[a][b] = f32x4{0, 0, 0, 0};
    v4u af[4][4];
    const char* afb = ab + (size_t)(4 * w) * 14336;
    unsigned ftr[4];
#pragma unroll
    for (int nb = 0; nb < 4; nb++) {
      const int row = 4 * q + ta;
      ftr[nb] = opaque(ring + (unsigned)(row * 128 + ((nb ^ ((row >> 1) & 3)) * 32) + tp * 8));
    }
#define ALD(U, i) gld16_sc1<(i) * 1024>(af[(U) & 3][i], avoff, afb + (size_t)((((U) >> 2) + rot2) & 3) * 14336 + ((U) & 3) * 4096)
#define ALDS(U, V, t)                             \
  {                                                                                                 \
    if ((V) < 16) {                                                                                 \
      if (0 * usz(U) / usz(V) == (t)) ALD(V, 0);                                                    \
      if (1 * usz(U) / usz(V) == (t)) ALD(V, 1);                                                    \
      if (usz(V) == 4 && 2 * usz(U) / usz(V) == (t)) ALD(V, 2);                                     \
      if (usz(V) == 4 && 3 * usz(U) / usz(V) == (t)) ALD(V, 3);                                     \
    }                                                                                               \
  }
#define WRD(U)                                           \
  {                                                                                                 \
    _Pragma("unroll") for (int nb = 0; nb < 4; nb++) {                                              \
      LAS char* b_ = lp(ftr[nb]) + (((U) >> 2) & 1) * 14336 + ((U) & 3) * 4096;                     \
      if (((U) & 3) == 3) { v2u t_ = lds_tr1(b_); wf[(U) & 1][nb].x = t_.x; wf[(U) & 1][nb].y = t_.y; } \
      else wf[(U) & 1][nb] = lds_tr(b_, b_ + 2048);                                                 \
    }                                                                                               \
  }
#define FSTEP(U, t)                                                                                 \
  {                                                                                                 \
    if (((U) & 3) < 3) {                                                                            \
      _Pragma("unroll") for (int nb = 0; nb < 4; nb++)                                              \
          acc[nb][t] = mfma16(wf[(U) & 1][nb], af[(U) & 3][t], acc[nb][t]);                         \
    } else {                                                                                        \
      _Pragma("unroll") for (int nb = 0; nb < 4; nb++) {                                            \
        const v2u wh_ = {wf[(U) & 1][nb].x, wf[(U) & 1][nb].y};                                     \
        acc[nb][(2 * (t)) & 3] = mfma16h(wh_, v2u{af[3][t].x, af[3][t].y}, acc[nb][(2 * (t)) & 3]); \
        acc[nb][(2 * (t) + 1) & 3] = mfma16h(wh_, v2u{af[3][t].z, af[3][t].w}, acc[nb][(2 * (t) + 1) & 3]); \
      }                                                                                             \
    }                                                                                               \
    SB0();                                                                                          \
    ALDS(U, (U) + FD, t)                                                                            \
    if ((U) < 12) WP(((U) >> 2) + 1, 4 * ((U) & 3) + (t));                                          \
    if ((t) == usz(U) - 2 && (U) < 15) {                                                            \
      if ((U) + 1 >= 4) wait_vm<f_cnt(false, (U) + 1, U, (t) + 1)>();                               \
      WRD((U) + 1)                                                                                  \
    }                                                                                               \
    SB0();                                                                                          \
  }
#define FU(U)                                                                                       \
  {                                                                                                 \
    if (((U) & 3) < 3) {                                                                            \
      wait_vm4<f_cnt(true, U, U, 0)>(af[(U) & 3][0], af[(U) & 3][1], af[(U) & 3][2], af[(U) & 3][3]); \
      FSTEP(U, 0) FSTEP(U, 1) FSTEP(U, 2) FSTEP(U, 3)                                               \
    } else {                                                                                        \
      asm volatile("s_waitcnt vmcnt(%2)" : "+v"(af[3][0]), "+v"(af[3][1]) : "i"(f_cnt(true, U, U, 0)) : "memory"); \
      FSTEP(U, 0) FSTEP(U, 1)                                                                       \
    }                                                                                               \
  }
    v4u wf[2][4];
    ALD(0, 0); ALD(0, 1); ALD(0, 2); ALD(0, 3);
    ALD(1, 0); ALD(1, 1); ALD(1, 2); ALD(1, 3);
    ALD(2, 0); ALD(2, 1); ALD(2, 2); ALD(2, 3);
    static_assert(FD == 3, "window");
    WRD(0)
    FU(0) STAMP(7); FU(1) FU(2) FU(3) STAMP(8); FU(4) FU(5) FU(6) FU(7) STAMP(9); FU(8) FU(9) FU(10) FU(11) STAMP(14); FU(12) FU(13) FU(14) FU(15)
#undef FU
#undef FSTEP
#undef WRD
#undef ALD
#undef ALDS
#undef WP
    STAMP(10);
    {
      LAS char* dst = lp(ring);
#pragma unroll
      for (int tb = 0; tb < 4; tb++) {
        const int tk = 32 * (tb >> 1) + 8 * (i16 >> 2) + 4 * (tb & 1) + (i16 & 3);
#pragma unroll
        for (int nb = 0; nb < 4; nb++)
          *(LAS f32x4*)(dst + tk * 256 + (((4 * nb + q) ^ i16) & 15) * 16) = acc[nb][tb];
      }
    }
    STAMP(11);
  }
  {
    const int ec = tid & 15, et = tid >> 4;
    v2u xe[2];
    const v2u dk = *(const GAS v2u*)(p.dskip + (64 * ct + 4 * ec) * 2);
#pragma unroll
    for (int n = 0; n < 2; n++)
      xe[n] = *(const GAS v2u*)(p.x + (size_t)(64 * hp2 + et + 32 * n) * 1024 + (64 * ct + 4 * ec) * 2);
    bar();
    STAMP(12);
    if (j == 15 && s == 0 && tid == 0)
      __hip_atomic_store((GAS u64*)(p.ws + WS_EPOCH), epoch, RLX_AGENT);
    const float dkf[4] = {bf2f(dk.x & 0xffff), bf2f(dk.x >> 16), bf2f(dk.y & 0xffff), bf2f(dk.y >> 16)};
#pragma unroll
    for (int n = 0; n < 2; n++) {
      const int tk = et + 32 * n;
      const int key = (tk & 3) | (((tk >> 3) & 3) << 2);
      const int o = tk * 256 + ((ec ^ key) & 15) * 16;
      LAS char* fb = lds + L_RING + o;
      const f32x4 f = (*(LAS f32x4*)(fb) + *(LAS f32x4*)(fb + RING)) + (*(LAS f32x4*)(fb + 2 * RING) + *(LAS f32x4*)(fb + 3 * RING));
      const f32x4 y = *(LAS f32x4*)(lds + L_YB0 + o) + *(LAS f32x4*)(lds + L_YB1 + o);
      const int tkp = 64 * (hp2 & 1) + tk;
      const f32x2 se = *(LAS f32x2*)(lds + L_STAT + ((tkp >> 5) * 32 + ((tkp >> 2) & 1) * 16 + key) * 8);
      const float xv[4] = {bf2f(xe[n].x & 0xffff), bf2f(xe[n].x >> 16), bf2f(xe[n].y & 0xffff), bf2f(xe[n].y >> 16)};
      f32x4 o4;
#pragma unroll
      for (int e = 0; e < 4; e++) {
        const float xn = (xv[e] - se[0]) * se[1];
        const float sv = fmaf(xn, dkf[e], y[e]);
        const float fe = f[e] * se[1];
        const float g = fe * __builtin_amdgcn_rcpf(1.f + __builtin_amdgcn_exp2f(fe * -1.44269504088896f));
        o4[e] = fmaf(sv, g, xv[e]);
      }
      *(GAS f32x4*)(p.out + (size_t)(64 * hp2 + tk) * 512 + 64 * ct + 4 * ec) = o4;
    }
  }
  STAMP(13);
  STAMP_OUT();
}

extern "C" void kernel_launch(void* const* d_in, const int* in_sizes, int n_in, void* d_out, int out_size, void* d_ws,
                              size_t ws_size, hipStream_t stream) {
  Params p;
  p.x = (const char*)d_in[0]; p.lre = (const char*)d_in[1]; p.lim = (const char*)d_in[2];
  p.bre = (const char*)d_in[3]; p.bim = (const char*)d_in[4]; p.cre = (const char*)d_in[5]; p.cim = (const char*)d_in[6];
  p.logdt = (const char*)d_in[7]; p.dskip = (const char*)d_in[8]; p.w1 = (const char*)d_in[10]; p.w2 = (const char*)d_in[11];
  p.out = (float*)d_out; p.ws = (char*)d_ws;
  (void)hipFuncSetAttribute((const void*)k_fused, hipFuncAttributeMaxDynamicSharedMemorySize, LDS_BYTES);
  k_fused<<<256, 512, LDS_BYTES, stream>>>(p);
}
```

```cpp
#include <hip/hip_runtime.h>
#include <hip/hip_bf16.h>
#include <type_traits>
#include <utility>

typedef unsigned v4u __attribute__((ext_vector_type(4)));
typedef unsigned v2u __attribute__((ext_vector_type(2)));
typedef float f32x4 __attribute__((ext_vector_type(4)));
typedef float f32x2 __attribute__((ext_vector_type(2)));
typedef short bf16x8 __attribute__((ext_vector_type(8)));
typedef short s16x4 __attribute__((ext_vector_type(4)));
typedef __bf16 bf16x2_t __attribute__((ext_vector_type(2)));
typedef unsigned long long u64;
#define LAS __attribute__((address_space(3)))
#define GAS __attribute__((address_space(1)))
#if defined(__HIP_DEVICE_COMPILE__)
#define NOPK __attribute__((target("no-packed-fp32-ops")))
#else
#define NOPK
#endif
#define DI __device__ __forceinline__ NOPK
#define RLX_AGENT __ATOMIC_RELAXED, __HIP_MEMORY_SCOPE_AGENT
#define RLX_WG __ATOMIC_RELAXED, __HIP_MEMORY_SCOPE_WORKGROUP

static constexpr unsigned HP_BYTES = 16 * 14336 + 32 * 4096;
static constexpr unsigned Y_OFF = 16 * 14336;
static constexpr size_t WS_Z = 0;
static constexpr size_t WS_Z_BYTES = (size_t)32 * HP_BYTES;
static constexpr size_t WS_GRAN = WS_Z + WS_Z_BYTES;
static constexpr size_t WS_SG = WS_GRAN + 16 * 16 * 512;
static constexpr size_t WS_FLAG = WS_SG + 2048 * 16;
static constexpr size_t WS_EPOCH = WS_FLAG + 2 * 16 * 16 * 128;
static constexpr size_t WS_END = WS_EPOCH + 128;

static constexpr int LDS_BYTES = 160 * 1024;
static constexpr int STG = 24576;
static constexpr int L_W1A = 96 * 1024;
static constexpr int L_W1B = 32 * 1024;
static constexpr int L_CST = 0;
static constexpr int L_YB1 = L_CST;
static constexpr int L_HT = 16 * 1024;
static constexpr int L_E = 24 * 1024;
static constexpr int L_KEEP = 28 * 1024;
static constexpr int L_YB0 = 32 * 1024;
static constexpr int L_RING = 48 * 1024;
static constexpr int L_STAT = L_KEEP;
static constexpr int L_SU = L_KEEP + 1024;
static constexpr int L_SH = L_KEEP + 1280;
static constexpr int L_CNT = L_KEEP + 1792;
static constexpr int RING = 28672;
static constexpr int L_HIN = L_KEEP + 2048;
enum { C_W = 9, C_SH = 10, C_B2 = 11, C_E = 0, C_CX = 1, C_HIN = 2, C_HS = 4, C_FS = 5, C_Y1 = 7 };
#define ROT 1
#define CIT 3
#define NOWARM 0
#define ALGKM 0
#define LATEC 1
#define VM0 0
#define CPACE 2
#define CODEPF 38
#define APRIO 2
#define NWARM 0
#define MWARM 1
#define ROT2 1

DI bool wall(bool x) { return __builtin_amdgcn_ballot_w64(!x) == 0ull; }
DI f32x4 mfma16(v4u a, v4u b, f32x4 c) {
  return __builtin_amdgcn_mfma_f32_16x16x32_bf16(__builtin_bit_cast(bf16x8, a), __builtin_bit_cast(bf16x8, b), c, 0, 0, 0);
}
DI unsigned pk_bf16(float lo, float hi) {
  f32x2 v = {lo, hi};
  return __builtin_bit_cast(unsigned, __builtin_convertvector(v, bf16x2_t));
}
DI unsigned pk_bf16v(f32x2 v) { return __builtin_bit_cast(unsigned, __builtin_convertvector(v, bf16x2_t)); }
DI float bf2f(unsigned u) { return __builtin_bit_cast(float, u << 16); }
DI unsigned opaque(unsigned v) { asm volatile("" : "+v"(v)); return v; }
DI LAS char* lp(unsigned a) { return (LAS char*)(size_t)a; }

template <int IMM> DI void gld16(v4u& d, unsigned voff, const void* sb) {
  asm volatile("global_load_dwordx4 %0, %1, %2 offset:%3" : "=v"(d) : "v"(voff), "s"(sb), "i"(IMM) : "memory");
}
template <int IMM> DI void gld16_sc1(v4u& d, unsigned voff, const void* sb) {
  asm volatile("global_load_dwordx4 %0, %1, %2 offset:%3 sc1" : "=v"(d) : "v"(voff), "s"(sb), "i"(IMM) : "memory");
}
template <int IMM> DI void gld_u16(unsigned& d, unsigned voff, const void* sb) {
  asm volatile("global_load_ushort %0, %1, %2 offset:%3" : "=v"(d) : "v"(voff), "s"(sb), "i"(IMM) : "memory");
}
DI void gld8_sc1(v2u& d, unsigned voff, const void* sb) {
  asm volatile("global_load_dwordx2 %0, %1, %2 sc1" : "=v"(d) : "v"(voff), "s"(sb) : "memory");
}
DI void dma4(unsigned lds, const void* sb, unsigned v0, unsigned v1, unsigned v2, unsigned v3) {
  asm volatile(
      "s_mov_b32 m0, %0\n\ts_nop 0\n\t"
      "global_load_lds_dwordx4 %2, %1\n\t"
      "global_load_lds_dwordx4 %3, %1 offset:1024\n\t"
      "global_load_lds_dwordx4 %4, %1 offset:2048\n\t"
      "global_load_lds_dwordx4 %5, %1 offset:3072"
      :: "s"(lds), "s"(sb), "v"(v0), "v"(v1), "v"(v2), "v"(v3) : "memory");
}
template <int N> DI void wait_vm() { asm volatile("s_waitcnt vmcnt(%0)" :: "i"(N) : "memory"); }
template <int N> DI void wait_vm4(v4u& a, v4u& b, v4u& c, v4u& d) {
  asm volatile("s_waitcnt vmcnt(%4)" : "+v"(a), "+v"(b), "+v"(c), "+v"(d) : "i"(N) : "memory");
}
DI void wait_lgkm() { asm volatile("s_waitcnt lgkmcnt(0)" ::: "memory"); }
DI void bar() { wait_lgkm(); __builtin_amdgcn_s_barrier(); asm volatile("" ::: "memory"); }

DI v4u lds_rd16(LAS char* p) { return *(LAS v4u*)p; }
DI v4u lds_tr(LAS char* p0, LAS char* p1) {
  s16x4 a = __builtin_amdgcn_ds_read_tr16_b64_v4i16((LAS s16x4*)p0);
  s16x4 b = __builtin_amdgcn_ds_read_tr16_b64_v4i16((LAS s16x4*)p1);
  bf16x8 f = __builtin_shufflevector(a, b, 0, 1, 2, 3, 4, 5, 6, 7);
  return __builtin_bit_cast(v4u, f);
}
DI unsigned cnt_arrive(LAS char* lds, int c) {
  wait_lgkm();
  unsigned old = 0;
  if ((threadIdx.x & 63) == 0) old = __hip_atomic_fetch_add((LAS unsigned*)(lds + L_CNT) + c, 1u, RLX_WG);
  return __builtin_amdgcn_readfirstlane(old);
}
DI void cnt_wait(LAS char* lds, int c, unsigned target) {
  unsigned spins = 0;
  while ((unsigned)__builtin_amdgcn_readfirstlane(__hip_atomic_load((LAS unsigned*)(lds + L_CNT) + c, RLX_WG)) < target) {
    if (++spins > (1u << 24)) break;
    __builtin_amdgcn_s_sleep(1);
  }
  asm volatile("" ::: "memory");
}
struct cpx { float r, i; };
DI cpx cmul(cpx a, cpx b) { return {a.r * b.r - a.i * b.i, a.r * b.i + a.i * b.r}; }
DI cpx cfma(cpx a, cpx h, cpx u) {
  return {fmaf(a.r, h.r, fmaf(-a.i, h.i, u.r)), fmaf(a.r, h.i, fmaf(a.i, h.r, u.i))};
}
DI float fma1(float a, float b, float c) { float d; asm("v_fma_f32 %0, %1, %2, %3" : "=v"(d) : "v"(a), "v"(b), "v"(c)); return d; }
DI float fms1(float a, float b, float c) { float d; asm("v_fma_f32 %0, -%1, %2, %3" : "=v"(d) : "v"(a), "v"(b), "v"(c)); return d; }
DI cpx cfma_s(cpx a, cpx h, cpx u) {
  return {fma1(a.r, h.r, fms1(a.i, h.i, u.r)), fma1(a.r, h.i, fma1(a.i, h.r, u.i))};
}
DI cpx csel(bool c, cpx a, cpx b) { return {c ? a.r : b.r, c ? a.i : b.i}; }

#define STAMP(i)
#define STAMP_DECL
#define STAMP_OUT()

struct Params {
  const char *x, *lre, *lim, *bre, *bim, *cre, *cim, *logdt, *dskip, *w1, *w2;
  float* out;
  char* ws;
};

DI void st16(__amdgpu_buffer_rsrc_t rs, unsigned off, v4u v) { __builtin_amdgcn_raw_buffer_store_b128(v, rs, (int)off, 0, 16); }
DI void st8(__amdgpu_buffer_rsrc_t rs, unsigned off, v2u v) { __builtin_amdgcn_raw_buffer_store_b64(v, rs, (int)off, 0, 16); }

DI void poll_flags(const char* fl, int l, u64 epoch) {
  GAS u64* fp = (GAS u64*)fl + (l & 3) * 16;
  unsigned spins = 0;
  while (!wall(__hip_atomic_load(fp, RLX_AGENT) == epoch)) {
    if (++spins > (1u << 20)) break;
    __builtin_amdgcn_s_sleep(4);
  }
  asm volatile("" ::: "memory");
}

DI f32x4 mfma16h(v2u a, v2u b, f32x4 c) {
  return __builtin_amdgcn_mfma_f32_16x16x16bf16_1k(__builtin_bit_cast(s16x4, a), __builtin_bit_cast(s16x4, b), c, 0, 0, 0);
}
DI v2u lds_tr1(LAS char* p0) { return __builtin_bit_cast(v2u, __builtin_amdgcn_ds_read_tr16_b64_v4i16((LAS s16x4*)p0)); }
DI void dma2(unsigned lds, const void* sb, unsigned v0, unsigned v1) {
  asm volatile(
      "s_mov_b32 m0, %0\n\ts_nop 0\n\t"
      "global_load_lds_dwordx4 %2, %1\n\t"
      "global_load_lds_dwordx4 %3, %1 offset:1024"
      :: "s"(lds), "s"(sb), "v"(v0), "v"(v1) : "memory");
}
DI void dma_stage(unsigned lds, const void* xb, const void* ub, const void* wb, unsigned vx0, unsigned vx1, unsigned vu, unsigned vw0, unsigned vw1) {
  asm volatile(
      "s_mov_b32 m0, %0\n\ts_nop 0\n\t"
      "global_load_lds_dwordx4 %5, %2\n\t"
      "global_load_lds_dwordx4 %6, %2 offset:1024\n\t"
      "global_load_lds_dwordx4 %7, %3 offset:2048\n\t"
      "global_load_lds_dwordx4 %8, %4 offset:3072\n\t"
      "s_mov_b32 m0, %1\n\ts_nop 0\n\t"
      "global_load_lds_dwordx4 %9, %4"
      :: "s"(lds), "s"(lds + 4096u), "s"(xb), "s"(ub), "s"(wb), "v"(vx0), "v"(vx1), "v"(vu), "v"(vw0), "v"(vw1) : "memory");
}
template <int CTRL> DI float dpp_add(float v) {
  return v + __builtin_bit_cast(float, __builtin_amdgcn_update_dpp(0, __builtin_bit_cast(int, v), CTRL, 0xf, 0xf, true));
}
DI float wave_sum(float v) {
  v = dpp_add<0x111>(v); v = dpp_add<0x112>(v); v = dpp_add<0x114>(v); v = dpp_add<0x118>(v);
  v = v + __builtin_bit_cast(float, __builtin_amdgcn_update_dpp(0, __builtin_bit_cast(int, v), 0x142, 0xa, 0xf, false));
  v = v + __builtin_bit_cast(float, __builtin_amdgcn_update_dpp(0, __builtin_bit_cast(int, v), 0x143, 0xc, 0xf, false));
  return __builtin_bit_cast(float, __builtin_amdgcn_readlane(__builtin_bit_cast(int, v), 63));
}
DI u64 mk_gran(u64 epoch, float v) {
  unsigned vb = __builtin_bit_cast(unsigned, v);
  vb = (vb + 0x80u) >> 8;
  return (epoch << 24) | (u64)(vb & 0xffffffu);
}
DI void dma1(unsigned lds, const void* sb, unsigned v) {
  asm volatile("s_mov_b32 m0, %0\n\ts_nop 0\n\tglobal_load_lds_dwordx4 %2, %1" :: "s"(lds), "s"(sb), "v"(v) : "memory");
}
#define FD 3
constexpr int usz(int u) { return (u & 3) == 3 ? 2 : 4; }
constexpr int f_cnt(bool kindA, int u, int at, int st) {
  int n = 0; bool seen = false;
  for (int a = 0; a < FD; a++) { if (seen) n += usz(a); if (kindA && a == u) seen = true; }
  for (int v = 0; v <= at; v++) {
    const int steps = v < at ? usz(v) : st;
    for (int t = 0; t < steps; t++) {
      if (v + FD < 16) {
        const int np = usz(v + FD), ns = usz(v);
        for (int i = 0; i < np; i++)
          if (i * ns / np == t) { if (seen) n++; if (kindA && v + FD == u && i == np - 1) seen = true; }
      }
      if (v + 4 < 16) { if (seen) n++; if (!kindA && v + 4 == u && t == usz(v) - 1) seen = true; }
    }
  }
  return n;
}
constexpr int hv_t(int n) { return n < 32 ? 8 + n / 4 : 8 + (n - 32) / 3; }
constexpr int hv_m(int n) { return n < 32 ? n % 4 : 4 + (n - 32) % 3; }
template <int... I, class F> DI void static_for(std::integer_sequence<int, I...>, F f) { (f(std::integral_constant<int, I>{}), ...); }
#define SB0() __builtin_amdgcn_sched_barrier(0)

__global__ __launch_bounds__(512, 2) NOPK void k_fused(Params p) {
  extern __shared__ __attribute__((aligned(16))) char smem[];
  LAS char* lds = (LAS char*)smem;
  STAMP_DECL
  STAMP(0);
  const int tid = threadIdx.x, l = tid & 63;
  const int w8 = __builtin_amdgcn_readfirstlane(tid >> 6);
  const int role = w8 >> 2, w = w8 & 3;
  const int bid = blockIdx.x, xcd = bid & 7, r = bid >> 3;
  const int j = 4 * (xcd >> 1) + (r >> 3);
  const int s = 8 * (xcd & 1) + (r & 7);
  const int hp2 = 8 * (xcd >> 1) + (r >> 2);
  const int ct = 4 * (xcd & 1) + (r & 3);
  const int odd = s & 1;
  const int i16 = l & 15, q = l >> 4;
  const int ta = i16 >> 2, tp = i16 & 3;
  const unsigned ldsb = (unsigned)(size_t)lds;
  const __amdgpu_buffer_rsrc_t rs = __builtin_amdgcn_make_buffer_rsrc((void*)p.ws, 0, (int)WS_END, 0x00020000);
  const v4u ones = {0x3F803F80u, 0x3F803F80u, 0x3F803F80u, 0x3F803F80u};

  const int rot = ROT ? ((2 * (r >> 3) + (r & 7)) & 7) : 0;
  u64 ep0;
  asm volatile("s_load_dwordx2 %0, %1, 0x0" : "=s"(ep0) : "s"(p.ws + WS_EPOCH) : "memory");
  unsigned plr[2], pli[2], pdt[2];
  v4u xr[2];
  unsigned cdum = 0;
  if (role == 0) {
    const char* rb = p.x + (size_t)(128 * j + 8 * s + 2 * w) * 1024;
    gld16<0>(xr[0], (unsigned)l * 16, rb); gld16<1024>(xr[1], (unsigned)l * 16, rb);
    const unsigned co = (unsigned)(32 * s + i16) * 2;
    gld_u16<0>(plr[0], co, p.lre); gld_u16<32>(plr[1], co, p.lre);
    gld_u16<0>(pli[0], co, p.lim); gld_u16<32>(pli[1], co, p.lim);
    gld_u16<0>(pdt[0], co, p.logdt); gld_u16<32>(pdt[1], co, p.logdt);
    {
      u64 pc;
      asm volatile("s_getpc_b64 %0" : "=s"(pc));
      const int ci = r * 4 + w;
      const char* cb = (const char*)(pc & ~255ull) + (ci < CODEPF ? ci : 0) * 1024;
      asm volatile("global_load_dword %0, %1, %2" : "=v"(cdum) : "v"((unsigned)l * 16), "s"(cb) : "memory");
    }
#pragma unroll
    for (int k = 0; k < 3; k++) {
      int row = bid * 11 + w + 4 * k;
      if (w + 4 * k >= 11) row = bid * 11;
      const char* src = row < 1792 ? p.w2 + (size_t)row * 1024 : (row < 2304 ? p.cre + (size_t)(row - 1792) * 1024 : p.cim + (size_t)(row - 2304) * 1024);
      asm volatile("global_load_dword %0, %1, %2" : "+v"(cdum) : "v"((unsigned)l * 16), "s"(src) : "memory");
    }
  }
  unsigned vx0, vx1, vu, vwA, vwB;
  {
    const int r8 = l >> 3, c8 = l & 7;
    const int tok0 = 8 * (r8 >> 2) + (r8 & 3);
    vx0 = (unsigned)(tok0 * 1024 + ((c8 ^ (r8 >> 1)) & 7) * 16);
    vx1 = (unsigned)((tok0 + 16) * 1024 + ((c8 ^ (4 + (r8 >> 1))) & 7) * 16);
    const int ch = 8 * w + r8, iu = 8 * (w & 1) + r8;
    vu = (unsigned)(ch * 1024 + ((c8 ^ (iu >> 1)) & 7) * 16);
    const int winoff = 448 * (s >> 1) + (odd ? 192 : 0);
    vwA = (unsigned)((16 * w + q) * 3584 + winoff + ((((i16 >> 1) ^ q) << 1) | (i16 & 1)) * 16);
    vwB = (unsigned)((16 * w + 8 + q) * 3584 + winoff + ((((i16 >> 1) ^ (q | 4)) << 1) | (i16 & 1)) * 16);
  }
  const char* xb = p.x + (size_t)(128 * j + 32 * (w >> 1) + 4 * (w & 1)) * 1024;
  const char* ub0 = p.bre + (size_t)(32 * s) * 1024;
  const char* ub1 = p.bim + (size_t)(32 * s) * 1024;
#define PIECE(N, I)                                                                                  \
  {                                                                                                  \
    constexpr int h_ = (N) / 3, n_ = (N) % 3;                                                        \
    const int kq_ = ((I) + rot) & 7;                                                                 \
    const unsigned d_ = ldsb + w * 3072 + ((I) & 3) * STG + h_ * 12288 + n_ * 1024;                  \
    if (n_ == 0) dma1(d_, xb + h_ * 65536 + kq_ * 128, vx0);                                         \
    if (n_ == 1) dma1(d_, xb + h_ * 65536 + kq_ * 128, vx1);                                         \
    if (n_ == 2) dma1(d_, (h_ ? ub1 : ub0) + kq_ * 128, vu);                                         \
  }
#define PALL(I) PIECE(0, I) PIECE(1, I) PIECE(2, I) PIECE(3, I) PIECE(4, I) PIECE(5, I)
#define W1P(C, n) dma1(ldsb + L_W1A + ((C) & 3) * 16384 + (16 * w + 4 * (n)) * 256,          \
                       p.w1 + (size_t)((((C) + rot) & 7) * 64 + ((n) & 1) * 4) * 3584, ((n) & 2) ? vwB : vwA)
  if (role == 1) {
    PALL(0)
    __builtin_amdgcn_s_barrier();
    PALL(1)
  } else {
    __builtin_amdgcn_s_barrier();
  }
  asm volatile("s_waitcnt lgkmcnt(0)" : "+s"(ep0) :: "memory");
  const u64 epoch = ep0 + 1ull;
  const unsigned tlo = (unsigned)(epoch & 0xffu), thi = (unsigned)(epoch >> 8);

  const unsigned sw0 = (unsigned)((i16 & 7) * 128 + (((0 + q) ^ (i16 >> 1)) & 7) * 16);
  const unsigned sw1 = (unsigned)((i16 & 7) * 128 + (((4 + q) ^ (i16 >> 1)) & 7) * 16);
  const unsigned xo = (unsigned)(2 * w * 3072 + (i16 >> 3) * 1024);
  const unsigned tro = (unsigned)((8 * q + ta) * 256 + 8 * tp);
  const int trkey = ta | ((q & 1) << 2);
  const unsigned zb = (unsigned)(2 * j + (w >> 1)) * HP_BYTES + (unsigned)l * 16u;
  const char* ab = p.ws + WS_Z + (size_t)hp2 * HP_BYTES;
  const unsigned avoff = (unsigned)l * 16;
  const int rot2 = ROT2 == 1 ? (hp2 & 3) : ROT2 == 2 ? (ct & 3) : 0;

  if (role == 0) {
    if (APRIO) __builtin_amdgcn_s_setprio(APRIO);
    asm volatile("s_waitcnt vmcnt(%8)" : "+v"(plr[0]), "+v"(plr[1]), "+v"(pli[0]), "+v"(pli[1]), "+v"(pdt[0]), "+v"(pdt[1]), "+v"(xr[0]), "+v"(xr[1]) : "i"((CODEPF ? 1 : 0) + (MWARM ? 3 : 0)) : "memory");
#pragma unroll
    for (int n = 0; n < 2; n++) {
      float v[8];
#pragma unroll
      for (int e = 0; e < 4; e++) { v[2 * e] = bf2f(xr[n][e] & 0xffffu); v[2 * e + 1] = __builtin_bit_cast(float, xr[n][e] & 0xffff0000u); }
      const float mean = wave_sum(((v[0] + v[1]) + (v[2] + v[3])) + ((v[4] + v[5]) + (v[6] + v[7]))) * (1.f / 512.f);
      float sq = 0.f;
#pragma unroll
      for (int e = 0; e < 8; e++) { const float d = v[e] - mean; sq = fmaf(d, d, sq); }
      const float rstd = rsqrtf(wave_sum(sq) * (1.f / 512.f) + 1e-5f);
      if (l == 0) *(LAS f32x2*)(lds + LDS_BYTES - 64 + (2 * w + n) * 8) = f32x2{mean, rstd};
    }
    wait_lgkm();
    cpx a1[2], cc[2], a8[2], pw[2], a128[2];
#pragma unroll
    for (int nb = 0; nb < 2; nb++) {
      const float lr = bf2f(plr[nb]), li = bf2f(pli[nb]);
      const float dtf = expf(bf2f(pdt[nb]));
      const float dt = bf2f(pk_bf16(dtf, 0.f) & 0xffffu);
      const float e = expf(dt * lr);
      float sn, cs;
      sincosf(dt * li, &sn, &cs);
      cpx a = {e * cs, e * sn};
      a1[nb] = a;
      const float nr = a.r - 1.f, ni = a.i, den = lr * lr + li * li;
      cc[nb] = {2.f * (nr * lr + ni * li) / den, 2.f * (ni * lr - nr * li) / den};
      cpx t = cmul(a, a); t = cmul(t, t); t = cmul(t, t);
      a8[nb] = t;
      cpx a16 = cmul(t, t), a32 = cmul(a16, a16), a64 = cmul(a32, a32);
      a128[nb] = cmul(a64, a64);
      cpx one = {1.f, 0.f};
      cpx pq = cmul(csel(q & 1, t, one), csel(q & 2, a16, one));
      cpx pwv = cmul(csel(w & 1, a32, one), csel(w & 2, a64, one));
      pw[nb] = cmul(pq, pwv);
    }
#pragma unroll
    for (int nb = 0; nb < 2; nb++)
      asm volatile("" : "+v"(a1[nb].r), "+v"(a1[nb].i), "+v"(cc[nb].r), "+v"(cc[nb].i), "+v"(a8[nb].r), "+v"(a8[nb].i), "+v"(pw[nb].r),
                   "+v"(pw[nb].i), "+v"(a128[nb].r), "+v"(a128[nb].i));
    STAMP(1);
    v4u cf[8][4];
    const unsigned cvoff = (unsigned)((64 * ct + (l >> 2)) * 1024 + (l & 3) * 16);
#define CLOAD(K0)                                                                         \
  _Pragma("unroll") for (int k = K0; k < K0 + 4; k++) {                                   \
    const int kr = (k + 2 * rot2) & 7;                                                    \
    const int sidx = 4 * w + 2 * (kr >> 2) + (kr & 1);                                    \
    const char* cb = (((kr >> 1) & 1) ? p.cim : p.cre) + sidx * 64;                       \
    _Pragma("unroll") for (int n = 0; n < 4; n++) gld16<0>(cf[k][n], cvoff, cb + n * 16384); \
    if (CPACE) __builtin_amdgcn_s_sleep(CPACE);                                           \
  }
#define CWAIT(K0, N)                                                                      \
  asm volatile("s_waitcnt vmcnt(%16)" : "+v"(cf[K0][0]), "+v"(cf[K0][1]), "+v"(cf[K0][2]), "+v"(cf[K0][3]), "+v"(cf[K0 + 1][0]),   \
               "+v"(cf[K0 + 1][1]), "+v"(cf[K0 + 1][2]), "+v"(cf[K0 + 1][3]), "+v"(cf[K0 + 2][0]), "+v"(cf[K0 + 2][1]),          \
               "+v"(cf[K0 + 2][2]), "+v"(cf[K0 + 2][3]), "+v"(cf[K0 + 3][0]), "+v"(cf[K0 + 3][1]), "+v"(cf[K0 + 3][2]),          \
               "+v"(cf[K0 + 3][3]) : "i"(N) : "memory");
#define CXPOSE(K0)                                                                        \
  _Pragma("unroll") for (int k = K0; k < K0 + 4; k++) {                                   \
    _Pragma("unroll") for (int n = 0; n < 4; n++) *(LAS v4u*)(lp(cwa) + n * 1024) = cf[k][n]; \
    _Pragma("unroll") for (int n = 0; n < 4; n++) cf[k][n] = lds_rd16(lp(cra) + n * 1024);  \
  }
    v2u sg;
    const unsigned sgoff = (unsigned)l * 8;
    const char* sgb = p.ws + WS_SG + (size_t)(128 * j + 32 * w) * 16;
    f32x4 au[2][2][2], asu;
#pragma unroll
    for (int b = 0; b < 2; b++)
#pragma unroll
      for (int nb = 0; nb < 2; nb++) { au[b][nb][0] = f32x4{0, 0, 0, 0}; au[b][nb][1] = f32x4{0, 0, 0, 0}; }
    asu = f32x4{0, 0, 0, 0};
    {
      const unsigned uo = (unsigned)((i16 >> 3) * 3072 + 2048);
      const int gs = (w & 1) * 2 + (w >> 1);
      unsigned cur = 0;
      v4u xd[2][2], wd[2][4], sd[2];
#pragma unroll
      for (int t = 0; t < 2; t++) xd[t][0] = xd[t][1] = wd[t][0] = wd[t][1] = wd[t][2] = wd[t][3] = sd[t] = v4u{0, 0, 0, 0};
#define A_STEP(RD, T, PX, PU, PS)                                                                     \
  {                                                                                                   \
    _Pragma("unroll") for (int g = 0; g < 4; g++) {                                                   \
      au[0][g & 1][g >> 1] = mfma16(xd[T][0], wd[T][g], au[0][g & 1][g >> 1]);                        \
      au[1][g & 1][g >> 1] = mfma16(xd[T][1], wd[T][g], au[1][g & 1][g >> 1]);                        \
      if (RD) wd[T][g] = lds_rd16(PU + g * 6144);                                                     \
      SB0();                                                                                          \
    }                                                                                                 \
    asu = mfma16(ones, sd[T], asu);                                                                   \
    if (RD) { xd[T][0] = lds_rd16(PX); xd[T][1] = lds_rd16(PX + 3072); sd[T] = lds_rd16(PS); }        \
    SB0();                                                                                            \
  }
#pragma unroll 1
      for (int it = 0; it < 8; it++) {
        const unsigned b0 = ldsb + cur;
        LAS char* px0 = lp(opaque(b0 + xo + sw0)); LAS char* px1 = lp(opaque(b0 + xo + sw1));
        LAS char* pu0 = lp(opaque(b0 + uo + sw0)); LAS char* pu1 = lp(opaque(b0 + uo + sw1));
        LAS char* ps0 = lp(opaque(b0 + uo + sw0 + gs * 6144)); LAS char* ps1 = lp(opaque(b0 + uo + sw1 + gs * 6144));
        cur = cur >= 3 * STG ? 0 : cur + STG;
        if (ALGKM) wait_lgkm();
        asm volatile("" ::: "memory");
        __builtin_amdgcn_s_barrier();
        asm volatile("" ::: "memory");
        if (it == 0 && w == 0 && l < 16)
          __hip_atomic_store((GAS u64*)(p.ws + WS_SG) + (size_t)(128 * j + 8 * s) * 2 + l,
                             mk_gran(epoch, *(LAS float*)(lds + LDS_BYTES - 64 + l * 4)), RLX_AGENT);
        SB0();
        A_STEP(true, 0, px0, pu0, ps0)
        A_STEP(true, 1, px1, pu1, ps1)
      }
      gld8_sc1(sg, sgoff, sgb);
      A_STEP(false, 0, lds, lds, lds)
      A_STEP(false, 1, lds, lds, lds)
#undef A_STEP
    }
    STAMP(2);
    {
      asm volatile("s_waitcnt vmcnt(0)" : "+v"(sg), "+v"(cdum) :: "memory");
      if (tid < 16) ((LAS unsigned*)(lds + L_CNT))[tid] = 0u;
      unsigned spins = 0;
      while (!wall(sg.y == thi && (sg.x >> 24) == tlo)) {
        if (++spins > (1u << 22)) break;
        __builtin_amdgcn_s_sleep(1);
        gld8_sc1(sg, sgoff, sgb);
        asm volatile("s_waitcnt vmcnt(0)" : "+v"(sg), "+v"(cdum) :: "memory");
      }
      const int tk = l >> 1;
      ((LAS unsigned*)(lds + L_STAT))[(w * 32 + ((tk >> 2) & 1) * 16 + ((tk & 3) | ((tk >> 3) << 2))) * 2 + (l & 1)] = sg.x << 8;
      if (q == 0) ((LAS float*)(lds + L_SU))[w * 16 + i16] = asu[0];
    }
    bar();
    STAMP(3);
    cpx u[2][8];
    {
      f32x2 stt[2][4];
#pragma unroll
      for (int b = 0; b < 2; b++) {
        f32x4 s0 = *(LAS f32x4*)(lds + L_STAT + (w * 32 + b * 16 + 4 * q) * 8);
        f32x4 s1 = *(LAS f32x4*)(lds + L_STAT + (w * 32 + b * 16 + 4 * q) * 8 + 16);
        stt[b][0] = f32x2{s0[0], s0[1]}; stt[b][1] = f32x2{s0[2], s0[3]};
        stt[b][2] = f32x2{s1[0], s1[1]}; stt[b][3] = f32x2{s1[2], s1[3]};
      }
#pragma unroll
      for (int nb = 0; nb < 2; nb++) {
        const float sre = ((LAS float*)(lds + L_SU))[(nb * 2 + 0) * 16 + i16];
        const float sim = ((LAS float*)(lds + L_SU))[(nb * 2 + 1) * 16 + i16];
        cpx h = {0.f, 0.f};
#pragma unroll
        for (int k = 0; k < 8; k++) {
          const int b = k >> 2, rg = k & 3;
          const float m = stt[b][rg][0], rr = stt[b][rg][1];
          u[nb][k].r = fmaf(-m, sre, au[b][nb][0][rg]) * rr;
          u[nb][k].i = fmaf(-m, sim, au[b][nb][1][rg]) * rr;
          h = cfma(a1[nb], h, u[nb][k]);
        }
        *(LAS f32x2*)(lds + L_E + ((nb * 16 + i16) * 16 + 4 * w + q) * 8) = f32x2{h.r, h.i};
      }
    }
    cnt_arrive(lds, C_E);
    cnt_wait(lds, C_E, 4);
    cpx cl[2];
    {
      cpx c[2] = {{0.f, 0.f}, {0.f, 0.f}};
      LAS f32x4* e4a = (LAS f32x4*)(lds + L_E + i16 * 128);
      LAS f32x4* e4b = (LAS f32x4*)(lds + L_E + (16 + i16) * 128);
#pragma unroll 1
      for (int k = 0; k < 2 * w; k++) {
        f32x4 ea = e4a[k], eb = e4b[k];
        c[0] = cfma(a8[0], c[0], cpx{ea[0], ea[1]}); c[1] = cfma(a8[1], c[1], cpx{eb[0], eb[1]});
        c[0] = cfma(a8[0], c[0], cpx{ea[2], ea[3]}); c[1] = cfma(a8[1], c[1], cpx{eb[2], eb[3]});
      }
      cpx tot[2];
#pragma unroll
      for (int nb = 0; nb < 2; nb++) {
        LAS f32x4* e4 = nb ? e4b : e4a;
        f32x4 e0 = e4[2 * w], e1 = e4[2 * w + 1];
        cpx c1 = cfma(a8[nb], c[nb], cpx{e0[0], e0[1]});
        cpx c2 = cfma(a8[nb], c1, cpx{e0[2], e0[3]});
        cpx c3 = cfma(a8[nb], c2, cpx{e1[0], e1[1]});
        tot[nb] = cfma(a8[nb], c3, cpx{e1[2], e1[3]});
        cl[nb] = csel(q & 2, csel(q & 1, c3, c2), csel(q & 1, c1, c[nb]));
      }
      if (w == 3 && j < 15) {
        const cpx t = (q & 2) ? tot[1] : tot[0];
        const float v = (q & 1) ? t.i : t.r;
        const u64 g = mk_gran(epoch, v);
        GAS u64* gp = (GAS u64*)(p.ws + WS_GRAN + (size_t)(j * 16 + s) * 512) + (((q >> 1) * 16 + i16) * 2 + (q & 1));
        __hip_atomic_store(gp, g, RLX_AGENT);
      }
    }
    STAMP(4);
    const unsigned cwa = opaque(ldsb + L_CST + w * 4096 + (l >> 2) * 64 + (((l & 3) ^ ((-(l >> 4)) & 3)) * 16));
    const unsigned cra = opaque(ldsb + L_CST + w * 4096 + i16 * 64 + ((q ^ ((-(i16 >> 2)) & 3)) * 16));
    unsigned wdum = 0;
    CLOAD(0)
    {
      const unsigned wv2 = (unsigned)((l >> 5) * 1024 + (l & 31) * 16);
#pragma unroll 1
      for (int k = 0; k < NWARM; k++)
        asm volatile("global_load_dword %0, %1, %2" : "+v"(wdum) : "v"(wv2), "s"(p.w2 + (size_t)((k * 32 + r) * 4 + w) * 2048 + (xcd & 1) * 512) : "memory");
    }
    CWAIT(0, NWARM)
    CXPOSE(0)
    if (w < 2) {
      const char* gb = p.ws + WS_GRAN + (size_t)s * 512 + w * 256;
      unsigned spins = 0;
      if (j > 0) {
        GAS u64* pp = (GAS u64*)(gb + (size_t)(l < j ? l : 0) * 8192 + 248);
        while (!wall((__hip_atomic_load(pp, RLX_AGENT) >> 24) == (epoch & 0xffffffffffull))) {
          if (++spins > (1u << 20)) break;
          __builtin_amdgcn_s_sleep(2);
        }
      }
      v4u gr[4];
      for (;;) {
#pragma unroll
        for (int m = 0; m < 4; m++) {
          const int pn = (4 * m + q) < j ? 4 * m + q : 0;
          gld16_sc1<0>(gr[m], (unsigned)(pn * 8192 + i16 * 16), gb);
        }
        wait_vm4<0>(gr[0], gr[1], gr[2], gr[3]);
        bool ok = true;
#pragma unroll
        for (int m = 0; m < 4; m++) {
          const bool need = (4 * m + q) < j;
          const bool good = (gr[m].y == thi) && ((gr[m].x >> 24) == tlo) && (gr[m].w == thi) && ((gr[m].z >> 24) == tlo);
          ok = ok && (good || !need);
        }
        if (wall(ok)) break;
        if (++spins > (1u << 20)) break;
        __builtin_amdgcn_s_sleep(2);
      }
      const cpx b128 = csel(w != 0, a128[1], a128[0]);
      const cpx a256 = cmul(b128, b128);
      const cpx a512 = cmul(a256, a256);
      cpx acc = {0.f, 0.f};
#pragma unroll
      for (int m = 0; m < 4; m++) {
        const bool need = (4 * m + q) < j;
        cpx e = {__builtin_bit_cast(float, gr[m].x << 8), __builtin_bit_cast(float, gr[m].z << 8)};
        acc = csel(need, cfma(a512, acc, e), acc);
      }
      const int dd = (j - 1 - q) & 3;
      cpx one = {1.f, 0.f};
      cpx fz = cmul(csel(dd & 1, b128, one), csel(dd & 2, a256, one));
      acc = cmul(acc, fz);
      *(LAS f32x2*)(lds + L_HIN + ((w * 16 + i16) * 4 + q) * 8) = f32x2{acc.r, acc.i};
      cnt_arrive(lds, C_HIN);
    }
    cnt_wait(lds, C_HIN, 2);
    STAMP(5);
    {
      LAS char* ht = lds + L_HT + w * 2048;
      unsigned hpk[2][2][2][2];
#pragma unroll
      for (int nb = 0; nb < 2; nb++) {
        const f32x4 h01 = *(LAS f32x4*)(lds + L_HIN + (nb * 16 + i16) * 32);
        const f32x4 h23 = *(LAS f32x4*)(lds + L_HIN + (nb * 16 + i16) * 32 + 16);
        const cpx acc = {(h01[0] + h01[2]) + (h23[0] + h23[2]), (h01[1] + h01[3]) + (h23[1] + h23[3])};
        cpx h = cfma(pw[nb], acc, cl[nb]);
        float orr[8], oi[8];
#pragma unroll
        for (int k = 0; k < 8; k++) {
          h = cfma(a1[nb], h, u[nb][k]);
          orr[k] = cc[nb].r * h.r - cc[nb].i * h.i;
          oi[k] = -(cc[nb].r * h.i + cc[nb].i * h.r);
        }
#pragma unroll
        for (int b = 0; b < 2; b++) {
          hpk[nb][0][b][0] = pk_bf16(orr[4 * b], orr[4 * b + 1]); hpk[nb][0][b][1] = pk_bf16(orr[4 * b + 2], orr[4 * b + 3]);
          hpk[nb][1][b][0] = pk_bf16(oi[4 * b], oi[4 * b + 1]);   hpk[nb][1][b][1] = pk_bf16(oi[4 * b + 2], oi[4 * b + 3]);
        }
      }
      const unsigned zy = zb + Y_OFF + (unsigned)(4 * (s >> 1) + (s & 1)) * 4096u + (unsigned)(2 * (w & 1)) * 1024u;
#pragma unroll
      for (int b = 0; b < 2; b++) {
#pragma unroll
        for (int nb = 0; nb < 2; nb++)
#pragma unroll
          for (int part = 0; part < 2; part++)
            *(LAS v2u*)(ht + (part * 32 + nb * 16 + i16) * 32 + q * 8) = v2u{hpk[nb][part][b][0], hpk[nb][part][b][1]};
#pragma unroll
        for (int part = 0; part < 2; part++) {
          LAS char* rp = ht + (part * 32 + 8 * q + ta) * 32 + tp * 8;
          v4u v = lds_tr(rp, rp + 4 * 32);
          st16(rs, (unsigned)WS_Z + zy + (unsigned)part * 8192u + b * 1024u, v);
        }
      }
    }
    STAMP(6);
    asm volatile("s_waitcnt vmcnt(0)" : "+v"(wdum) :: "memory");
    if (cnt_arrive(lds, C_HS) == 3 && l < 16)
      __hip_atomic_store((GAS u64*)(p.ws + WS_FLAG) + (j * 16 + s) * 16 + l, epoch, RLX_AGENT);
    STAMP(7);
    CLOAD(4)
    CWAIT(4, 0)
    CXPOSE(4)
    cnt_arrive(lds, C_CX);
    STAMP(8);
    poll_flags(p.ws + WS_FLAG + (size_t)(j * 16 + 4 * w) * 128, l, epoch);
    STAMP(9);
    f32x4 acc[4][4];
#pragma unroll
    for (int a = 0; a < 4; a++)
#pragma unroll
      for (int b = 0; b < 4; b++) acc[a][b] = f32x4{0, 0, 0, 0};
    v4u af[3][4];
    const char* ay = ab + Y_OFF + (size_t)(8 * w) * 4096;
#define YL(K, i) gld16_sc1<(i) * 1024>(af[(K) % 3][i], avoff, ay + ((((K) + 2 * rot2) & 7) * 4096))
#define YST(K, tb)                                                                                  \
  {                                                                                                 \
    _Pragma("unroll") for (int nb = 0; nb < 4; nb++) acc[nb][tb] = mfma16(cf[K][nb], af[(K) % 3][tb], acc[nb][tb]); \
    SB0();                                                                                          \
    if ((K) + 2 < 8) YL((K) + 2, tb);                                                               \
    SB0();                                                                                          \
  }
#define YS(K)                                                                                       \
  {                                                                                                 \
    wait_vm4<((K) < 7 ? 4 : 0)>(af[(K) % 3][0], af[(K) % 3][1], af[(K) % 3][2], af[(K) % 3][3]);    \
    YST(K, 0) YST(K, 1) YST(K, 2) YST(K, 3)                                                         \
  }
    YL(0, 0); YL(0, 1); YL(0, 2); YL(0, 3); YL(1, 0); YL(1, 1); YL(1, 2); YL(1, 3);
    YS(0) YS(1) YS(2) YS(3) YS(4) YS(5) YS(6) YS(7)
#undef YL
#undef YS
#undef YST
    STAMP(10);
    cnt_wait(lds, C_CX, 4);
    cnt_wait(lds, C_B2, 4);
    {
      LAS char* yb = lds + ((w & 1) ? L_YB1 : L_YB0);
      if (w < 2) cnt_wait(lds, C_Y1 + (w & 1), 1);
#pragma unroll
      for (int tb = 0; tb < 4; tb++) {
        const int tk = 32 * (tb >> 1) + 8 * (i16 >> 2) + 4 * (tb & 1) + (i16 & 3);
#pragma unroll
        for (int nb = 0; nb < 4; nb++) {
          LAS f32x4* d = (LAS f32x4*)(yb + tk * 256 + (((4 * nb + q) ^ i16) & 15) * 16);
          if (w < 2) *d = *d + acc[nb][tb]; else *d = acc[nb][tb];
        }
      }
      if (w >= 2) cnt_arrive(lds, C_Y1 + (w & 1));
    }
    STAMP(11);
  } else {
    v4u xf[2][16];
    f32x4 ah[7][2];
#pragma unroll
    for (int mb = 0; mb < 7; mb++) { ah[mb][0] = f32x4{0, 0, 0, 0}; ah[mb][1] = f32x4{0, 0, 0, 0}; }
    unsigned pt[7];
#pragma unroll
    for (int mb = 0; mb < 7; mb++) pt[mb] = opaque(ldsb + L_W1A + tro + (unsigned)(((mb + odd) ^ trkey) * 32));
    const int m1 = w < 3 ? w + 4 : 6;
    const unsigned hq0 = opaque(ldsb + L_W1A + tro + (unsigned)(((w + odd) ^ trkey) * 32));
    const unsigned hq1 = opaque(ldsb + L_W1A + tro + (unsigned)(((m1 + odd) ^ trkey) * 32));
    f32x4 sh0 = {0, 0, 0, 0}, sh1 = {0, 0, 0, 0};
    {
      LAS char* px0 = lp(opaque(ldsb + xo + sw0)); LAS char* px1 = lp(opaque(ldsb + xo + sw1));
#define L1IT(I, NW)                                                                                   \
  {                                                                                                   \
    wait_vm<NW>();                                                                                    \
    __builtin_amdgcn_s_barrier();                                                                     \
    asm volatile("" ::: "memory");                                                                    \
    xf[0][2 * (I)] = lds_rd16(px0 + ((I) & 3) * STG); xf[1][2 * (I)] = lds_rd16(px0 + ((I) & 3) * STG + 3072); \
    xf[0][2 * (I) + 1] = lds_rd16(px1 + ((I) & 3) * STG); xf[1][2 * (I) + 1] = lds_rd16(px1 + ((I) & 3) * STG + 3072); \
    SB0();                                                                                            \
    if ((I) < 3 || (I) == 7) {                                                                        \
      if ((I) < 6) { PALL((I) + 2) }                                                                  \
      W1P(I, 0); W1P(I, 1); W1P(I, 2); W1P(I, 3);                                                     \
    } else {                                                                                          \
      constexpr int c_ = (I) - 3;                                                                     \
      v4u wa[7], wb_[7], h0[2], h1[2];                                                                \
      _Pragma("unroll") for (int mb = 0; mb < 7; mb++) wa[mb] = lds_tr(lp(pt[mb]) + c_ * 16384, lp(pt[mb]) + c_ * 16384 + 1024); \
      SB0();                                                                                          \
      if ((I) < 6) { PIECE(0, (I) + 2) PIECE(1, (I) + 2) PIECE(2, (I) + 2) }                          \
      SB0();                                                                                          \
      _Pragma("unroll") for (int mb = 0; mb < 7; mb++) {                                              \
        ah[mb][0] = mfma16(wa[mb], xf[0][2 * c_], ah[mb][0]);                                         \
        ah[mb][1] = mfma16(wa[mb], xf[1][2 * c_], ah[mb][1]);                                         \
        wb_[mb] = lds_tr(lp(pt[mb]) + c_ * 16384 + 8192, lp(pt[mb]) + c_ * 16384 + 9216);             \
        if ((I) < 6) { if (mb == 1) PIECE(3, (I) + 2) if (mb == 3) PIECE(4, (I) + 2) if (mb == 5) PIECE(5, (I) + 2) } \
        SB0();                                                                                        \
      }                                                                                               \
      _Pragma("unroll") for (int mb = 0; mb < 7; mb++) {                                              \
        ah[mb][0] = mfma16(wb_[mb], xf[0][2 * c_ + 1], ah[mb][0]);                                    \
        ah[mb][1] = mfma16(wb_[mb], xf[1][2 * c_ + 1], ah[mb][1]);                                    \
        if (mb == 0) { h0[0] = lds_tr(lp(hq0) + c_ * 16384, lp(hq0) + c_ * 16384 + 1024); h1[0] = lds_tr(lp(hq1) + c_ * 16384, lp(hq1) + c_ * 16384 + 1024); } \
        if (mb == 1) { h0[1] = lds_tr(lp(hq0) + c_ * 16384 + 8192, lp(hq0) + c_ * 16384 + 9216); h1[1] = lds_tr(lp(hq1) + c_ * 16384 + 8192, lp(hq1) + c_ * 16384 + 9216); } \
        if (mb == 0) W1P(I, 0); if (mb == 2) W1P(I, 1); if (mb == 4) W1P(I, 2); if (mb == 6) W1P(I, 3); \
        SB0();                                                                                        \
      }                                                                                               \
      sh0 = mfma16(h0[0], ones, sh0); sh1 = mfma16(h1[0], ones, sh1);                                 \
      sh0 = mfma16(h0[1], ones, sh0); sh1 = mfma16(h1[1], ones, sh1);                                 \
    }                                                                                                 \
    wait_lgkm();                                                                                      \
  }
      L1IT(0, 6) L1IT(1, 10) L1IT(2, 14) STAMP(15); L1IT(3, 14) L1IT(4, 14) L1IT(5, 14) L1IT(6, 14) L1IT(7, 8)
#undef L1IT
    }
    STAMP(2);
    wait_vm<8>();
    bar();
    STAMP(3);
    const unsigned ring = ldsb + L_RING + w * RING;
    unsigned fv;
    {
      const int row = l >> 3, c16p = l & 7;
      fv = (unsigned)(row * 1024 + ct * 128 + ((((c16p >> 1) ^ ((row >> 1) & 3)) << 1) | (c16p & 1)) * 16);
    }
    const char* w2b = p.w2 + (size_t)(448 * w) * 1024;
#define WP(N, n) dma1(ring + ((N) & 1) * 14336 + (n) * 1024, w2b + (size_t)(((N) + rot2) & 3) * (112 * 1024) + (n) * 8192, fv)
    {
      v4u wd[7];
      static_for(std::make_integer_sequence<int, 7>{}, [&](auto NC) __attribute__((always_inline)) NOPK {
        constexpr int n = decltype(NC)::value, T2 = hv_t(n), m2 = hv_m(n);
        wd[n] = lds_tr(lp(pt[m2]) + (T2 & 7) * 8192, lp(pt[m2]) + (T2 & 7) * 8192 + 1024);
      });
      const unsigned zf = zb + (unsigned)s * 14336u;
      float nm[2], r2[2];
      v4u hs0, hs1;
      unsigned pkd[7][2][2];
      auto epi = [&](auto MB, auto BB) __attribute__((always_inline)) NOPK {
        constexpr int mb = decltype(MB)::value, b = decltype(BB)::value;
        const f32x4 sh = *(LAS f32x4*)(lds + L_SH + (mb * 16 + 4 * q) * 4);
        float y[4];
#pragma unroll
        for (int e = 0; e < 4; e++) {
          const float d = fmaf(sh[e], nm[b], ah[mb][b][e]);
          y[e] = d * __builtin_amdgcn_rcpf(1.f + __builtin_amdgcn_exp2f(d * r2[b]));
        }
        pkd[mb][b][0] = pk_bf16(y[0], y[1]); pkd[mb][b][1] = pk_bf16(y[2], y[3]);
        if constexpr ((mb & 1) == 1) {
          v4u v = {pkd[mb - 1][b][0], pkd[mb - 1][b][1], pkd[mb][b][0], pkd[mb][b][1]};
          st16(rs, (unsigned)WS_Z + zf + (unsigned)(mb >> 1) * 4096u + (unsigned)(2 * (w & 1) + b) * 1024u, v);
        }
      };
      static_for(std::make_integer_sequence<int, 56>{}, [&](auto NC) __attribute__((always_inline)) NOPK {
        constexpr int n = decltype(NC)::value, T = hv_t(n), mb = hv_m(n), sl = n % 7;
        if constexpr (n == 9) {
          wait_vm<0>();
          cnt_arrive(lds, C_W);
          cnt_wait(lds, C_W, 4);
        }
        if constexpr (n < 32 && (n & 3) == 0) {
          hs0 = lds_tr(lp(hq0) + (n / 4) * 8192, lp(hq0) + (n / 4) * 8192 + 1024);
          hs1 = lds_tr(lp(hq1) + (n / 4) * 8192, lp(hq1) + (n / 4) * 8192 + 1024);
        }
        if constexpr (n == 32) {
          if (i16 == 0) {
            *(LAS f32x4*)(lds + L_SH + (w * 16 + 4 * q) * 4) = sh0;
            if (w < 3) *(LAS f32x4*)(lds + L_SH + (m1 * 16 + 4 * q) * 4) = sh1;
          }
          cnt_arrive(lds, C_SH);
          cnt_wait(lds, C_SH, 4);
          const f32x2 s0 = *(LAS f32x2*)(lds + L_STAT + (w * 32 + i16) * 8), s1 = *(LAS f32x2*)(lds + L_STAT + (w * 32 + 16 + i16) * 8);
          nm[0] = -s0[0]; nm[1] = -s1[0]; r2[0] = s0[1] * -1.44269504088896f; r2[1] = s1[1] * -1.44269504088896f;
        }
        ah[mb][0] = mfma16(wd[sl], xf[0][T], ah[mb][0]);
        ah[mb][1] = mfma16(wd[sl], xf[1][T], ah[mb][1]);
        if constexpr (n < 32 && (n & 3) == 3) { sh0 = mfma16(hs0, ones, sh0); sh1 = mfma16(hs1, ones, sh1); }
        if constexpr (n + 7 < 56) {
          constexpr int T2 = hv_t(n + 7), m2 = hv_m(n + 7);
          wd[sl] = lds_tr(lp(pt[m2]) + (T2 & 7) * 8192, lp(pt[m2]) + (T2 & 7) * 8192 + 1024);
        }
        if constexpr (n >= 32 && (n - 32) % 3 == 1) {
          constexpr int u = (n - 32) / 3;
          epi(std::integral_constant<int, (u >> 2) * 2 + (u & 1)>{}, std::integral_constant<int, (u >> 1) & 1>{});
        }
        SB0();
      });
      STAMP(1);
      cnt_arrive(lds, C_B2);
      epi(std::integral_constant<int, 4>{}, std::integral_constant<int, 0>{});
      epi(std::integral_constant<int, 5>{}, std::integral_constant<int, 0>{});
      epi(std::integral_constant<int, 4>{}, std::integral_constant<int, 1>{});
      epi(std::integral_constant<int, 5>{}, std::integral_constant<int, 1>{});
      epi(std::integral_constant<int, 6>{}, std::integral_constant<int, 0>{});
      epi(std::integral_constant<int, 6>{}, std::integral_constant<int, 1>{});
      v4u v = {pkd[6][0][0], pkd[6][0][1], pkd[6][1][0], pkd[6][1][1]};
      st16(rs, (unsigned)WS_Z + zf + 12288u + (unsigned)(w & 1) * 1024u, v);
    }
    STAMP(4);
    wait_vm<0>();
    if (cnt_arrive(lds, C_FS) == 3 && l < 16)
      __hip_atomic_store((GAS u64*)(p.ws + WS_FLAG) + (256 + j * 16 + s) * 16 + l, epoch, RLX_AGENT);
    STAMP(5);
    cnt_wait(lds, C_B2, 4);
    cnt_wait(lds, C_CX, 4);
#pragma unroll
    for (int n = 0; n < 14; n++) WP(0, n);
    poll_flags(p.ws + WS_FLAG + (size_t)(256 + j * 16 + 4 * w) * 128, l, epoch);
    STAMP(6);
    f32x4 acc[4][4];
#pragma unroll
    for (int a = 0; a < 4; a++)
#pragma unroll
      for (int b = 0; b < 4; b++) acc[a][b] = f32x4{0, 0, 0, 0};
    v4u af[4][4];
    const char* afb = ab + (size_t)(4 * w) * 14336;
    unsigned ftr[4];
#pragma unroll
    for (int nb = 0; nb < 4; nb++) {
      const int row = 4 * q + ta;
      ftr[nb] = opaque(ring + (unsigned)(row * 128 + ((nb ^ ((row >> 1) & 3)) * 32) + tp * 8));
    }
#define ALD(U, i) gld16_sc1<(i) * 1024>(af[(U) & 3][i], avoff, afb + (size_t)((((U) >> 2) + rot2) & 3) * 14336 + ((U) & 3) * 4096)
#define ALDS(U, V, t)                             \
  {                                                                                                 \
    if ((V) < 16) {                                                                                 \
      if (0 * usz(U) / usz(V) == (t)) ALD(V, 0);                                                    \
      if (1 * usz(U) / usz(V) == (t)) ALD(V, 1);                                                    \
      if (usz(V) == 4 && 2 * usz(U) / usz(V) == (t)) ALD(V, 2);                                     \
      if (usz(V) == 4 && 3 * usz(U) / usz(V) == (t)) ALD(V, 3);                                     \
    }                                                                                               \
  }
#define WRD(U)                                           \
  {                                                                                                 \
    _Pragma("unroll") for (int nb = 0; nb < 4; nb++) {                                              \
      LAS char* b_ = lp(ftr[nb]) + (((U) >> 2) & 1) * 14336 + ((U) & 3) * 4096;                     \
      if (((U) & 3) == 3) { v2u t_ = lds_tr1(b_); wf[(U) & 1][nb].x = t_.x; wf[(U) & 1][nb].y = t_.y; } \
      else wf[(U) & 1][nb] = lds_tr(b_, b_ + 2048);                                                 \
    }                                                                                               \
  }
#define FSTEP(U, t)                                                                                 \
  {                                                                                                 \
    if (((U) & 3) < 3) {                                                                            \
      _Pragma("unroll") for (int nb = 0; nb < 4; nb++)                                              \
          acc[nb][t] = mfma16(wf[(U) & 1][nb], af[(U) & 3][t], acc[nb][t]);                         \
    } else {                                                                                        \
      _Pragma("unroll") for (int nb = 0; nb < 4; nb++) {                                            \
        const v2u wh_ = {wf[(U) & 1][nb].x, wf[(U) & 1][nb].y};                                     \
        acc[nb][(2 * (t)) & 3] = mfma16h(wh_, v2u{af[3][t].x, af[3][t].y}, acc[nb][(2 * (t)) & 3]); \
        acc[nb][(2 * (t) + 1) & 3] = mfma16h(wh_, v2u{af[3][t].z, af[3][t].w}, acc[nb][(2 * (t) + 1) & 3]); \
      }                                                                                             \
    }                                                                                               \
    SB0();                                                                                          \
    ALDS(U, (U) + FD, t)                                                                            \
    if ((U) < 12) WP(((U) >> 2) + 1, 4 * ((U) & 3) + (t));                                          \
    if ((t) == usz(U) - 2 && (U) < 15) {                                                            \
      if ((U) + 1 >= 4) wait_vm<f_cnt(false, (U) + 1, U, (t) + 1)>();                               \
      WRD((U) + 1)                                                                                  \
    }                                                                                               \
    SB0();                                                                                          \
  }
#define FU(U)                                                                                       \
  {                                                                                                 \
    if (((U) & 3) < 3) {                                                                            \
      wait_vm4<f_cnt(true, U, U, 0)>(af[(U) & 3][0], af[(U) & 3][1], af[(U) & 3][2], af[(U) & 3][3]); \
      FSTEP(U, 0) FSTEP(U, 1) FSTEP(U, 2) FSTEP(U, 3)                                               \
    } else {                                                                                        \
      asm volatile("s_waitcnt vmcnt(%2)" : "+v"(af[3][0]), "+v"(af[3][1]) : "i"(f_cnt(true, U, U, 0)) : "memory"); \
      FSTEP(U, 0) FSTEP(U, 1)                                                                       \
    }                                                                                               \
  }
    v4u wf[2][4];
    ALD(0, 0); ALD(0, 1); ALD(0, 2); ALD(0, 3);
    ALD(1, 0); ALD(1, 1); ALD(1, 2); ALD(1, 3);
    ALD(2, 0); ALD(2, 1); ALD(2, 2); ALD(2, 3);
    static_assert(FD == 3, "window");
    WRD(0)
    FU(0) STAMP(7); FU(1) FU(2) FU(3) STAMP(8); FU(4) FU(5) FU(6) FU(7) STAMP(9); FU(8) FU(9) FU(10) FU(11) STAMP(14); FU(12) FU(13) FU(14) FU(15)
#undef FU
#undef FSTEP
#undef WRD
#undef ALD
#undef ALDS
#undef WP
    STAMP(10);
    {
      LAS char* dst = lp(ring);
#pragma unroll
      for (int tb = 0; tb < 4; tb++) {
        const int tk = 32 * (tb >> 1) + 8 * (i16 >> 2) + 4 * (tb & 1) + (i16 & 3);
#pragma unroll
        for (int nb = 0; nb < 4; nb++)
          *(LAS f32x4*)(dst + tk * 256 + (((4 * nb + q) ^ i16) & 15) * 16) = acc[nb][tb];
      }
    }
    STAMP(11);
  }
  {
    const int ec = tid & 15, et = tid >> 4;
    v2u xe[2];
    const v2u dk = *(const GAS v2u*)(p.dskip + (64 * ct + 4 * ec) * 2);
#pragma unroll
    for (int n = 0; n < 2; n++)
      xe[n] = *(const GAS v2u*)(p.x + (size_t)(64 * hp2 + et + 32 * n) * 1024 + (64 * ct + 4 * ec) * 2);
    bar();
    STAMP(12);
    if (j == 15 && s == 0 && tid == 0)
      __hip_atomic_store((GAS u64*)(p.ws + WS_EPOCH), epoch, RLX_AGENT);
    const float dkf[4] = {bf2f(dk.x & 0xffff), bf2f(dk.x >> 16), bf2f(dk.y & 0xffff), bf2f(dk.y >> 16)};
#pragma unroll
    for (int n = 0; n < 2; n++) {
      const int tk = et + 32 * n;
      const int key = (tk & 3) | (((tk >> 3) & 3) << 2);
      const int o = tk * 256 + ((ec ^ key) & 15) * 16;
      LAS char* fb = lds + L_RING + o;
      const f32x4 f = (*(LAS f32x4*)(fb) + *(LAS f32x4*)(fb + RING)) + (*(LAS f32x4*)(fb + 2 * RING) + *(LAS f32x4*)(fb + 3 * RING));
      const f32x4 y = *(LAS f32x4*)(lds + L_YB0 + o) + *(LAS f32x4*)(lds + L_YB1 + o);
      const int tkp = 64 * (hp2 & 1) + tk;
      const f32x2 se = *(LAS f32x2*)(lds + L_STAT + ((tkp >> 5) * 32 + ((tkp >> 2) & 1) * 16 + key) * 8);
      const float xv[4] = {bf2f(xe[n].x & 0xffff), bf2f(xe[n].x >> 16), bf2f(xe[n].y & 0xffff), bf2f(xe[n].y >> 16)};
      f32x4 o4;
#pragma unroll
      for (int e = 0; e < 4; e++) {
        const float xn = (xv[e] - se[0]) * se[1];
        const float sv = fmaf(xn, dkf[e], y[e]);
        const float fe = f[e] * se[1];
        const float g = fe * __builtin_amdgcn_rcpf(1.f + __builtin_amdgcn_exp2f(fe * -1.44269504088896f));
        o4[e] = fmaf(sv, g, xv[e]);
      }
      *(GAS f32x4*)(p.out + (size_t)(64 * hp2 + tk) * 512 + 64 * ct + 4 * ec) = o4;
    }
  }
  STAMP(13);
  STAMP_OUT();
}

extern "C" void kernel_launch(void* const* d_in, const int* in_sizes, int n_in, void* d_out, int out_size, void* d_ws,
                              size_t ws_size, hipStream_t stream) {
  Params p;
  p.x = (const char*)d_in[0]; p.lre = (const char*)d_in[1]; p.lim = (const char*)d_in[2];
  p.bre = (const char*)d_in[3]; p.bim = (const char*)d_in[4]; p.cre = (const char*)d_in[5]; p.cim = (const char*)d_in[6];
  p.logdt = (const char*)d_in[7]; p.dskip = (const char*)d_in[8]; p.w1 = (const char*)d_in[10]; p.w2 = (const char*)d_in[11];
  p.out = (float*)d_out; p.ws = (char*)d_ws;
  (void)hipFuncSetAttribute((const void*)k_fused, hipFuncAttributeMaxDynamicSharedMemorySize, LDS_BYTES);
  k_fused<<<256, 512, LDS_BYTES, stream>>>(p);
}
```

```cpp
#include <hip/hip_runtime.h>
#include <hip/hip_bf16.h>
#include <type_traits>
#include <utility>

typedef unsigned v4u __attribute__((ext_vector_type(4)));
typedef unsigned v2u __attribute__((ext_vector_type(2)));
typedef float f32x4 __attribute__((ext_vector_type(4)));
typedef float f32x2 __attribute__((ext_vector_type(2)));
typedef short bf16x8 __attribute__((ext_vector_type(8)));
typedef short s16x4 __attribute__((ext_vector_type(4)));
typedef __bf16 bf16x2_t __attribute__((ext_vector_type(2)));
typedef unsigned long long u64;
#define LAS __attribute__((address_space(3)))
#define GAS __attribute__((address_space(1)))
#if defined(__HIP_DEVICE_COMPILE__)
#define NOPK __attribute__((target("no-packed-fp32-ops")))
#else
#define NOPK
#endif
#define DI __device__ __forceinline__ NOPK
#define RLX_AGENT __ATOMIC_RELAXED, __HIP_MEMORY_SCOPE_AGENT
#define RLX_WG __ATOMIC_RELAXED, __HIP_MEMORY_SCOPE_WORKGROUP

static constexpr unsigned HP_BYTES = 16 * 14336 + 32 * 4096;
static constexpr unsigned Y_OFF = 16 * 14336;
static constexpr size_t WS_Z = 0;
static constexpr size_t WS_Z_BYTES = (size_t)32 * HP_BYTES;
static constexpr size_t WS_GRAN = WS_Z + WS_Z_BYTES;
static constexpr size_t WS_SG = WS_GRAN + 16 * 16 * 512;
static constexpr size_t WS_FLAG = WS_SG + 2048 * 16;
static constexpr size_t WS_EPOCH = WS_FLAG + 2 * 16 * 16 * 128;
static constexpr size_t WS_END = WS_EPOCH + 128;

static constexpr int LDS_BYTES = 160 * 1024;
static constexpr int STG = 24576;
static constexpr int L_W1A = 96 * 1024;
static constexpr int L_W1B = 32 * 1024;
static constexpr int L_CST = 0;
static constexpr int L_YB1 = L_CST;
static constexpr int L_HT = 16 * 1024;
static constexpr int L_E = 24 * 1024;
static constexpr int L_KEEP = 28 * 1024;
static constexpr int L_YB0 = 32 * 1024;
static constexpr int L_RING = 48 * 1024;
static constexpr int L_STAT = L_KEEP;
static constexpr int L_SU = L_KEEP + 1024;
static constexpr int L_SH = L_KEEP + 1280;
static constexpr int L_CNT = L_KEEP + 1792;
static constexpr int RING = 28672;
static constexpr int L_HIN = L_KEEP + 2048;
enum { C_W = 9, C_SH = 10, C_B2 = 11, C_E = 0, C_CX = 1, C_HIN = 2, C_HS = 4, C_FS = 5, C_Y1 = 7 };
#define ROT 1
#define CIT 3
#define NOWARM 0
#define ALGKM 0
#define LATEC 1
#define VM0 0
#define CPACE 2
#define CODEPF 32
#define APRIO 2
#define NWARM 0
#define MWARM 1
#define ROT2 1

DI bool wall(bool x) { return __builtin_amdgcn_ballot_w64(!x) == 0ull; }
DI f32x4 mfma16(v4u a, v4u b, f32x4 c) {
  return __builtin_amdgcn_mfma_f32_16x16x32_bf16(__builtin_bit_cast(bf16x8, a), __builtin_bit_cast(bf16x8, b), c, 0, 0, 0);
}
DI unsigned pk_bf16(float lo, float hi) {
  f32x2 v = {lo, hi};
  return __builtin_bit_cast(unsigned, __builtin_convertvector(v, bf16x2_t));
}
DI unsigned pk_bf16v(f32x2 v) { return __builtin_bit_cast(unsigned, __builtin_convertvector(v, bf16x2_t)); }
DI float bf2f(unsigned u) { return __builtin_bit_cast(float, u << 16); }
DI unsigned opaque(unsigned v) { asm volatile("" : "+v"(v)); return v; }
DI LAS char* lp(unsigned a) { return (LAS char*)(size_t)a; }

template <int IMM> DI void gld16(v4u& d, unsigned voff, const void* sb) {
  asm volatile("global_load_dwordx4 %0, %1, %2 offset:%3" : "=v"(d) : "v"(voff), "s"(sb), "i"(IMM) : "memory");
}
template <int IMM> DI void gld16_sc1(v4u& d, unsigned voff, const void* sb) {
  asm volatile("global_load_dwordx4 %0, %1, %2 offset:%3 sc1" : "=v"(d) : "v"(voff), "s"(sb), "i"(IMM) : "memory");
}
template <int IMM> DI void gld_u16(unsigned& d, unsigned voff, const void* sb) {
  asm volatile("global_load_ushort %0, %1, %2 offset:%3" : "=v"(d) : "v"(voff), "s"(sb), "i"(IMM) : "memory");
}
DI void gld8_sc1(v2u& d, unsigned voff, const void* sb) {
  asm volatile("global_load_dwordx2 %0, %1, %2 sc1" : "=v"(d) : "v"(voff), "s"(sb) : "memory");
}
DI void dma4(unsigned lds, const void* sb, unsigned v0, unsigned v1, unsigned v2, unsigned v3) {
  asm volatile(
      "s_mov_b32 m0, %0\n\ts_nop 0\n\t"
      "global_load_lds_dwordx4 %2, %1\n\t"
      "global_load_lds_dwordx4 %3, %1 offset:1024\n\t"
      "global_load_lds_dwordx4 %4, %1 offset:2048\n\t"
      "global_load_lds_dwordx4 %5, %1 offset:3072"
      :: "s"(lds), "s"(sb), "v"(v0), "v"(v1), "v"(v2), "v"(v3) : "memory");
}
template <int N> DI void wait_vm() { asm volatile("s_waitcnt vmcnt(%0)" :: "i"(N) : "memory"); }
template <int N> DI void wait_vm4(v4u& a, v4u& b, v4u& c, v4u& d) {
  asm volatile("s_waitcnt vmcnt(%4)" : "+v"(a), "+v"(b), "+v"(c), "+v"(d) : "i"(N) : "memory");
}
DI void wait_lgkm() { asm volatile("s_waitcnt lgkmcnt(0)" ::: "memory"); }
DI void bar() { wait_lgkm(); __builtin_amdgcn_s_barrier(); asm volatile("" ::: "memory"); }

DI v4u lds_rd16(LAS char* p) { return *(LAS v4u*)p; }
DI v4u lds_tr(LAS char* p0, LAS char* p1) {
  s16x4 a = __builtin_amdgcn_ds_read_tr16_b64_v4i16((LAS s16x4*)p0);
  s16x4 b = __builtin_amdgcn_ds_read_tr16_b64_v4i16((LAS s16x4*)p1);
  bf16x8 f = __builtin_shufflevector(a, b, 0, 1, 2, 3, 4, 5, 6, 7);
  return __builtin_bit_cast(v4u, f);
}
DI unsigned cnt_arrive(LAS char* lds, int c) {
  wait_lgkm();
  unsigned old = 0;
  if ((threadIdx.x & 63) == 0) old = __hip_atomic_fetch_add((LAS unsigned*)(lds + L_CNT) + c, 1u, RLX_WG);
  return __builtin_amdgcn_readfirstlane(old);
}
DI void cnt_wait(LAS char* lds, int c, unsigned target) {
  unsigned spins = 0;
  while ((unsigned)__builtin_amdgcn_readfirstlane(__hip_atomic_load((LAS unsigned*)(lds + L_CNT) + c, RLX_WG)) < target) {
    if (++spins > (1u << 24)) break;
    __builtin_amdgcn_s_sleep(1);
  }
  asm volatile("" ::: "memory");
}
struct cpx { float r, i; };
DI cpx cmul(cpx a, cpx b) { return {a.r * b.r - a.i * b.i, a.r * b.i + a.i * b.r}; }
DI cpx cfma(cpx a, cpx h, cpx u) {
  return {fmaf(a.r, h.r, fmaf(-a.i, h.i, u.r)), fmaf(a.r, h.i, fmaf(a.i, h.r, u.i))};
}
DI float fma1(float a, float b, float c) { float d; asm("v_fma_f32 %0, %1, %2, %3" : "=v"(d) : "v"(a), "v"(b), "v"(c)); return d; }
DI float fms1(float a, float b, float c) { float d; asm("v_fma_f32 %0, -%1, %2, %3" : "=v"(d) : "v"(a), "v"(b), "v"(c)); return d; }
DI cpx cfma_s(cpx a, cpx h, cpx u) {
  return {fma1(a.r, h.r, fms1(a.i, h.i, u.r)), fma1(a.r, h.i, fma1(a.i, h.r, u.i))};
}
DI cpx csel(bool c, cpx a, cpx b) { return {c ? a.r : b.r, c ? a.i : b.i}; }

#define STAMP(i)
#define STAMP_DECL
#define STAMP_OUT()

struct Params {
  const char *x, *lre, *lim, *bre, *bim, *cre, *cim, *logdt, *dskip, *w1, *w2;
  float* out;
  char* ws;
};

DI void st16(__amdgpu_buffer_rsrc_t rs, unsigned off, v4u v) { __builtin_amdgcn_raw_buffer_store_b128(v, rs, (int)off, 0, 16); }
DI void st8(__amdgpu_buffer_rsrc_t rs, unsigned off, v2u v) { __builtin_amdgcn_raw_buffer_store_b64(v, rs, (int)off, 0, 16); }

DI void poll_flags(const char* fl, int l, u64 epoch) {
  GAS u64* fp = (GAS u64*)fl + (l & 3) * 16;
  unsigned spins = 0;
  while (!wall(__hip_atomic_load(fp, RLX_AGENT) == epoch)) {
    if (++spins > (1u << 20)) break;
    __builtin_amdgcn_s_sleep(4);
  }
  asm volatile("" ::: "memory");
}

DI f32x4 mfma16h(v2u a, v2u b, f32x4 c) {
  return __builtin_amdgcn_mfma_f32_16x16x16bf16_1k(__builtin_bit_cast(s16x4, a), __builtin_bit_cast(s16x4, b), c, 0, 0, 0);
}
DI v2u lds_tr1(LAS char* p0) { return __builtin_bit_cast(v2u, __builtin_amdgcn_ds_read_tr16_b64_v4i16((LAS s16x4*)p0)); }
DI void dma2(unsigned lds, const void* sb, unsigned v0, unsigned v1) {
  asm volatile(
      "s_mov_b32 m0, %0\n\ts_nop 0\n\t"
      "global_load_lds_dwordx4 %2, %1\n\t"
      "global_load_lds_dwordx4 %3, %1 offset:1024"
      :: "s"(lds), "s"(sb), "v"(v0), "v"(v1) : "memory");
}
DI void dma_stage(unsigned lds, const void* xb, const void* ub, const void* wb, unsigned vx0, unsigned vx1, unsigned vu, unsigned vw0, unsigned vw1) {
  asm volatile(
      "s_mov_b32 m0, %0\n\ts_nop 0\n\t"
      "global_load_lds_dwordx4 %5, %2\n\t"
      "global_load_lds_dwordx4 %6, %2 offset:1024\n\t"
      "global_load_lds_dwordx4 %7, %3 offset:2048\n\t"
      "global_load_lds_dwordx4 %8, %4 offset:3072\n\t"
      "s_mov_b32 m0, %1\n\ts_nop 0\n\t"
      "global_load_lds_dwordx4 %9, %4"
      :: "s"(lds), "s"(lds + 4096u), "s"(xb), "s"(ub), "s"(wb), "v"(vx0), "v"(vx1), "v"(vu), "v"(vw0), "v"(vw1) : "memory");
}
template <int CTRL> DI float dpp_add(float v) {
  return v + __builtin_bit_cast(float, __builtin_amdgcn_update_dpp(0, __builtin_bit_cast(int, v), CTRL, 0xf, 0xf, true));
}
DI float wave_sum(float v) {
  v = dpp_add<0x111>(v); v = dpp_add<0x112>(v); v = dpp_add<0x114>(v); v = dpp_add<0x118>(v);
  v = v + __builtin_bit_cast(float, __builtin_amdgcn_update_dpp(0, __builtin_bit_cast(int, v), 0x142, 0xa, 0xf, false));
  v = v + __builtin_bit_cast(float, __builtin_amdgcn_update_dpp(0, __builtin_bit_cast(int, v), 0x143, 0xc, 0xf, false));
  return __builtin_bit_cast(float, __builtin_amdgcn_readlane(__builtin_bit_cast(int, v), 63));
}
DI u64 mk_gran(u64 epoch, float v) {
  unsigned vb = __builtin_bit_cast(unsigned, v);
  vb = (vb + 0x80u) >> 8;
  return (epoch << 24) | (u64)(vb & 0xffffffu);
}
DI void dma1(unsigned lds, const void* sb, unsigned v) {
  asm volatile("s_mov_b32 m0, %0\n\ts_nop 0\n\tglobal_load_lds_dwordx4 %2, %1" :: "s"(lds), "s"(sb), "v"(v) : "memory");
}
#define FD 3
constexpr int usz(int u) { return (u & 3) == 3 ? 2 : 4; }
constexpr int f_cnt(bool kindA, int u, int at, int st) {
  int n = 0; bool seen = false;
  for (int a = 0; a < FD; a++) { if (seen) n += usz(a); if (kindA && a == u) seen = true; }
  for (int v = 0; v <= at; v++) {
    const int steps = v < at ? usz(v) : st;
    for (int t = 0; t < steps; t++) {
      if (v + FD < 16) {
        const int np = usz(v + FD), ns = usz(v);
        for (int i = 0; i < np; i++)
          if (i * ns / np == t) { if (seen) n++; if (kindA && v + FD == u && i == np - 1) seen = true; }
      }
      if (v + 4 < 16) { if (seen) n++; if (!kindA && v + 4 == u && t == usz(v) - 1) seen = true; }
    }
  }
  return n;
}
constexpr int hv_t(int n) { return n < 32 ? 8 + n / 4 : 8 + (n - 32) / 3; }
constexpr int hv_m(int n) { return n < 32 ? n % 4 : 4 + (n - 32) % 3; }
template <int... I, class F> DI void static_for(std::integer_sequence<int, I...>, F f) { (f(std::integral_constant<int, I>{}), ...); }
#define SB0() __builtin_amdgcn_sched_barrier(0)

__global__ __launch_bounds__(512, 2) NOPK void k_fused(Params p) {
  extern __shared__ __attribute__((aligned(16))) char smem[];
  LAS char* lds = (LAS char*)smem;
  STAMP_DECL
  STAMP(0);
  const int tid = threadIdx.x, l = tid & 63;
  const int w8 = __builtin_amdgcn_readfirstlane(tid >> 6);
  const int role = w8 >> 2, w = w8 & 3;
  const int bid = blockIdx.x, xcd = bid & 7, r = bid >> 3;
  const int j = 4 * (xcd >> 1) + (r >> 3);
  const int s = 8 * (xcd & 1) + (r & 7);
  const int hp2 = 8 * (xcd >> 1) + (r >> 2);
  const int ct = 4 * (xcd & 1) + (r & 3);
  const int odd = s & 1;
  const int i16 = l & 15, q = l >> 4;
  const int ta = i16 >> 2, tp = i16 & 3;
  const unsigned ldsb = (unsigned)(size_t)lds;
  const __amdgpu_buffer_rsrc_t rs = __builtin_amdgcn_make_buffer_rsrc((void*)p.ws, 0, (int)WS_END, 0x00020000);
  const v4u ones = {0x3F803F80u, 0x3F803F80u, 0x3F803F80u, 0x3F803F80u};

  const int rot = ROT ? ((2 * (r >> 3) + (r & 7)) & 7) : 0;
  u64 ep0;
  asm volatile("s_load_dwordx2 %0, %1, 0x0" : "=s"(ep0) : "s"(p.ws + WS_EPOCH) : "memory");
  unsigned plr[2], pli[2], pdt[2];
  v4u xr[2];
  unsigned cdum = 0;
  if (role == 0) {
    const char* rb = p.x + (size_t)(128 * j + 8 * s + 2 * w) * 1024;
    gld16<0>(xr[0], (unsigned)l * 16, rb); gld16<1024>(xr[1], (unsigned)l * 16, rb);
    const unsigned co = (unsigned)(32 * s + i16) * 2;
    gld_u16<0>(plr[0], co, p.lre); gld_u16<32>(plr[1], co, p.lre);
    gld_u16<0>(pli[0], co, p.lim); gld_u16<32>(pli[1], co, p.lim);
    gld_u16<0>(pdt[0], co, p.logdt); gld_u16<32>(pdt[1], co, p.logdt);
    {
      u64 pc;
      asm volatile("s_getpc_b64 %0" : "=s"(pc));
      const int ci = r * 4 + w;
      const char* cb = (const char*)(pc & ~255ull) + (ci < CODEPF ? ci : 0) * 1024;
      asm volatile("global_load_dword %0, %1, %2" : "=v"(cdum) : "v"((unsigned)l * 16), "s"(cb) : "memory");
    }
#pragma unroll
    for (int k = 0; k < 3; k++) {
      int row = bid * 11 + w + 4 * k;
      if (w + 4 * k >= 11) row = bid * 11;
      const char* src = row < 1792 ? p.w2 + (size_t)row * 1024 : (row < 2304 ? p.cre + (size_t)(row - 1792) * 1024 : p.cim + (size_t)(row - 2304) * 1024);
      asm volatile("global_load_dword %0, %1, %2" : "+v"(cdum) : "v"((unsigned)l * 16), "s"(src) : "memory");
    }
  }
  unsigned vx0, vx1, vu, vwA, vwB;
  {
    const int r8 = l >> 3, c8 = l & 7;
    const int tok0 = 8 * (r8 >> 2) + (r8 & 3);
    vx0 = (unsigned)(tok0 * 1024 + ((c8 ^ (r8 >> 1)) & 7) * 16);
    vx1 = (unsigned)((tok0 + 16) * 1024 + ((c8 ^ (4 + (r8 >> 1))) & 7) * 16);
    const int ch = 8 * w + r8, iu = 8 * (w & 1) + r8;
    vu = (unsigned)(ch * 1024 + ((c8 ^ (iu >> 1)) & 7) * 16);
    const int winoff = 448 * (s >> 1) + (odd ? 192 : 0);
    vwA = (unsigned)((16 * w + q) * 3584 + winoff + ((((i16 >> 1) ^ q) << 1) | (i16 & 1)) * 16);
    vwB = (unsigned)((16 * w + 8 + q) * 3584 + winoff + ((((i16 >> 1) ^ (q | 4)) << 1) | (i16 & 1)) * 16);
  }
  const char* xb = p.x + (size_t)(128 * j + 32 * (w >> 1) + 4 * (w & 1)) * 1024;
  const char* ub0 = p.bre + (size_t)(32 * s) * 1024;
  const char* ub1 = p.bim + (size_t)(32 * s) * 1024;
#define PIECE(N, I)                                                                                  \
  {                                                                                                  \
    constexpr int h_ = (N) / 3, n_ = (N) % 3;                                                        \
    const int kq_ = ((I) + rot) & 7;                                                                 \
    const unsigned d_ = ldsb + w * 3072 + ((I) & 3) * STG + h_ * 12288 + n_ * 1024;                  \
    if (n_ == 0) dma1(d_, xb + h_ * 65536 + kq_ * 128, vx0);                                         \
    if (n_ == 1) dma1(d_, xb + h_ * 65536 + kq_ * 128, vx1);                                         \
    if (n_ == 2) dma1(d_, (h_ ? ub1 : ub0) + kq_ * 128, vu);                                         \
  }
#define PALL(I) PIECE(0, I) PIECE(1, I) PIECE(2, I) PIECE(3, I) PIECE(4, I) PIECE(5, I)
#define W1P(C, n) dma1(ldsb + L_W1A + ((C) & 3) * 16384 + (16 * w + 4 * (n)) * 256,          \
                       p.w1 + (size_t)((((C) + rot) & 7) * 64 + ((n) & 1) * 4) * 3584, ((n) & 2) ? vwB : vwA)
  if (role == 1) {
    PALL(0)
    __builtin_amdgcn_s_barrier();
    PALL(1)
  } else {
    __builtin_amdgcn_s_barrier();
  }
  asm volatile("s_waitcnt lgkmcnt(0)" : "+s"(ep0) :: "memory");
  const u64 epoch = ep0 + 1ull;
  const unsigned tlo = (unsigned)(epoch & 0xffu), thi = (unsigned)(epoch >> 8);

  const unsigned sw0 = (unsigned)((i16 & 7) * 128 + (((0 + q) ^ (i16 >> 1)) & 7) * 16);
  const unsigned sw1 = (unsigned)((i16 & 7) * 128 + (((4 + q) ^ (i16 >> 1)) & 7) * 16);
  const unsigned xo = (unsigned)(2 * w * 3072 + (i16 >> 3) * 1024);
  const unsigned tro = (unsigned)((8 * q + ta) * 256 + 8 * tp);
  const int trkey = ta | ((q & 1) << 2);
  const unsigned zb = (unsigned)(2 * j + (w >> 1)) * HP_BYTES + (unsigned)l * 16u;
  const char* ab = p.ws + WS_Z + (size_t)hp2 * HP_BYTES;
  const unsigned avoff = (unsigned)l * 16;
  const int rot2 = ROT2 == 1 ? (hp2 & 3) : ROT2 == 2 ? (ct & 3) : 0;

  const int ec = tid & 15, et = tid >> 4;
  v2u xe[2], dk;
#define EPI_LOADS()                                                                                  \
  {                                                                                                  \
    xe[0] = *(const GAS v2u*)(p.x + (size_t)(64 * hp2 + et) * 1024 + (64 * ct + 4 * ec) * 2);        \
    xe[1] = *(const GAS v2u*)(p.x + (size_t)(64 * hp2 + et + 32) * 1024 + (64 * ct + 4 * ec) * 2);   \
    dk = *(const GAS v2u*)(p.dskip + (64 * ct + 4 * ec) * 2);                                        \
  }
  if (role == 0) {
    if (APRIO) __builtin_amdgcn_s_setprio(APRIO);
    asm volatile("s_waitcnt vmcnt(%8)" : "+v"(plr[0]), "+v"(plr[1]), "+v"(pli[0]), "+v"(pli[1]), "+v"(pdt[0]), "+v"(pdt[1]), "+v"(xr[0]), "+v"(xr[1]) : "i"((CODEPF ? 1 : 0) + (MWARM ? 3 : 0)) : "memory");
#pragma unroll
    for (int n = 0; n < 2; n++) {
      float v[8];
#pragma unroll
      for (int e = 0; e < 4; e++) { v[2 * e] = bf2f(xr[n][e] & 0xffffu); v[2 * e + 1] = __builtin_bit_cast(float, xr[n][e] & 0xffff0000u); }
      const float mean = wave_sum(((v[0] + v[1]) + (v[2] + v[3])) + ((v[4] + v[5]) + (v[6] + v[7]))) * (1.f / 512.f);
      float sq = 0.f;
#pragma unroll
      for (int e = 0; e < 8; e++) { const float d = v[e] - mean; sq = fmaf(d, d, sq); }
      const float rstd = rsqrtf(wave_sum(sq) * (1.f / 512.f) + 1e-5f);
      if (l == 0) *(LAS f32x2*)(lds + LDS_BYTES - 64 + (2 * w + n) * 8) = f32x2{mean, rstd};
    }
    wait_lgkm();
    cpx a1[2], cc[2], a8[2], pw[2], a128[2];
#pragma unroll
    for (int nb = 0; nb < 2; nb++) {
      const float lr = bf2f(plr[nb]), li = bf2f(pli[nb]);
      const float dtf = expf(bf2f(pdt[nb]));
      const float dt = bf2f(pk_bf16(dtf, 0.f) & 0xffffu);
      const float e = expf(dt * lr);
      float sn, cs;
      sincosf(dt * li, &sn, &cs);
      cpx a = {e * cs, e * sn};
      a1[nb] = a;
      const float nr = a.r - 1.f, ni = a.i, den = lr * lr + li * li;
      cc[nb] = {2.f * (nr * lr + ni * li) / den, 2.f * (ni * lr - nr * li) / den};
      cpx t = cmul(a, a); t = cmul(t, t); t = cmul(t, t);
      a8[nb] = t;
      cpx a16 = cmul(t, t), a32 = cmul(a16, a16), a64 = cmul(a32, a32);
      a128[nb] = cmul(a64, a64);
      cpx one = {1.f, 0.f};
      cpx pq = cmul(csel(q & 1, t, one), csel(q & 2, a16, one));
      cpx pwv = cmul(csel(w & 1, a32, one), csel(w & 2, a64, one));
      pw[nb] = cmul(pq, pwv);
    }
#pragma unroll
    for (int nb = 0; nb < 2; nb++)
      asm volatile("" : "+v"(a1[nb].r), "+v"(a1[nb].i), "+v"(cc[nb].r), "+v"(cc[nb].i), "+v"(a8[nb].r), "+v"(a8[nb].i), "+v"(pw[nb].r),
                   "+v"(pw[nb].i), "+v"(a128[nb].r), "+v"(a128[nb].i));
    STAMP(1);
    v4u cf[8][4];
    const unsigned cvoff = (unsigned)((64 * ct + (l >> 2)) * 1024 + (l & 3) * 16);
#define CLOAD(K0)                                                                         \
  _Pragma("unroll") for (int k = K0; k < K0 + 4; k++) {                                   \
    const int kr = (k + 2 * rot2) & 7;                                                    \
    const int sidx = 4 * w + 2 * (kr >> 2) + (kr & 1);                                    \
    const char* cb = (((kr >> 1) & 1) ? p.cim : p.cre) + sidx * 64;                       \
    _Pragma("unroll") for (int n = 0; n < 4; n++) gld16<0>(cf[k][n], cvoff, cb + n * 16384); \
    if (CPACE) __builtin_amdgcn_s_sleep(CPACE);                                           \
  }
#define CWAIT(K0, N)                                                                      \
  asm volatile("s_waitcnt vmcnt(%16)" : "+v"(cf[K0][0]), "+v"(cf[K0][1]), "+v"(cf[K0][2]), "+v"(cf[K0][3]), "+v"(cf[K0 + 1][0]),   \
               "+v"(cf[K0 + 1][1]), "+v"(cf[K0 + 1][2]), "+v"(cf[K0 + 1][3]), "+v"(cf[K0 + 2][0]), "+v"(cf[K0 + 2][1]),          \
               "+v"(cf[K0 + 2][2]), "+v"(cf[K0 + 2][3]), "+v"(cf[K0 + 3][0]), "+v"(cf[K0 + 3][1]), "+v"(cf[K0 + 3][2]),          \
               "+v"(cf[K0 + 3][3]) : "i"(N) : "memory");
#define CXPOSE(K0)                                                                        \
  _Pragma("unroll") for (int k = K0; k < K0 + 4; k++) {                                   \
    _Pragma("unroll") for (int n = 0; n < 4; n++) *(LAS v4u*)(lp(cwa) + n * 1024) = cf[k][n]; \
    _Pragma("unroll") for (int n = 0; n < 4; n++) cf[k][n] = lds_rd16(lp(cra) + n * 1024);  \
  }
    v2u sg;
    const unsigned sgoff = (unsigned)l * 8;
    const char* sgb = p.ws + WS_SG + (size_t)(128 * j + 32 * w) * 16;
    f32x4 au[2][2][2], asu;
#pragma unroll
    for (int b = 0; b < 2; b++)
#pragma unroll
      for (int nb = 0; nb < 2; nb++) { au[b][nb][0] = f32x4{0, 0, 0, 0}; au[b][nb][1] = f32x4{0, 0, 0, 0}; }
    asu = f32x4{0, 0, 0, 0};
    {
      const unsigned uo = (unsigned)((i16 >> 3) * 3072 + 2048);
      const int gs = (w & 1) * 2 + (w >> 1);
      unsigned cur = 0;
      v4u xd[2][2], wd[2][4], sd[2];
#pragma unroll
      for (int t = 0; t < 2; t++) xd[t][0] = xd[t][1] = wd[t][0] = wd[t][1] = wd[t][2] = wd[t][3] = sd[t] = v4u{0, 0, 0, 0};
#define A_STEP(RD, T, PX, PU, PS)                                                                     \
  {                                                                                                   \
    _Pragma("unroll") for (int g = 0; g < 4; g++) {                                                   \
      au[0][g & 1][g >> 1] = mfma16(xd[T][0], wd[T][g], au[0][g & 1][g >> 1]);                        \
      au[1][g & 1][g >> 1] = mfma16(xd[T][1], wd[T][g], au[1][g & 1][g >> 1]);                        \
      if (RD) wd[T][g] = lds_rd16(PU + g * 6144);                                                     \
      SB0();                                                                                          \
    }                                                                                                 \
    asu = mfma16(ones, sd[T], asu);                                                                   \
    if (RD) { xd[T][0] = lds_rd16(PX); xd[T][1] = lds_rd16(PX + 3072); sd[T] = lds_rd16(PS); }        \
    SB0();                                                                                            \
  }
#pragma unroll 1
      for (int it = 0; it < 8; it++) {
        const unsigned b0 = ldsb + cur;
        LAS char* px0 = lp(opaque(b0 + xo + sw0)); LAS char* px1 = lp(opaque(b0 + xo + sw1));
        LAS char* pu0 = lp(opaque(b0 + uo + sw0)); LAS char* pu1 = lp(opaque(b0 + uo + sw1));
        LAS char* ps0 = lp(opaque(b0 + uo + sw0 + gs * 6144)); LAS char* ps1 = lp(opaque(b0 + uo + sw1 + gs * 6144));
        cur = cur >= 3 * STG ? 0 : cur + STG;
        if (ALGKM) wait_lgkm();
        asm volatile("" ::: "memory");
        __builtin_amdgcn_s_barrier();
        asm volatile("" ::: "memory");
        if (it == 0 && w == 0 && l < 16)
          __hip_atomic_store((GAS u64*)(p.ws + WS_SG) + (size_t)(128 * j + 8 * s) * 2 + l,
                             mk_gran(epoch, *(LAS float*)(lds + LDS_BYTES - 64 + l * 4)), RLX_AGENT);
        SB0();
        A_STEP(true, 0, px0, pu0, ps0)
        A_STEP(true, 1, px1, pu1, ps1)
      }
      gld8_sc1(sg, sgoff, sgb);
      A_STEP(false, 0, lds, lds, lds)
      A_STEP(false, 1, lds, lds, lds)
#undef A_STEP
    }
    STAMP(2);
    {
      asm volatile("s_waitcnt vmcnt(0)" : "+v"(sg), "+v"(cdum) :: "memory");
      if (tid < 16) ((LAS unsigned*)(lds + L_CNT))[tid] = 0u;
      unsigned spins = 0;
      while (!wall(sg.y == thi && (sg.x >> 24) == tlo)) {
        if (++spins > (1u << 22)) break;
        __builtin_amdgcn_s_sleep(1);
        gld8_sc1(sg, sgoff, sgb);
        asm volatile("s_waitcnt vmcnt(0)" : "+v"(sg), "+v"(cdum) :: "memory");
      }
      const int tk = l >> 1;
      ((LAS unsigned*)(lds + L_STAT))[(w * 32 + ((tk >> 2) & 1) * 16 + ((tk & 3) | ((tk >> 3) << 2))) * 2 + (l & 1)] = sg.x << 8;
      if (q == 0) ((LAS float*)(lds + L_SU))[w * 16 + i16] = asu[0];
    }
    bar();
    STAMP(3);
    cpx u[2][8];
    {
      f32x2 stt[2][4];
#pragma unroll
      for (int b = 0; b < 2; b++) {
        f32x4 s0 = *(LAS f32x4*)(lds + L_STAT + (w * 32 + b * 16 + 4 * q) * 8);
        f32x4 s1 = *(LAS f32x4*)(lds + L_STAT + (w * 32 + b * 16 + 4 * q) * 8 + 16);
        stt[b][0] = f32x2{s0[0], s0[1]}; stt[b][1] = f32x2{s0[2], s0[3]};
        stt[b][2] = f32x2{s1[0], s1[1]}; stt[b][3] = f32x2{s1[2], s1[3]};
      }
#pragma unroll
      for (int nb = 0; nb < 2; nb++) {
        const float sre = ((LAS float*)(lds + L_SU))[(nb * 2 + 0) * 16 + i16];
        const float sim = ((LAS float*)(lds + L_SU))[(nb * 2 + 1) * 16 + i16];
        cpx h = {0.f, 0.f};
#pragma unroll
        for (int k = 0; k < 8; k++) {
          const int b = k >> 2, rg = k & 3;
          const float m = stt[b][rg][0], rr = stt[b][rg][1];
          u[nb][k].r = fmaf(-m, sre, au[b][nb][0][rg]) * rr;
          u[nb][k].i = fmaf(-m, sim, au[b][nb][1][rg]) * rr;
          h = cfma(a1[nb], h, u[nb][k]);
        }
        *(LAS f32x2*)(lds + L_E + ((nb * 16 + i16) * 16 + 4 * w + q) * 8) = f32x2{h.r, h.i};
      }
    }
    cnt_arrive(lds, C_E);
    cnt_wait(lds, C_E, 4);
    cpx cl[2];
    {
      cpx c[2] = {{0.f, 0.f}, {0.f, 0.f}};
      LAS f32x4* e4a = (LAS f32x4*)(lds + L_E + i16 * 128);
      LAS f32x4* e4b = (LAS f32x4*)(lds + L_E + (16 + i16) * 128);
#pragma unroll 1
      for (int k = 0; k < 2 * w; k++) {
        f32x4 ea = e4a[k], eb = e4b[k];
        c[0] = cfma(a8[0], c[0], cpx{ea[0], ea[1]}); c[1] = cfma(a8[1], c[1], cpx{eb[0], eb[1]});
        c[0] = cfma(a8[0], c[0], cpx{ea[2], ea[3]}); c[1] = cfma(a8[1], c[1], cpx{eb[2], eb[3]});
      }
      cpx tot[2];
#pragma unroll
      for (int nb = 0; nb < 2; nb++) {
        LAS f32x4* e4 = nb ? e4b : e4a;
        f32x4 e0 = e4[2 * w], e1 = e4[2 * w + 1];
        cpx c1 = cfma(a8[nb], c[nb], cpx{e0[0], e0[1]});
        cpx c2 = cfma(a8[nb], c1, cpx{e0[2], e0[3]});
        cpx c3 = cfma(a8[nb], c2, cpx{e1[0], e1[1]});
        tot[nb] = cfma(a8[nb], c3, cpx{e1[2], e1[3]});
        cl[nb] = csel(q & 2, csel(q & 1, c3, c2), csel(q & 1, c1, c[nb]));
      }
      if (w == 3 && j < 15) {
        const cpx t = (q & 2) ? tot[1] : tot[0];
        const float v = (q & 1) ? t.i : t.r;
        const u64 g = mk_gran(epoch, v);
        GAS u64* gp = (GAS u64*)(p.ws + WS_GRAN + (size_t)(j * 16 + s) * 512) + (((q >> 1) * 16 + i16) * 2 + (q & 1));
        __hip_atomic_store(gp, g, RLX_AGENT);
      }
    }
    STAMP(4);
    const unsigned cwa = opaque(ldsb + L_CST + w * 4096 + (l >> 2) * 64 + (((l & 3) ^ ((-(l >> 4)) & 3)) * 16));
    const unsigned cra = opaque(ldsb + L_CST + w * 4096 + i16 * 64 + ((q ^ ((-(i16 >> 2)) & 3)) * 16));
    unsigned wdum = 0;
    CLOAD(0)
    {
      const unsigned wv2 = (unsigned)((l >> 5) * 1024 + (l & 31) * 16);
#pragma unroll 1
      for (int k = 0; k < NWARM; k++)
        asm volatile("global_load_dword %0, %1, %2" : "+v"(wdum) : "v"(wv2), "s"(p.w2 + (size_t)((k * 32 + r) * 4 + w) * 2048 + (xcd & 1) * 512) : "memory");
    }
    CWAIT(0, NWARM)
    CXPOSE(0)
    if (w < 2) {
      const char* gb = p.ws + WS_GRAN + (size_t)s * 512 + w * 256;
      unsigned spins = 0;
      if (j > 0) {
        GAS u64* pp = (GAS u64*)(gb + (size_t)(l < j ? l : 0) * 8192 + 248);
        while (!wall((__hip_atomic_load(pp, RLX_AGENT) >> 24) == (epoch & 0xffffffffffull))) {
          if (++spins > (1u << 20)) break;
          __builtin_amdgcn_s_sleep(2);
        }
      }
      v4u gr[4];
      for (;;) {
#pragma unroll
        for (int m = 0; m < 4; m++) {
          const int pn = (4 * m + q) < j ? 4 * m + q : 0;
          gld16_sc1<0>(gr[m], (unsigned)(pn * 8192 + i16 * 16), gb);
        }
        wait_vm4<0>(gr[0], gr[1], gr[2], gr[3]);
        bool ok = true;
#pragma unroll
        for (int m = 0; m < 4; m++) {
          const bool need = (4 * m + q) < j;
          const bool good = (gr[m].y == thi) && ((gr[m].x >> 24) == tlo) && (gr[m].w == thi) && ((gr[m].z >> 24) == tlo);
          ok = ok && (good || !need);
        }
        if (wall(ok)) break;
        if (++spins > (1u << 20)) break;
        __builtin_amdgcn_s_sleep(2);
      }
      const cpx b128 = csel(w != 0, a128[1], a128[0]);
      const cpx a256 = cmul(b128, b128);
      const cpx a512 = cmul(a256, a256);
      cpx acc = {0.f, 0.f};
#pragma unroll
      for (int m = 0; m < 4; m++) {
        const bool need = (4 * m + q) < j;
        cpx e = {__builtin_bit_cast(float, gr[m].x << 8), __builtin_bit_cast(float, gr[m].z << 8)};
        acc = csel(need, cfma(a512, acc, e), acc);
      }
      const int dd = (j - 1 - q) & 3;
      cpx one = {1.f, 0.f};
      cpx fz = cmul(csel(dd & 1, b128, one), csel(dd & 2, a256, one));
      acc = cmul(acc, fz);
      *(LAS f32x2*)(lds + L_HIN + ((w * 16 + i16) * 4 + q) * 8) = f32x2{acc.r, acc.i};
      cnt_arrive(lds, C_HIN);
    }
    cnt_wait(lds, C_HIN, 2);
    STAMP(5);
    {
      LAS char* ht = lds + L_HT + w * 2048;
      unsigned hpk[2][2][2][2];
#pragma unroll
      for (int nb = 0; nb < 2; nb++) {
        const f32x4 h01 = *(LAS f32x4*)(lds + L_HIN + (nb * 16 + i16) * 32);
        const f32x4 h23 = *(LAS f32x4*)(lds + L_HIN + (nb * 16 + i16) * 32 + 16);
        const cpx acc = {(h01[0] + h01[2]) + (h23[0] + h23[2]), (h01[1] + h01[3]) + (h23[1] + h23[3])};
        cpx h = cfma(pw[nb], acc, cl[nb]);
        float orr[8], oi[8];
#pragma unroll
        for (int k = 0; k < 8; k++) {
          h = cfma(a1[nb], h, u[nb][k]);
          orr[k] = cc[nb].r * h.r - cc[nb].i * h.i;
          oi[k] = -(cc[nb].r * h.i + cc[nb].i * h.r);
        }
#pragma unroll
        for (int b = 0; b < 2; b++) {
          hpk[nb][0][b][0] = pk_bf16(orr[4 * b], orr[4 * b + 1]); hpk[nb][0][b][1] = pk_bf16(orr[4 * b + 2], orr[4 * b + 3]);
          hpk[nb][1][b][0] = pk_bf16(oi[4 * b], oi[4 * b + 1]);   hpk[nb][1][b][1] = pk_bf16(oi[4 * b + 2], oi[4 * b + 3]);
        }
      }
      const unsigned zy = zb + Y_OFF + (unsigned)(4 * (s >> 1) + (s & 1)) * 4096u + (unsigned)(2 * (w & 1)) * 1024u;
#pragma unroll
      for (int b = 0; b < 2; b++) {
#pragma unroll
        for (int nb = 0; nb < 2; nb++)
#pragma unroll
          for (int part = 0; part < 2; part++)
            *(LAS v2u*)(ht + (part * 32 + nb * 16 + i16) * 32 + q * 8) = v2u{hpk[nb][part][b][0], hpk[nb][part][b][1]};
#pragma unroll
        for (int part = 0; part < 2; part++) {
          LAS char* rp = ht + (part * 32 + 8 * q + ta) * 32 + tp * 8;
          v4u v = lds_tr(rp, rp + 4 * 32);
          st16(rs, (unsigned)WS_Z + zy + (unsigned)part * 8192u + b * 1024u, v);
        }
      }
    }
    STAMP(6);
    CLOAD(4)
    asm volatile("s_waitcnt vmcnt(16)" : "+v"(wdum) :: "memory");
    if (cnt_arrive(lds, C_HS) == 3 && l < 16)
      __hip_atomic_store((GAS u64*)(p.ws + WS_FLAG) + (j * 16 + s) * 16 + l, epoch, RLX_AGENT);
    STAMP(7);
    CWAIT(4, 0)
    CXPOSE(4)
    cnt_arrive(lds, C_CX);
    STAMP(8);
    EPI_LOADS()
    poll_flags(p.ws + WS_FLAG + (size_t)(j * 16 + 4 * w) * 128, l, epoch);
    asm volatile("" : "+v"(xe[0]), "+v"(xe[1]), "+v"(dk));
    STAMP(9);
    f32x4 acc[4][4];
#pragma unroll
    for (int a = 0; a < 4; a++)
#pragma unroll
      for (int b = 0; b < 4; b++) acc[a][b] = f32x4{0, 0, 0, 0};
    v4u af[3][4];
    const char* ay = ab + Y_OFF + (size_t)(8 * w) * 4096;
#define YL(K, i) gld16_sc1<(i) * 1024>(af[(K) % 3][i], avoff, ay + ((((K) + 2 * rot2) & 7) * 4096))
#define YST(K, tb)                                                                                  \
  {                                                                                                 \
    _Pragma("unroll") for (int nb = 0; nb < 4; nb++) acc[nb][tb] = mfma16(cf[K][nb], af[(K) % 3][tb], acc[nb][tb]); \
    SB0();                                                                                          \
    if ((K) + 2 < 8) YL((K) + 2, tb);                                                               \
    SB0();                                                                                          \
  }
#define YS(K)                                                                                       \
  {                                                                                                 \
    wait_vm4<((K) < 7 ? 4 : 0)>(af[(K) % 3][0], af[(K) % 3][1], af[(K) % 3][2], af[(K) % 3][3]);    \
    YST(K, 0) YST(K, 1) YST(K, 2) YST(K, 3)                                                         \
  }
    YL(0, 0); YL(0, 1); YL(0, 2); YL(0, 3); YL(1, 0); YL(1, 1); YL(1, 2); YL(1, 3);
    YS(0) YS(1) YS(2) YS(3) YS(4) YS(5) YS(6) YS(7)
#undef YL
#undef YS
#undef YST
    STAMP(10);
    cnt_wait(lds, C_CX, 4);
    cnt_wait(lds, C_B2, 4);
    {
      LAS char* yb = lds + ((w & 1) ? L_YB1 : L_YB0);
      if (w < 2) cnt_wait(lds, C_Y1 + (w & 1), 1);
#pragma unroll
      for (int tb = 0; tb < 4; tb++) {
        const int tk = 32 * (tb >> 1) + 8 * (i16 >> 2) + 4 * (tb & 1) + (i16 & 3);
#pragma unroll
        for (int nb = 0; nb < 4; nb++) {
          LAS f32x4* d = (LAS f32x4*)(yb + tk * 256 + (((4 * nb + q) ^ i16) & 15) * 16);
          if (w < 2) *d = *d + acc[nb][tb]; else *d = acc[nb][tb];
        }
      }
      if (w >= 2) cnt_arrive(lds, C_Y1 + (w & 1));
    }
    STAMP(11);
  } else {
    v4u xf[2][16];
    f32x4 ah[7][2];
#pragma unroll
    for (int mb = 0; mb < 7; mb++) { ah[mb][0] = f32x4{0, 0, 0, 0}; ah[mb][1] = f32x4{0, 0, 0, 0}; }
    unsigned pt[7];
#pragma unroll
    for (int mb = 0; mb < 7; mb++) pt[mb] = opaque(ldsb + L_W1A + tro + (unsigned)(((mb + odd) ^ trkey) * 32));
    const int m1 = w < 3 ? w + 4 : 6;
    const unsigned hq0 = opaque(ldsb + L_W1A + tro + (unsigned)(((w + odd) ^ trkey) * 32));
    const unsigned hq1 = opaque(ldsb + L_W1A + tro + (unsigned)(((m1 + odd) ^ trkey) * 32));
    f32x4 sh0 = {0, 0, 0, 0}, sh1 = {0, 0, 0, 0};
    {
      LAS char* px0 = lp(opaque(ldsb + xo + sw0)); LAS char* px1 = lp(opaque(ldsb + xo + sw1));
#define L1IT(I, NW)                                                                                   \
  {                                                                                                   \
    wait_vm<NW>();                                                                                    \
    __builtin_amdgcn_s_barrier();                                                                     \
    asm volatile("" ::: "memory");                                                                    \
    xf[0][2 * (I)] = lds_rd16(px0 + ((I) & 3) * STG); xf[1][2 * (I)] = lds_rd16(px0 + ((I) & 3) * STG + 3072); \
    xf[0][2 * (I) + 1] = lds_rd16(px1 + ((I) & 3) * STG); xf[1][2 * (I) + 1] = lds_rd16(px1 + ((I) & 3) * STG + 3072); \
    SB0();                                                                                            \
    if ((I) < 3 || (I) == 7) {                                                                        \
      if ((I) < 6) { PALL((I) + 2) }                                                                  \
      W1P(I, 0); W1P(I, 1); W1P(I, 2); W1P(I, 3);                                                     \
    } else {                                                                                          \
      constexpr int c_ = (I) - 3;                                                                     \
      v4u wa[7], wb_[7], h0[2], h1[2];                                                                \
      _Pragma("unroll") for (int mb = 0; mb < 7; mb++) wa[mb] = lds_tr(lp(pt[mb]) + c_ * 16384, lp(pt[mb]) + c_ * 16384 + 1024); \
      SB0();                                                                                          \
      if ((I) < 6) { PIECE(0, (I) + 2) PIECE(1, (I) + 2) PIECE(2, (I) + 2) }                          \
      SB0();                                                                                          \
      _Pragma("unroll") for (int mb = 0; mb < 7; mb++) {                                              \
        ah[mb][0] = mfma16(wa[mb], xf[0][2 * c_], ah[mb][0]);                                         \
        ah[mb][1] = mfma16(wa[mb], xf[1][2 * c_], ah[mb][1]);                                         \
        wb_[mb] = lds_tr(lp(pt[mb]) + c_ * 16384 + 8192, lp(pt[mb]) + c_ * 16384 + 9216);             \
        if ((I) < 6) { if (mb == 1) PIECE(3, (I) + 2) if (mb == 3) PIECE(4, (I) + 2) if (mb == 5) PIECE(5, (I) + 2) } \
        SB0();                                                                                        \
      }                                                                                               \
      _Pragma("unroll") for (int mb = 0; mb < 7; mb++) {                                              \
        ah[mb][0] = mfma16(wb_[mb], xf[0][2 * c_ + 1], ah[mb][0]);                                    \
        ah[mb][1] = mfma16(wb_[mb], xf[1][2 * c_ + 1], ah[mb][1]);                                    \
        if (mb == 0) { h0[0] = lds_tr(lp(hq0) + c_ * 16384, lp(hq0) + c_ * 16384 + 1024); h1[0] = lds_tr(lp(hq1) + c_ * 16384, lp(hq1) + c_ * 16384 + 1024); } \
        if (mb == 1) { h0[1] = lds_tr(lp(hq0) + c_ * 16384 + 8192, lp(hq0) + c_ * 16384 + 9216); h1[1] = lds_tr(lp(hq1) + c_ * 16384 + 8192, lp(hq1) + c_ * 16384 + 9216); } \
        if (mb == 0) W1P(I, 0); if (mb == 2) W1P(I, 1); if (mb == 4) W1P(I, 2); if (mb == 6) W1P(I, 3); \
        SB0();                                                                                        \
      }                                                                                               \
      sh0 = mfma16(h0[0], ones, sh0); sh1 = mfma16(h1[0], ones, sh1);                                 \
      sh0 = mfma16(h0[1], ones, sh0); sh1 = mfma16(h1[1], ones, sh1);                                 \
    }                                                                                                 \
    wait_lgkm();                                                                                      \
  }
      L1IT(0, 6) L1IT(1, 10) L1IT(2, 14) STAMP(15); L1IT(3, 14) L1IT(4, 14) L1IT(5, 14) L1IT(6, 14) L1IT(7, 8)
#undef L1IT
    }
    STAMP(2);
    wait_vm<8>();
    bar();
    STAMP(3);
    const unsigned ring = ldsb + L_RING + w * RING;
    unsigned fv;
    {
      const int row = l >> 3, c16p = l & 7;
      fv = (unsigned)(row * 1024 + ct * 128 + ((((c16p >> 1) ^ ((row >> 1) & 3)) << 1) | (c16p & 1)) * 16);
    }
    const char* w2b = p.w2 + (size_t)(448 * w) * 1024;
#define WP(N, n) dma1(ring + ((N) & 1) * 14336 + (n) * 1024, w2b + (size_t)(((N) + rot2) & 3) * (112 * 1024) + (n) * 8192, fv)
    {
      v4u wd[7];
      static_for(std::make_integer_sequence<int, 7>{}, [&](auto NC) __attribute__((always_inline)) NOPK {
        constexpr int n = decltype(NC)::value, T2 = hv_t(n), m2 = hv_m(n);
        wd[n] = lds_tr(lp(pt[m2]) + (T2 & 7) * 8192, lp(pt[m2]) + (T2 & 7) * 8192 + 1024);
      });
      const unsigned zf = zb + (unsigned)s * 14336u;
      float nm[2], r2[2];
      v4u hs0, hs1;
      unsigned pkd[7][2][2];
      auto epi = [&](auto MB, auto BB) __attribute__((always_inline)) NOPK {
        constexpr int mb = decltype(MB)::value, b = decltype(BB)::value;
        const f32x4 sh = *(LAS f32x4*)(lds + L_SH + (mb * 16 + 4 * q) * 4);
        float y[4];
#pragma unroll
        for (int e = 0; e < 4; e++) {
          const float d = fmaf(sh[e], nm[b], ah[mb][b][e]);
          y[e] = d * __builtin_amdgcn_rcpf(1.f + __builtin_amdgcn_exp2f(d * r2[b]));
        }
        pkd[mb][b][0] = pk_bf16(y[0], y[1]); pkd[mb][b][1] = pk_bf16(y[2], y[3]);
        if constexpr ((mb & 1) == 1) {
          v4u v = {pkd[mb - 1][b][0], pkd[mb - 1][b][1], pkd[mb][b][0], pkd[mb][b][1]};
          st16(rs, (unsigned)WS_Z + zf + (unsigned)(mb >> 1) * 4096u + (unsigned)(2 * (w & 1) + b) * 1024u, v);
        }
      };
      static_for(std::make_integer_sequence<int, 56>{}, [&](auto NC) __attribute__((always_inline)) NOPK {
        constexpr int n = decltype(NC)::value, T = hv_t(n), mb = hv_m(n), sl = n % 7;
        if constexpr (n == 9) {
          wait_vm<0>();
          cnt_arrive(lds, C_W);
          cnt_wait(lds, C_W, 4);
        }
        if constexpr (n < 32 && (n & 3) == 0) {
          hs0 = lds_tr(lp(hq0) + (n / 4) * 8192, lp(hq0) + (n / 4) * 8192 + 1024);
          hs1 = lds_tr(lp(hq1) + (n / 4) * 8192, lp(hq1) + (n / 4) * 8192 + 1024);
        }
        if constexpr (n == 32) {
          if (i16 == 0) {
            *(LAS f32x4*)(lds + L_SH + (w * 16 + 4 * q) * 4) = sh0;
            if (w < 3) *(LAS f32x4*)(lds + L_SH + (m1 * 16 + 4 * q) * 4) = sh1;
          }
          cnt_arrive(lds, C_SH);
          cnt_wait(lds, C_SH, 4);
          const f32x2 s0 = *(LAS f32x2*)(lds + L_STAT + (w * 32 + i16) * 8), s1 = *(LAS f32x2*)(lds + L_STAT + (w * 32 + 16 + i16) * 8);
          nm[0] = -s0[0]; nm[1] = -s1[0]; r2[0] = s0[1] * -1.44269504088896f; r2[1] = s1[1] * -1.44269504088896f;
        }
        ah[mb][0] = mfma16(wd[sl], xf[0][T], ah[mb][0]);
        ah[mb][1] = mfma16(wd[sl], xf[1][T], ah[mb][1]);
        if constexpr (n < 32 && (n & 3) == 3) { sh0 = mfma16(hs0, ones, sh0); sh1 = mfma16(hs1, ones, sh1); }
        if constexpr (n + 7 < 56) {
          constexpr int T2 = hv_t(n + 7), m2 = hv_m(n + 7);
          wd[sl] = lds_tr(lp(pt[m2]) + (T2 & 7) * 8192, lp(pt[m2]) + (T2 & 7) * 8192 + 1024);
        }
        if constexpr (n >= 32 && (n - 32) % 3 == 1) {
          constexpr int u = (n - 32) / 3;
          epi(std::integral_constant<int, (u >> 2) * 2 + (u & 1)>{}, std::integral_constant<int, (u >> 1) & 1>{});
        }
        SB0();
      });
      STAMP(1);
      cnt_arrive(lds, C_B2);
      epi(std::integral_constant<int, 4>{}, std::integral_constant<int, 0>{});
      epi(std::integral_constant<int, 5>{}, std::integral_constant<int, 0>{});
      epi(std::integral_constant<int, 4>{}, std::integral_constant<int, 1>{});
      epi(std::integral_constant<int, 5>{}, std::integral_constant<int, 1>{});
      epi(std::integral_constant<int, 6>{}, std::integral_constant<int, 0>{});
      epi(std::integral_constant<int, 6>{}, std::integral_constant<int, 1>{});
      v4u v = {pkd[6][0][0], pkd[6][0][1], pkd[6][1][0], pkd[6][1][1]};
      st16(rs, (unsigned)WS_Z + zf + 12288u + (unsigned)(w & 1) * 1024u, v);
    }
    STAMP(4);
    wait_vm<0>();
    if (cnt_arrive(lds, C_FS) == 3 && l < 16)
      __hip_atomic_store((GAS u64*)(p.ws + WS_FLAG) + (256 + j * 16 + s) * 16 + l, epoch, RLX_AGENT);
    STAMP(5);
    cnt_wait(lds, C_B2, 4);
    cnt_wait(lds, C_HS, 4);
#pragma unroll
    for (int n = 0; n < 14; n++) WP(0, n);
    EPI_LOADS()
    poll_flags(p.ws + WS_FLAG + (size_t)(256 + j * 16 + 4 * w) * 128, l, epoch);
    asm volatile("" : "+v"(xe[0]), "+v"(xe[1]), "+v"(dk));
    STAMP(6);
    f32x4 acc[4][4];
#pragma unroll
    for (int a = 0; a < 4; a++)
#pragma unroll
      for (int b = 0; b < 4; b++) acc[a][b] = f32x4{0, 0, 0, 0};
    v4u af[4][4];
    const char* afb = ab + (size_t)(4 * w) * 14336;
    unsigned ftr[4];
#pragma unroll
    for (int nb = 0; nb < 4; nb++) {
      const int row = 4 * q + ta;
      ftr[nb] = opaque(ring + (unsigned)(row * 128 + ((nb ^ ((row >> 1) & 3)) * 32) + tp * 8));
    }
#define ALD(U, i) gld16_sc1<(i) * 1024>(af[(U) & 3][i], avoff, afb + (size_t)((((U) >> 2) + rot2) & 3) * 14336 + ((U) & 3) * 4096)
#define ALDS(U, V, t)                             \
  {                                                                                                 \
    if ((V) < 16) {                                                                                 \
      if (0 * usz(U) / usz(V) == (t)) ALD(V, 0);                                                    \
      if (1 * usz(U) / usz(V) == (t)) ALD(V, 1);                                                    \
      if (usz(V) == 4 && 2 * usz(U) / usz(V) == (t)) ALD(V, 2);                                     \
      if (usz(V) == 4 && 3 * usz(U) / usz(V) == (t)) ALD(V, 3);                                     \
    }                                                                                               \
  }
#define WRD(U)                                           \
  {                                                                                                 \
    _Pragma("unroll") for (int nb = 0; nb < 4; nb++) {                                              \
      LAS char* b_ = lp(ftr[nb]) + (((U) >> 2) & 1) * 14336 + ((U) & 3) * 4096;                     \
      if (((U) & 3) == 3) { v2u t_ = lds_tr1(b_); wf[(U) & 1][nb].x = t_.x; wf[(U) & 1][nb].y = t_.y; } \
      else wf[(U) & 1][nb] = lds_tr(b_, b_ + 2048);                                                 \
    }                                                                                               \
  }
#define FSTEP(U, t)                                                                                 \
  {                                                                                                 \
    if (((U) & 3) < 3) {                                                                            \
      _Pragma("unroll") for (int nb = 0; nb < 4; nb++)                                              \
          acc[nb][t] = mfma16(wf[(U) & 1][nb], af[(U) & 3][t], acc[nb][t]);                         \
    } else {                                                                                        \
      _Pragma("unroll") for (int nb = 0; nb < 4; nb++) {                                            \
        const v2u wh_ = {wf[(U) & 1][nb].x, wf[(U) & 1][nb].y};                                     \
        acc[nb][(2 * (t)) & 3] = mfma16h(wh_, v2u{af[3][t].x, af[3][t].y}, acc[nb][(2 * (t)) & 3]); \
        acc[nb][(2 * (t) + 1) & 3] = mfma16h(wh_, v2u{af[3][t].z, af[3][t].w}, acc[nb][(2 * (t) + 1) & 3]); \
      }                                                                                             \
    }                                                                                               \
    SB0();                                                                                          \
    ALDS(U, (U) + FD, t)                                                                            \
    if ((U) < 12) WP(((U) >> 2) + 1, 4 * ((U) & 3) + (t));                                          \
    if ((t) == usz(U) - 2 && (U) < 15) {                                                            \
      if ((U) + 1 >= 4) wait_vm<f_cnt(false, (U) + 1, U, (t) + 1)>();                               \
      WRD((U) + 1)                                                                                  \
    }                                                                                               \
    SB0();                                                                                          \
  }
#define FU(U)                                                                                       \
  {                                                                                                 \
    if (((U) & 3) < 3) {                                                                            \
      wait_vm4<f_cnt(true, U, U, 0)>(af[(U) & 3][0], af[(U) & 3][1], af[(U) & 3][2], af[(U) & 3][3]); \
      FSTEP(U, 0) FSTEP(U, 1) FSTEP(U, 2) FSTEP(U, 3)                                               \
    } else {                                                                                        \
      asm volatile("s_waitcnt vmcnt(%2)" : "+v"(af[3][0]), "+v"(af[3][1]) : "i"(f_cnt(true, U, U, 0)) : "memory"); \
      FSTEP(U, 0) FSTEP(U, 1)                                                                       \
    }                                                                                               \
  }
    v4u wf[2][4];
    ALD(0, 0); ALD(0, 1); ALD(0, 2); ALD(0, 3);
    ALD(1, 0); ALD(1, 1); ALD(1, 2); ALD(1, 3);
    ALD(2, 0); ALD(2, 1); ALD(2, 2); ALD(2, 3);
    static_assert(FD == 3, "window");
    WRD(0)
    FU(0) STAMP(7); FU(1) FU(2) FU(3) STAMP(8); FU(4) FU(5) FU(6) FU(7) STAMP(9); FU(8) FU(9) FU(10) FU(11) STAMP(14); FU(12) FU(13) FU(14) FU(15)
#undef FU
#undef FSTEP
#undef WRD
#undef ALD
#undef ALDS
#undef WP
    STAMP(10);
    {
      LAS char* dst = lp(ring);
#pragma unroll
      for (int tb = 0; tb < 4; tb++) {
        const int tk = 32 * (tb >> 1) + 8 * (i16 >> 2) + 4 * (tb & 1) + (i16 & 3);
#pragma unroll
        for (int nb = 0; nb < 4; nb++)
          *(LAS f32x4*)(dst + tk * 256 + (((4 * nb + q) ^ i16) & 15) * 16) = acc[nb][tb];
      }
    }
    STAMP(11);
  }
  {
    bar();
    STAMP(12);
    if (j == 15 && s == 0 && tid == 0)
      __hip_atomic_store((GAS u64*)(p.ws + WS_EPOCH), epoch, RLX_AGENT);
    const __amdgpu_buffer_rsrc_t ro = __builtin_amdgcn_make_buffer_rsrc((void*)p.out, 0, 2048 * 512 * 4, 0x00020000);
    const float dkf[4] = {bf2f(dk.x & 0xffff), bf2f(dk.x >> 16), bf2f(dk.y & 0xffff), bf2f(dk.y >> 16)};
#pragma unroll
    for (int n = 0; n < 2; n++) {
      const int tk = et + 32 * n;
      const int key = (tk & 3) | (((tk >> 3) & 3) << 2);
      const int o = tk * 256 + ((ec ^ key) & 15) * 16;
      LAS char* fb = lds + L_RING + o;
      const f32x4 f = (*(LAS f32x4*)(fb) + *(LAS f32x4*)(fb + RING)) + (*(LAS f32x4*)(fb + 2 * RING) + *(LAS f32x4*)(fb + 3 * RING));
      const f32x4 y = *(LAS f32x4*)(lds + L_YB0 + o) + *(LAS f32x4*)(lds + L_YB1 + o);
      const int tkp = 64 * (hp2 & 1) + tk;
      const f32x2 se = *(LAS f32x2*)(lds + L_STAT + ((tkp >> 5) * 32 + ((tkp >> 2) & 1) * 16 + key) * 8);
      const float xv[4] = {bf2f(xe[n].x & 0xffff), bf2f(xe[n].x >> 16), bf2f(xe[n].y & 0xffff), bf2f(xe[n].y >> 16)};
      f32x4 o4;
#pragma unroll
      for (int e = 0; e < 4; e++) {
        const float xn = (xv[e] - se[0]) * se[1];
        const float sv = fmaf(xn, dkf[e], y[e]);
        const float fe = f[e] * se[1];
        const float g = fe * __builtin_amdgcn_rcpf(1.f + __builtin_amdgcn_exp2f(fe * -1.44269504088896f));
        o4[e] = fmaf(sv, g, xv[e]);
      }
      __builtin_amdgcn_raw_buffer_store_b128(__builtin_bit_cast(v4u, o4), ro, (int)(((64 * hp2 + tk) * 512 + 64 * ct + 4 * ec) * 4), 0, 16);
    }
  }
  STAMP(13);
  STAMP_OUT();
}

extern "C" void kernel_launch(void* const* d_in, const int* in_sizes, int n_in, void* d_out, int out_size, void* d_ws,
                              size_t ws_size, hipStream_t stream) {
  Params p;
  p.x = (const char*)d_in[0]; p.lre = (const char*)d_in[1]; p.lim = (const char*)d_in[2];
  p.bre = (const char*)d_in[3]; p.bim = (const char*)d_in[4]; p.cre = (const char*)d_in[5]; p.cim = (const char*)d_in[6];
  p.logdt = (const char*)d_in[7]; p.dskip = (const char*)d_in[8]; p.w1 = (const char*)d_in[10]; p.w2 = (const char*)d_in[11];
  p.out = (float*)d_out; p.ws = (char*)d_ws;
  (void)hipFuncSetAttribute((const void*)k_fused, hipFuncAttributeMaxDynamicSharedMemorySize, LDS_BYTES);
  k_fused<<<256, 512, LDS_BYTES, stream>>>(p);
}
```

```cpp
#include <hip/hip_runtime.h>
#include <hip/hip_bf16.h>
#include <type_traits>
#include <utility>

typedef unsigned v4u __attribute__((ext_vector_type(4)));
typedef unsigned v2u __attribute__((ext_vector_type(2)));
typedef float f32x4 __attribute__((ext_vector_type(4)));
typedef float f32x2 __attribute__((ext_vector_type(2)));
typedef short bf16x8 __attribute__((ext_vector_type(8)));
typedef short s16x4 __attribute__((ext_vector_type(4)));
typedef __bf16 bf16x2_t __attribute__((ext_vector_type(2)));
typedef unsigned long long u64;
#define LAS __attribute__((address_space(3)))
#define GAS __attribute__((address_space(1)))
#if defined(__HIP_DEVICE_COMPILE__)
#define NOPK __attribute__((target("no-packed-fp32-ops")))
#else
#define NOPK
#endif
#define DI __device__ __forceinline__ NOPK
#define RLX_AGENT __ATOMIC_RELAXED, __HIP_MEMORY_SCOPE_AGENT
#define RLX_WG __ATOMIC_RELAXED, __HIP_MEMORY_SCOPE_WORKGROUP

static constexpr unsigned HP_BYTES = 16 * 14336 + 32 * 4096;
static constexpr unsigned Y_OFF = 16 * 14336;
static constexpr size_t WS_Z = 0;
static constexpr size_t WS_Z_BYTES = (size_t)32 * HP_BYTES;
static constexpr size_t WS_GRAN = WS_Z + WS_Z_BYTES;
static constexpr size_t WS_SG = WS_GRAN + 16 * 16 * 512;
static constexpr size_t WS_FLAG = WS_SG + 2048 * 16;
static constexpr size_t WS_EPOCH = WS_FLAG + 2 * 16 * 16 * 128;
static constexpr size_t WS_END = WS_EPOCH + 128;

static constexpr int LDS_BYTES = 160 * 1024;
static constexpr int STG = 24576;
static constexpr int L_W1A = 96 * 1024;
static constexpr int L_CST = 0;
static constexpr int L_YB1 = L_CST;
static constexpr int L_HT = 16 * 1024;
static constexpr int L_E = 24 * 1024;
static constexpr int L_KEEP = 28 * 1024;
static constexpr int L_YB0 = 32 * 1024;
static constexpr int L_RING = 48 * 1024;
static constexpr int L_STAT = L_KEEP;
static constexpr int L_SU = L_KEEP + 1024;
static constexpr int L_SH = L_KEEP + 1280;
static constexpr int L_CNT = L_KEEP + 1792;
static constexpr int RING = 28672;
static constexpr int L_HIN = L_KEEP + 2048;
enum { C_W = 9, C_SH = 10, C_B2 = 11, C_E = 0, C_CX = 1, C_HIN = 2, C_HS = 4, C_FS = 5, C_Y1 = 7 };
#define ROT 1
#define CPACE 2
#define CODEPF 32
#define APRIO 2
#define MWARM 1
#define ROT2 1

DI bool wall(bool x) { return __builtin_amdgcn_ballot_w64(!x) == 0ull; }
DI f32x4 mfma16(v4u a, v4u b, f32x4 c) {
  return __builtin_amdgcn_mfma_f32_16x16x32_bf16(__builtin_bit_cast(bf16x8, a), __builtin_bit_cast(bf16x8, b), c, 0, 0, 0);
}
DI unsigned pk_bf16(float lo, float hi) {
  f32x2 v = {lo, hi};
  return __builtin_bit_cast(unsigned, __builtin_convertvector(v, bf16x2_t));
}
DI float bf2f(unsigned u) { return __builtin_bit_cast(float, u << 16); }
DI unsigned opaque(unsigned v) { asm volatile("" : "+v"(v)); return v; }
DI LAS char* lp(unsigned a) { return (LAS char*)(size_t)a; }

template <int IMM> DI void gld16(v4u& d, unsigned voff, const void* sb) {
  asm volatile("global_load_dwordx4 %0, %1, %2 offset:%3" : "=v"(d) : "v"(voff), "s"(sb), "i"(IMM) : "memory");
}
template <int IMM> DI void gld16_sc1(v4u& d, unsigned voff, const void* sb) {
  asm volatile("global_load_dwordx4 %0, %1, %2 offset:%3 sc1" : "=v"(d) : "v"(voff), "s"(sb), "i"(IMM) : "memory");
}
template <int IMM> DI void gld_u16(unsigned& d, unsigned voff, const void* sb) {
  asm volatile("global_load_ushort %0, %1, %2 offset:%3" : "=v"(d) : "v"(voff), "s"(sb), "i"(IMM) : "memory");
}
DI void gld8_sc1(v2u& d, unsigned voff, const void* sb) {
  asm volatile("global_load_dwordx2 %0, %1, %2 sc1" : "=v"(d) : "v"(voff), "s"(sb) : "memory");
}
template <int N> DI void wait_vm() { asm volatile("s_waitcnt vmcnt(%0)" :: "i"(N) : "memory"); }
template <int N> DI void wait_vm4(v4u& a, v4u& b, v4u& c, v4u& d) {
  asm volatile("s_waitcnt vmcnt(%4)" : "+v"(a), "+v"(b), "+v"(c), "+v"(d) : "i"(N) : "memory");
}
DI void wait_lgkm() { asm volatile("s_waitcnt lgkmcnt(0)" ::: "memory"); }
DI void bar() { wait_lgkm(); __builtin_amdgcn_s_barrier(); asm volatile("" ::: "memory"); }

DI v4u lds_rd16(LAS char* p) { return *(LAS v4u*)p; }
DI v4u lds_tr(LAS char* p0, LAS char* p1) {
  s16x4 a = __builtin_amdgcn_ds_read_tr16_b64_v4i16((LAS s16x4*)p0);
  s16x4 b = __builtin_amdgcn_ds_read_tr16_b64_v4i16((LAS s16x4*)p1);
  bf16x8 f = __builtin_shufflevector(a, b, 0, 1, 2, 3, 4, 5, 6, 7);
  return __builtin_bit_cast(v4u, f);
}
DI unsigned cnt_arrive(LAS char* lds, int c) {
  wait_lgkm();
  unsigned old = 0;
  if ((threadIdx.x & 63) == 0) old = __hip_atomic_fetch_add((LAS unsigned*)(lds + L_CNT) + c, 1u, RLX_WG);
  return __builtin_amdgcn_readfirstlane(old);
}
DI void cnt_wait(LAS char* lds, int c, unsigned target) {
  unsigned spins = 0;
  while ((unsigned)__builtin_amdgcn_readfirstlane(__hip_atomic_load((LAS unsigned*)(lds + L_CNT) + c, RLX_WG)) < target) {
    if (++spins > (1u << 24)) break;
    __builtin_amdgcn_s_sleep(1);
  }
  asm volatile("" ::: "memory");
}
struct cpx { float r, i; };
DI cpx cmul(cpx a, cpx b) { return {a.r * b.r - a.i * b.i, a.r * b.i + a.i * b.r}; }
DI cpx cfma(cpx a, cpx h, cpx u) {
  return {fmaf(a.r, h.r, fmaf(-a.i, h.i, u.r)), fmaf(a.r, h.i, fmaf(a.i, h.r, u.i))};
}
DI cpx csel(bool c, cpx a, cpx b) { return {c ? a.r : b.r, c ? a.i : b.i}; }

struct Params {
  const char *x, *lre, *lim, *bre, *bim, *cre, *cim, *logdt, *dskip, *w1, *w2;
  float* out;
  char* ws;
};

DI void st16(__amdgpu_buffer_rsrc_t rs, unsigned off, v4u v) { __builtin_amdgcn_raw_buffer_store_b128(v, rs, (int)off, 0, 16); }

DI void poll_flags(const char* fl, int l, u64 epoch) {
  GAS u64* fp = (GAS u64*)fl + (l & 3) * 16;
  unsigned spins = 0;
  while (!wall(__hip_atomic_load(fp, RLX_AGENT) == epoch)) {
    if (++spins > (1u << 20)) break;
    __builtin_amdgcn_s_sleep(4);
  }
  asm volatile("" ::: "memory");
}

DI f32x4 mfma16h(v2u a, v2u b, f32x4 c) {
  return __builtin_amdgcn_mfma_f32_16x16x16bf16_1k(__builtin_bit_cast(s16x4, a), __builtin_bit_cast(s16x4, b), c, 0, 0, 0);
}
DI v2u lds_tr1(LAS char* p0) { return __builtin_bit_cast(v2u, __builtin_amdgcn_ds_read_tr16_b64_v4i16((LAS s16x4*)p0)); }
template <int CTRL> DI float dpp_add(float v) {
  return v + __builtin_bit_cast(float, __builtin_amdgcn_update_dpp(0, __builtin_bit_cast(int, v), CTRL, 0xf, 0xf, true));
}
DI float wave_sum(float v) {
  v = dpp_add<0x111>(v); v = dpp_add<0x112>(v); v = dpp_add<0x114>(v); v = dpp_add<0x118>(v);
  v = v + __builtin_bit_cast(float, __builtin_amdgcn_update_dpp(0, __builtin_bit_cast(int, v), 0x142, 0xa, 0xf, false));
  v = v + __builtin_bit_cast(float, __builtin_amdgcn_update_dpp(0, __builtin_bit_cast(int, v), 0x143, 0xc, 0xf, false));
  return __builtin_bit_cast(float, __builtin_amdgcn_readlane(__builtin_bit_cast(int, v), 63));
}
DI u64 mk_gran(u64 epoch, float v) {
  unsigned vb = __builtin_bit_cast(unsigned, v);
  vb = (vb + 0x80u) >> 8;
  return (epoch << 24) | (u64)(vb & 0xffffffu);
}
DI void dma1(unsigned lds, const void* sb, unsigned v) {
  asm volatile("s_mov_b32 m0, %0\n\ts_nop 0\n\tglobal_load_lds_dwordx4 %2, %1" :: "s"(lds), "s"(sb), "v"(v) : "memory");
}
#define FD 3
constexpr int usz(int u) { return (u & 3) == 3 ? 2 : 4; }
constexpr int f_cnt(bool kindA, int u, int at, int st) {
  int n = 0; bool seen = false;
  for (int a = 0; a < FD; a++) { if (seen) n += usz(a); if (kindA && a == u) seen = true; }
  for (int v = 0; v <= at; v++) {
    const int steps = v < at ? usz(v) : st;
    for (int t = 0; t < steps; t++) {
      if (v + FD < 16) {
        const int np = usz(v + FD), ns = usz(v);
        for (int i = 0; i < np; i++)
          if (i * ns / np == t) { if (seen) n++; if (kindA && v + FD == u && i == np - 1) seen = true; }
      }
      if (v + 4 < 16) { if (seen) n++; if (!kindA && v + 4 == u && t == usz(v) - 1) seen = true; }
    }
  }
  return n;
}
constexpr int hv_t(int n) { return n < 32 ? 8 + n / 4 : 8 + (n - 32) / 3; }
constexpr int hv_m(int n) { return n < 32 ? n % 4 : 4 + (n - 32) % 3; }
template <int... I, class F> DI void static_for(std::integer_sequence<int, I...>, F f) { (f(std::integral_constant<int, I>{}), ...); }
#define SB0() __builtin_amdgcn_sched_barrier(0)

__global__ __launch_bounds__(512, 2) NOPK void k_fused(Params p) {
  extern __shared__ __attribute__((aligned(16))) char smem[];
  LAS char* lds = (LAS char*)smem;
  const int tid = threadIdx.x, l = tid & 63;
  const int w8 = __builtin_amdgcn_readfirstlane(tid >> 6);
  const int role = w8 >> 2, w = w8 & 3;
  const int bid = blockIdx.x, xcd = bid & 7, r = bid >> 3;
  const int j = 4 * (xcd >> 1) + (r >> 3);
  const int s = 8 * (xcd & 1) + (r & 7);
  const int hp2 = 8 * (xcd >> 1) + (r >> 2);
  const int ct = 4 * (xcd & 1) + (r & 3);
  const int odd = s & 1;
  const int i16 = l & 15, q = l >> 4;
  const int ta = i16 >> 2, tp = i16 & 3;
  const unsigned ldsb = (unsigned)(size_t)lds;
  const __amdgpu_buffer_rsrc_t rs = __builtin_amdgcn_make_buffer_rsrc((void*)p.ws, 0, (int)WS_END, 0x00020000);
  const v4u ones = {0x3F803F80u, 0x3F803F80u, 0x3F803F80u, 0x3F803F80u};

  const int rot = ROT ? ((2 * (r >> 3) + (r & 7)) & 7) : 0;
  u64 ep0;
  asm volatile("s_load_dwordx2 %0, %1, 0x0" : "=s"(ep0) : "s"(p.ws + WS_EPOCH) : "memory");
  unsigned plr[2], pli[2], pdt[2];
  v4u xr[2];
  unsigned cdum = 0;
  if (role == 0) {
    const char* rb = p.x + (size_t)(128 * j + 8 * s + 2 * w) * 1024;
    gld16<0>(xr[0], (unsigned)l * 16, rb); gld16<1024>(xr[1], (unsigned)l * 16, rb);
    const unsigned co = (unsigned)(32 * s + i16) * 2;
    gld_u16<0>(plr[0], co, p.lre); gld_u16<32>(plr[1], co, p.lre);
    gld_u16<0>(pli[0], co, p.lim); gld_u16<32>(pli[1], co, p.lim);
    gld_u16<0>(pdt[0], co, p.logdt); gld_u16<32>(pdt[1], co, p.logdt);
    {
      u64 pc;
      asm volatile("s_getpc_b64 %0" : "=s"(pc));
      const int ci = r * 4 + w;
      const char* cb = (const char*)(pc & ~255ull) + (ci < CODEPF ? ci : 0) * 1024;
      asm volatile("global_load_dword %0, %1, %2" : "=v"(cdum) : "v"((unsigned)l * 16), "s"(cb) : "memory");
    }
#pragma unroll
    for (int k = 0; k < 3; k++) {
      int row = bid * 11 + w + 4 * k;
      if (w + 4 * k >= 11) row = bid * 11;
      const char* src = row < 1792 ? p.w2 + (size_t)row * 1024 : (row < 2304 ? p.cre + (size_t)(row - 1792) * 1024 : p.cim + (size_t)(row - 2304) * 1024);
      asm volatile("global_load_dword %0, %1, %2" : "+v"(cdum) : "v"((unsigned)l * 16), "s"(src) : "memory");
    }
  }
  unsigned vx0, vx1, vu, vwA, vwB;
  {
    const int r8 = l >> 3, c8 = l & 7;
    const int tok0 = 8 * (r8 >> 2) + (r8 & 3);
    vx0 = (unsigned)(tok0 * 1024 + ((c8 ^ (r8 >> 1)) & 7) * 16);
    vx1 = (unsigned)((tok0 + 16) * 1024 + ((c8 ^ (4 + (r8 >> 1))) & 7) * 16);
    const int ch = 8 * w + r8, iu = 8 * (w & 1) + r8;
    vu = (unsigned)(ch * 1024 + ((c8 ^ (iu >> 1)) & 7) * 16);
    const int winoff = 448 * (s >> 1) + (odd ? 192 : 0);
    vwA = (unsigned)((16 * w + q) * 3584 + winoff + ((((i16 >> 1) ^ q) << 1) | (i16 & 1)) * 16);
    vwB = (unsigned)((16 * w + 8 + q) * 3584 + winoff + ((((i16 >> 1) ^ (q | 4)) << 1) | (i16 & 1)) * 16);
  }
  const char* xb = p.x + (size_t)(128 * j + 32 * (w >> 1) + 4 * (w & 1)) * 1024;
  const char* ub0 = p.bre + (size_t)(32 * s) * 1024;
  const char* ub1 = p.bim + (size_t)(32 * s) * 1024;
#define PIECE(N, I)                                                                                  \
  {                                                                                                  \
    constexpr int h_ = (N) / 3, n_ = (N) % 3;                                                        \
    const int kq_ = ((I) + rot) & 7;                                                                 \
    const unsigned d_ = ldsb + w * 3072 + ((I) & 3) * STG + h_ * 12288 + n_ * 1024;                  \
    if (n_ == 0) dma1(d_, xb + h_ * 65536 + kq_ * 128, vx0);                                         \
    if (n_ == 1) dma1(d_, xb + h_ * 65536 + kq_ * 128, vx1);                                         \
    if (n_ == 2) dma1(d_, (h_ ? ub1 : ub0) + kq_ * 128, vu);                                         \
  }
#define PALL(I) PIECE(0, I) PIECE(1, I) PIECE(2, I) PIECE(3, I) PIECE(4, I) PIECE(5, I)
#define W1P(C, n) dma1(ldsb + L_W1A + ((C) & 3) * 16384 + (16 * w + 4 * (n)) * 256,          \
                       p.w1 + (size_t)((((C) + rot) & 7) * 64 + ((n) & 1) * 4) * 3584, ((n) & 2) ? vwB : vwA)
  if (role == 1) {
    PALL(0)
    __builtin_amdgcn_s_barrier();
    PALL(1)
  } else {
    __builtin_amdgcn_s_barrier();
  }
  asm volatile("s_waitcnt lgkmcnt(0)" : "+s"(ep0) :: "memory");
  const u64 epoch = ep0 + 1ull;
  const unsigned tlo = (unsigned)(epoch & 0xffu), thi = (unsigned)(epoch >> 8);

  const unsigned sw0 = (unsigned)((i16 & 7) * 128 + (((0 + q) ^ (i16 >> 1)) & 7) * 16);
  const unsigned sw1 = (unsigned)((i16 & 7) * 128 + (((4 + q) ^ (i16 >> 1)) & 7) * 16);
  const unsigned xo = (unsigned)(2 * w * 3072 + (i16 >> 3) * 1024);
  const unsigned tro = (unsigned)((8 * q + ta) * 256 + 8 * tp);
  const int trkey = ta | ((q & 1) << 2);
  const unsigned zb = (unsigned)(2 * j + (w >> 1)) * HP_BYTES + (unsigned)l * 16u;
  const char* ab = p.ws + WS_Z + (size_t)hp2 * HP_BYTES;
  const unsigned avoff = (unsigned)l * 16;
  const int rot2 = ROT2 == 1 ? (hp2 & 3) : ROT2 == 2 ? (ct & 3) : 0;

  const int ec = tid & 15, et = tid >> 4;
  v2u xe[2], dk;
#define EPI_LOADS()                                                                                  \
  {                                                                                                  \
    xe[0] = *(const GAS v2u*)(p.x + (size_t)(64 * hp2 + et) * 1024 + (64 * ct + 4 * ec) * 2);        \
    xe[1] = *(const GAS v2u*)(p.x + (size_t)(64 * hp2 + et + 32) * 1024 + (64 * ct + 4 * ec) * 2);   \
    dk = *(const GAS v2u*)(p.dskip + (64 * ct + 4 * ec) * 2);                                        \
  }
  if (role == 0) {
    if (APRIO) __builtin_amdgcn_s_setprio(APRIO);
    asm volatile("s_waitcnt vmcnt(%8)" : "+v"(plr[0]), "+v"(plr[1]), "+v"(pli[0]), "+v"(pli[1]), "+v"(pdt[0]), "+v"(pdt[1]), "+v"(xr[0]), "+v"(xr[1]) : "i"((CODEPF ? 1 : 0) + (MWARM ? 3 : 0)) : "memory");
#pragma unroll
    for (int n = 0; n < 2; n++) {
      float v[8];
#pragma unroll
      for (int e = 0; e < 4; e++) { v[2 * e] = bf2f(xr[n][e] & 0xffffu); v[2 * e + 1] = __builtin_bit_cast(float, xr[n][e] & 0xffff0000u); }
      const float mean = wave_sum(((v[0] + v[1]) + (v[2] + v[3])) + ((v[4] + v[5]) + (v[6] + v[7]))) * (1.f / 512.f);
      float sq = 0.f;
#pragma unroll
      for (int e = 0; e < 8; e++) { const float d = v[e] - mean; sq = fmaf(d, d, sq); }
      const float rstd = rsqrtf(wave_sum(sq) * (1.f / 512.f) + 1e-5f);
      if (l == 0) *(LAS f32x2*)(lds + LDS_BYTES - 64 + (2 * w + n) * 8) = f32x2{mean, rstd};
    }
    wait_lgkm();
    cpx a1[2], cc[2], a8[2], pw[2], a128[2];
#pragma unroll
    for (int nb = 0; nb < 2; nb++) {
      const float lr = bf2f(plr[nb]), li = bf2f(pli[nb]);
      const float dtf = expf(bf2f(pdt[nb]));
      const float dt = bf2f(pk_bf16(dtf, 0.f) & 0xffffu);
      const float e = expf(dt * lr);
      float sn, cs;
      sincosf(dt * li, &sn, &cs);
      cpx a = {e * cs, e * sn};
      a1[nb] = a;
      const float nr = a.r - 1.f, ni = a.i, den = lr * lr + li * li;
      cc[nb] = {2.f * (nr * lr + ni * li) / den, 2.f * (ni * lr - nr * li) / den};
      cpx t = cmul(a, a); t = cmul(t, t); t = cmul(t, t);
      a8[nb] = t;
      cpx a16 = cmul(t, t), a32 = cmul(a16, a16), a64 = cmul(a32, a32);
      a128[nb] = cmul(a64, a64);
      cpx one = {1.f, 0.f};
      cpx pq = cmul(csel(q & 1, t, one), csel(q & 2, a16, one));
      cpx pwv = cmul(csel(w & 1, a32, one), csel(w & 2, a64, one));
      pw[nb] = cmul(pq, pwv);
    }
#pragma unroll
    for (int nb = 0; nb < 2; nb++)
      asm volatile("" : "+v"(a1[nb].r), "+v"(a1[nb].i), "+v"(cc[nb].r), "+v"(cc[nb].i), "+v"(a8[nb].r), "+v"(a8[nb].i), "+v"(pw[nb].r),
                   "+v"(pw[nb].i), "+v"(a128[nb].r), "+v"(a128[nb].i));
    v4u cf[8][4];
    const unsigned cvoff = (unsigned)((64 * ct + (l >> 2)) * 1024 + (l & 3) * 16);
#define CLOAD(K0)                                                                         \
  _Pragma("unroll") for (int k = K0; k < K0 + 4; k++) {                                   \
    const int kr = (k + 2 * rot2) & 7;                                                    \
    const int sidx = 4 * w + 2 * (kr >> 2) + (kr & 1);                                    \
    const char* cb = (((kr >> 1) & 1) ? p.cim : p.cre) + sidx * 64;                       \
    _Pragma("unroll") for (int n = 0; n < 4; n++) gld16<0>(cf[k][n], cvoff, cb + n * 16384); \
    if (CPACE) __builtin_amdgcn_s_sleep(CPACE);                                           \
  }
#define CWAIT(K0, N)                                                                      \
  asm volatile("s_waitcnt vmcnt(%16)" : "+v"(cf[K0][0]), "+v"(cf[K0][1]), "+v"(cf[K0][2]), "+v"(cf[K0][3]), "+v"(cf[K0 + 1][0]),   \
               "+v"(cf[K0 + 1][1]), "+v"(cf[K0 + 1][2]), "+v"(cf[K0 + 1][3]), "+v"(cf[K0 + 2][0]), "+v"(cf[K0 + 2][1]),          \
               "+v"(cf[K0 + 2][2]), "+v"(cf[K0 + 2][3]), "+v"(cf[K0 + 3][0]), "+v"(cf[K0 + 3][1]), "+v"(cf[K0 + 3][2]),          \
               "+v"(cf[K0 + 3][3]) : "i"(N) : "memory");
#define CXPOSE(K0)                                                                        \
  _Pragma("unroll") for (int k = K0; k < K0 + 4; k++) {                                   \
    _Pragma("unroll") for (int n = 0; n < 4; n++) *(LAS v4u*)(lp(cwa) + n * 1024) = cf[k][n]; \
    _Pragma("unroll") for (int n = 0; n < 4; n++) cf[k][n] = lds_rd16(lp(cra) + n * 1024);  \
  }
    v2u sg;
    const unsigned sgoff = (unsigned)l * 8;
    const char* sgb = p.ws + WS_SG + (size_t)(128 * j + 32 * w) * 16;
    f32x4 au[2][2][2], asu;
#pragma unroll
    for (int b = 0; b < 2; b++)
#pragma unroll
      for (int nb = 0; nb < 2; nb++) { au[b][nb][0] = f32x4{0, 0, 0, 0}; au[b][nb][1] = f32x4{0, 0, 0, 0}; }
    asu = f32x4{0, 0, 0, 0};
    {
      const unsigned uo = (unsigned)((i16 >> 3) * 3072 + 2048);
      const int gs = (w & 1) * 2 + (w >> 1);
      unsigned cur = 0;
      v4u xd[2][2], wd[2][4], sd[2];
#pragma unroll
      for (int t = 0; t < 2; t++) xd[t][0] = xd[t][1] = wd[t][0] = wd[t][1] = wd[t][2] = wd[t][3] = sd[t] = v4u{0, 0, 0, 0};
#define A_STEP(RD, T, PX, PU, PS)                                                                     \
  {                                                                                                   \
    _Pragma("unroll") for (int g = 0; g < 4; g++) {                                                   \
      au[0][g & 1][g >> 1] = mfma16(xd[T][0], wd[T][g], au[0][g & 1][g >> 1]);                        \
      au[1][g & 1][g >> 1] = mfma16(xd[T][1], wd[T][g], au[1][g & 1][g >> 1]);                        \
      if (RD) wd[T][g] = lds_rd16(PU + g * 6144);                                                     \
      SB0();                                                                                          \
    }                                                                                                 \
    asu = mfma16(ones, sd[T], asu);                                                                   \
    if (RD) { xd[T][0] = lds_rd16(PX); xd[T][1] = lds_rd16(PX + 3072); sd[T] = lds_rd16(PS); }        \
    SB0();                                                                                            \
  }
#pragma unroll 1
      for (int it = 0; it < 8; it++) {
        const unsigned b0 = ldsb + cur;
        LAS char* px0 = lp(opaque(b0 + xo + sw0)); LAS char* px1 = lp(opaque(b0 + xo + sw1));
        LAS char* pu0 = lp(opaque(b0 + uo + sw0)); LAS char* pu1 = lp(opaque(b0 + uo + sw1));
        LAS char* ps0 = lp(opaque(b0 + uo + sw0 + gs * 6144)); LAS char* ps1 = lp(opaque(b0 + uo + sw1 + gs * 6144));
        cur = cur >= 3 * STG ? 0 : cur + STG;
        asm volatile("" ::: "memory");
        __builtin_amdgcn_s_barrier();
        asm volatile("" ::: "memory");
        if (it == 0 && w == 0 && l < 16)
          __hip_atomic_store((GAS u64*)(p.ws + WS_SG) + (size_t)(128 * j + 8 * s) * 2 + l,
                             mk_gran(epoch, *(LAS float*)(lds + LDS_BYTES - 64 + l * 4)), RLX_AGENT);
        SB0();
        A_STEP(true, 0, px0, pu0, ps0)
        A_STEP(true, 1, px1, pu1, ps1)
      }
      gld8_sc1(sg, sgoff, sgb);
      A_STEP(false, 0, lds, lds, lds)
      A_STEP(false, 1, lds, lds, lds)
#undef A_STEP
    }
    {
      asm volatile("s_waitcnt vmcnt(0)" : "+v"(sg), "+v"(cdum) :: "memory");
      if (tid < 16) ((LAS unsigned*)(lds + L_CNT))[tid] = 0u;
      unsigned spins = 0;
      while (!wall(sg.y == thi && (sg.x >> 24) == tlo)) {
        if (++spins > (1u << 22)) break;
        __builtin_amdgcn_s_sleep(1);
        gld8_sc1(sg, sgoff, sgb);
        asm volatile("s_waitcnt vmcnt(0)" : "+v"(sg), "+v"(cdum) :: "memory");
      }
      const int tk = l >> 1;
      ((LAS unsigned*)(lds + L_STAT))[(w * 32 + ((tk >> 2) & 1) * 16 + ((tk & 3) | ((tk >> 3) << 2))) * 2 + (l & 1)] = sg.x << 8;
      if (q == 0) ((LAS float*)(lds + L_SU))[w * 16 + i16] = asu[0];
    }
    bar();
    cpx u[2][8];
    {
      f32x2 stt[2][4];
#pragma unroll
      for (int b = 0; b < 2; b++) {
        f32x4 s0 = *(LAS f32x4*)(lds + L_STAT + (w * 32 + b * 16 + 4 * q) * 8);
        f32x4 s1 = *(LAS f32x4*)(lds + L_STAT + (w * 32 + b * 16 + 4 * q) * 8 + 16);
        stt[b][0] = f32x2{s0[0], s0[1]}; stt[b][1] = f32x2{s0[2], s0[3]};
        stt[b][2] = f32x2{s1[0], s1[1]}; stt[b][3] = f32x2{s1[2], s1[3]};
      }
#pragma unroll
      for (int nb = 0; nb < 2; nb++) {
        const float sre = ((LAS float*)(lds + L_SU))[(nb * 2 + 0) * 16 + i16];
        const float sim = ((LAS float*)(lds + L_SU))[(nb * 2 + 1) * 16 + i16];
        cpx h = {0.f, 0.f};
#pragma unroll
        for (int k = 0; k < 8; k++) {
          const int b = k >> 2, rg = k & 3;
          const float m = stt[b][rg][0], rr = stt[b][rg][1];
          u[nb][k].r = fmaf(-m, sre, au[b][nb][0][rg]) * rr;
          u[nb][k].i = fmaf(-m, sim, au[b][nb][1][rg]) * rr;
          h = cfma(a1[nb], h, u[nb][k]);
        }
        *(LAS f32x2*)(lds + L_E + ((nb * 16 + i16) * 16 + 4 * w + q) * 8) = f32x2{h.r, h.i};
      }
    }
    cnt_arrive(lds, C_E);
    cnt_wait(lds, C_E, 4);
    cpx cl[2];
    {
      cpx c[2] = {{0.f, 0.f}, {0.f, 0.f}};
      LAS f32x4* e4a = (LAS f32x4*)(lds + L_E + i16 * 128);
      LAS f32x4* e4b = (LAS f32x4*)(lds + L_E + (16 + i16) * 128);
#pragma unroll 1
      for (int k = 0; k < 2 * w; k++) {
        f32x4 ea = e4a[k], eb = e4b[k];
        c[0] = cfma(a8[0], c[0], cpx{ea[0], ea[1]}); c[1] = cfma(a8[1], c[1], cpx{eb[0], eb[1]});
        c[0] = cfma(a8[0], c[0], cpx{ea[2], ea[3]}); c[1] = cfma(a8[1], c[1], cpx{eb[2], eb[3]});
      }
      cpx tot[2];
#pragma unroll
      for (int nb = 0; nb < 2; nb++) {
        LAS f32x4* e4 = nb ? e4b : e4a;
        f32x4 e0 = e4[2 * w], e1 = e4[2 * w + 1];
        cpx c1 = cfma(a8[nb], c[nb], cpx{e0[0], e0[1]});
        cpx c2 = cfma(a8[nb], c1, cpx{e0[2], e0[3]});
        cpx c3 = cfma(a8[nb], c2, cpx{e1[0], e1[1]});
        tot[nb] = cfma(a8[nb], c3, cpx{e1[2], e1[3]});
        cl[nb] = csel(q & 2, csel(q & 1, c3, c2), csel(q & 1, c1, c[nb]));
      }
      if (w == 3 && j < 15) {
        const cpx t = (q & 2) ? tot[1] : tot[0];
        const float v = (q & 1) ? t.i : t.r;
        const u64 g = mk_gran(epoch, v);
        GAS u64* gp = (GAS u64*)(p.ws + WS_GRAN + (size_t)(j * 16 + s) * 512) + (((q >> 1) * 16 + i16) * 2 + (q & 1));
        __hip_atomic_store(gp, g, RLX_AGENT);
      }
    }
    const unsigned cwa = opaque(ldsb + L_CST + w * 4096 + (l >> 2) * 64 + (((l & 3) ^ ((-(l >> 4)) & 3)) * 16));
    const unsigned cra = opaque(ldsb + L_CST + w * 4096 + i16 * 64 + ((q ^ ((-(i16 >> 2)) & 3)) * 16));
    CLOAD(0)
    CWAIT(0, 0)
    CXPOSE(0)
    if (w < 2) {
      const char* gb = p.ws + WS_GRAN + (size_t)s * 512 + w * 256;
      unsigned spins = 0;
      if (j > 0) {
        GAS u64* pp = (GAS u64*)(gb + (size_t)(l < j ? l : 0) * 8192 + 248);
        while (!wall((__hip_atomic_load(pp, RLX_AGENT) >> 24) == (epoch & 0xffffffffffull))) {
          if (++spins > (1u << 20)) break;
          __builtin_amdgcn_s_sleep(2);
        }
      }
      v4u gr[4];
      for (;;) {
#pragma unroll
        for (int m = 0; m < 4; m++) {
          const int pn = (4 * m + q) < j ? 4 * m + q : 0;
          gld16_sc1<0>(gr[m], (unsigned)(pn * 8192 + i16 * 16), gb);
        }
        wait_vm4<0>(gr[0], gr[1], gr[2], gr[3]);
        bool ok = true;
#pragma unroll
        for (int m = 0; m < 4; m++) {
          const bool need = (4 * m + q) < j;
          const bool good = (gr[m].y == thi) && ((gr[m].x >> 24) == tlo) && (gr[m].w == thi) && ((gr[m].z >> 24) == tlo);
          ok = ok && (good || !need);
        }
        if (wall(ok)) break;
        if (++spins > (1u << 20)) break;
        __builtin_amdgcn_s_sleep(2);
      }
      const cpx b128 = csel(w != 0, a128[1], a128[0]);
      const cpx a256 = cmul(b128, b128);
      const cpx a512 = cmul(a256, a256);
      cpx acc = {0.f, 0.f};
#pragma unroll
      for (int m = 0; m < 4; m++) {
        const bool need = (4 * m + q) < j;
        cpx e = {__builtin_bit_cast(float, gr[m].x << 8), __builtin_bit_cast(float, gr[m].z << 8)};
        acc = csel(need, cfma(a512, acc, e), acc);
      }
      const int dd = (j - 1 - q) & 3;
      cpx one = {1.f, 0.f};
      cpx fz = cmul(csel(dd & 1, b128, one), csel(dd & 2, a256, one));
      acc = cmul(acc, fz);
      *(LAS f32x2*)(lds + L_HIN + ((w * 16 + i16) * 4 + q) * 8) = f32x2{acc.r, acc.i};
      cnt_arrive(lds, C_HIN);
    }
    cnt_wait(lds, C_HIN, 2);
    {
      LAS char* ht = lds + L_HT + w * 2048;
      unsigned hpk[2][2][2][2];
#pragma unroll
      for (int nb = 0; nb < 2; nb++) {
        const f32x4 h01 = *(LAS f32x4*)(lds + L_HIN + (nb * 16 + i16) * 32);
        const f32x4 h23 = *(LAS f32x4*)(lds + L_HIN + (nb * 16 + i16) * 32 + 16);
        const cpx acc = {(h01[0] + h01[2]) + (h23[0] + h23[2]), (h01[1] + h01[3]) + (h23[1] + h23[3])};
        cpx h = cfma(pw[nb], acc, cl[nb]);
        float orr[8], oi[8];
#pragma unroll
        for (int k = 0; k < 8; k++) {
          h = cfma(a1[nb], h, u[nb][k]);
          orr[k] = cc[nb].r * h.r - cc[nb].i * h.i;
          oi[k] = -(cc[nb].r * h.i + cc[nb].i * h.r);
        }
#pragma unroll
        for (int b = 0; b < 2; b++) {
          hpk[nb][0][b][0] = pk_bf16(orr[4 * b], orr[4 * b + 1]); hpk[nb][0][b][1] = pk_bf16(orr[4 * b + 2], orr[4 * b + 3]);
          hpk[nb][1][b][0] = pk_bf16(oi[4 * b], oi[4 * b + 1]);   hpk[nb][1][b][1] = pk_bf16(oi[4 * b + 2], oi[4 * b + 3]);
        }
      }
      const unsigned zy = zb + Y_OFF + (unsigned)(4 * (s >> 1) + (s & 1)) * 4096u + (unsigned)(2 * (w & 1)) * 1024u;
#pragma unroll
      for (int b = 0; b < 2; b++) {
#pragma unroll
        for (int nb = 0; nb < 2; nb++)
#pragma unroll
          for (int part = 0; part < 2; part++)
            *(LAS v2u*)(ht + (part * 32 + nb * 16 + i16) * 32 + q * 8) = v2u{hpk[nb][part][b][0], hpk[nb][part][b][1]};
#pragma unroll
        for (int part = 0; part < 2; part++) {
          LAS char* rp = ht + (part * 32 + 8 * q + ta) * 32 + tp * 8;
          v4u v = lds_tr(rp, rp + 4 * 32);
          st16(rs, (unsigned)WS_Z + zy + (unsigned)part * 8192u + b * 1024u, v);
        }
      }
    }
    CLOAD(4)
    wait_vm<16>();
    if (cnt_arrive(lds, C_HS) == 3 && l < 16)
      __hip_atomic_store((GAS u64*)(p.ws + WS_FLAG) + (j * 16 + s) * 16 + l, epoch, RLX_AGENT);
    CWAIT(4, 0)
    CXPOSE(4)
    cnt_arrive(lds, C_CX);
    EPI_LOADS()
    poll_flags(p.ws + WS_FLAG + (size_t)(j * 16 + 4 * w) * 128, l, epoch);
    asm volatile("" : "+v"(xe[0]), "+v"(xe[1]), "+v"(dk));
    f32x4 acc[4][4];
#pragma unroll
    for (int a = 0; a < 4; a++)
#pragma unroll
      for (int b = 0; b < 4; b++) acc[a][b] = f32x4{0, 0, 0, 0};
    v4u af[3][4];
    const char* ay = ab + Y_OFF + (size_t)(8 * w) * 4096;
#define YL(K, i) gld16_sc1<(i) * 1024>(af[(K) % 3][i], avoff, ay + ((((K) + 2 * rot2) & 7) * 4096))
#define YST(K, tb)                                                                                  \
  {                                                                                                 \
    _Pragma("unroll") for (int nb = 0; nb < 4; nb++) acc[nb][tb] = mfma16(cf[K][nb], af[(K) % 3][tb], acc[nb][tb]); \
    SB0();                                                                                          \
    if ((K) + 2 < 8) YL((K) + 2, tb);                                                               \
    SB0();                                                                                          \
  }
#define YS(K)                                                                                       \
  {                                                                                                 \
    wait_vm4<((K) < 7 ? 4 : 0)>(af[(K) % 3][0], af[(K) % 3][1], af[(K) % 3][2], af[(K) % 3][3]);    \
    YST(K, 0) YST(K, 1) YST(K, 2) YST(K, 3)                                                         \
  }
    YL(0, 0); YL(0, 1); YL(0, 2); YL(0, 3); YL(1, 0); YL(1, 1); YL(1, 2); YL(1, 3);
    YS(0) YS(1) YS(2) YS(3) YS(4) YS(5) YS(6) YS(7)
#undef YL
#undef YS
#undef YST
    cnt_wait(lds, C_CX, 4);
    cnt_wait(lds, C_B2, 4);
    {
      LAS char* yb = lds + ((w & 1) ? L_YB1 : L_YB0);
      if (w < 2) cnt_wait(lds, C_Y1 + (w & 1), 1);
#pragma unroll
      for (int tb = 0; tb < 4; tb++) {
        const int tk = 32 * (tb >> 1) + 8 * (i16 >> 2) + 4 * (tb & 1) + (i16 & 3);
#pragma unroll
        for (int nb = 0; nb < 4; nb++) {
          LAS f32x4* d = (LAS f32x4*)(yb + tk * 256 + (((4 * nb + q) ^ i16) & 15) * 16);
          if (w < 2) *d = *d + acc[nb][tb]; else *d = acc[nb][tb];
        }
      }
      if (w >= 2) cnt_arrive(lds, C_Y1 + (w & 1));
    }
  } else {
    v4u xf[2][16];
    f32x4 ah[7][2];
#pragma unroll
    for (int mb = 0; mb < 7; mb++) { ah[mb][0] = f32x4{0, 0, 0, 0}; ah[mb][1] = f32x4{0, 0, 0, 0}; }
    unsigned pt[7];
#pragma unroll
    for (int mb = 0; mb < 7; mb++) pt[mb] = opaque(ldsb + L_W1A + tro + (unsigned)(((mb + odd) ^ trkey) * 32));
    const int m1 = w < 3 ? w + 4 : 6;
    const unsigned hq0 = opaque(ldsb + L_W1A + tro + (unsigned)(((w + odd) ^ trkey) * 32));
    const unsigned hq1 = opaque(ldsb + L_W1A + tro + (unsigned)(((m1 + odd) ^ trkey) * 32));
    f32x4 sh0 = {0, 0, 0, 0}, sh1 = {0, 0, 0, 0};
    {
      LAS char* px0 = lp(opaque(ldsb + xo + sw0)); LAS char* px1 = lp(opaque(ldsb + xo + sw1));
#define L1IT(I, NW)                                                                                   \
  {                                                                                                   \
    wait_vm<NW>();                                                                                    \
    __builtin_amdgcn_s_barrier();                                                                     \
    asm volatile("" ::: "memory");                                                                    \
    xf[0][2 * (I)] = lds_rd16(px0 + ((I) & 3) * STG); xf[1][2 * (I)] = lds_rd16(px0 + ((I) & 3) * STG + 3072); \
    xf[0][2 * (I) + 1] = lds_rd16(px1 + ((I) & 3) * STG); xf[1][2 * (I) + 1] = lds_rd16(px1 + ((I) & 3) * STG + 3072); \
    SB0();                                                                                            \
    if ((I) < 3 || (I) == 7) {                                                                        \
      if ((I) < 6) { PALL((I) + 2) }                                                                  \
      W1P(I, 0); W1P(I, 1); W1P(I, 2); W1P(I, 3);                                                     \
    } else {                                                                                          \
      constexpr int c_ = (I) - 3;                                                                     \
      v4u wa[7], wb_[7], h0[2], h1[2];                                                                \
      _Pragma("unroll") for (int mb = 0; mb < 7; mb++) wa[mb] = lds_tr(lp(pt[mb]) + c_ * 16384, lp(pt[mb]) + c_ * 16384 + 1024); \
      SB0();                                                                                          \
      if ((I) < 6) { PIECE(0, (I) + 2) PIECE(1, (I) + 2) PIECE(2, (I) + 2) }                          \
      SB0();                                                                                          \
      _Pragma("unroll") for (int mb = 0; mb < 7; mb++) {                                              \
        ah[mb][0] = mfma16(wa[mb], xf[0][2 * c_], ah[mb][0]);                                         \
        ah[mb][1] = mfma16(wa[mb], xf[1][2 * c_], ah[mb][1]);                                         \
        wb_[mb] = lds_tr(lp(pt[mb]) + c_ * 16384 + 8192, lp(pt[mb]) + c_ * 16384 + 9216);             \
        if ((I) < 6) { if (mb == 1) PIECE(3, (I) + 2) if (mb == 3) PIECE(4, (I) + 2) if (mb == 5) PIECE(5, (I) + 2) } \
        SB0();                                                                                        \
      }                                                                                               \
      _Pragma("unroll") for (int mb = 0; mb < 7; mb++) {                                              \
        ah[mb][0] = mfma16(wb_[mb], xf[0][2 * c_ + 1], ah[mb][0]);                                    \
        ah[mb][1] = mfma16(wb_[mb], xf[1][2 * c_ + 1], ah[mb][1]);                                    \
        if (mb == 0) { h0[0] = lds_tr(lp(hq0) + c_ * 16384, lp(hq0) + c_ * 16384 + 1024); h1[0] = lds_tr(lp(hq1) + c_ * 16384, lp(hq1) + c_ * 16384 + 1024); } \
        if (mb == 1) { h0[1] = lds_tr(lp(hq0) + c_ * 16384 + 8192, lp(hq0) + c_ * 16384 + 9216); h1[1] = lds_tr(lp(hq1) + c_ * 16384 + 8192, lp(hq1) + c_ * 16384 + 9216); } \
        if (mb == 0) W1P(I, 0); if (mb == 2) W1P(I, 1); if (mb == 4) W1P(I, 2); if (mb == 6) W1P(I, 3); \
        SB0();                                                                                        \
      }                                                                                               \
      sh0 = mfma16(h0[0], ones, sh0); sh1 = mfma16(h1[0], ones, sh1);                                 \
      sh0 = mfma16(h0[1], ones, sh0); sh1 = mfma16(h1[1], ones, sh1);                                 \
    }                                                                                                 \
    wait_lgkm();                                                                                      \
  }
      L1IT(0, 6) L1IT(1, 10) L1IT(2, 14) L1IT(3, 14) L1IT(4, 14) L1IT(5, 14) L1IT(6, 14) L1IT(7, 8)
#undef L1IT
    }
    wait_vm<8>();
    bar();
    const unsigned ring = ldsb + L_RING + w * RING;
    unsigned fv;
    {
      const int row = l >> 3, c16p = l & 7;
      fv = (unsigned)(row * 1024 + ct * 128 + ((((c16p >> 1) ^ ((row >> 1) & 3)) << 1) | (c16p & 1)) * 16);
    }
    const char* w2b = p.w2 + (size_t)(448 * w) * 1024;
#define WP(N, n) dma1(ring + ((N) & 1) * 14336 + (n) * 1024, w2b + (size_t)(((N) + rot2) & 3) * (112 * 1024) + (n) * 8192, fv)
    {
      v4u wd[7];
      static_for(std::make_integer_sequence<int, 7>{}, [&](auto NC) __attribute__((always_inline)) NOPK {
        constexpr int n = decltype(NC)::value, T2 = hv_t(n), m2 = hv_m(n);
        wd[n] = lds_tr(lp(pt[m2]) + (T2 & 7) * 8192, lp(pt[m2]) + (T2 & 7) * 8192 + 1024);
      });
      const unsigned zf = zb + (unsigned)s * 14336u;
      float nm[2], r2[2];
      v4u hs0, hs1;
      unsigned pkd[7][2][2];
      auto epi = [&](auto MB, auto BB) __attribute__((always_inline)) NOPK {
        constexpr int mb = decltype(MB)::value, b = decltype(BB)::value;
        const f32x4 sh = *(LAS f32x4*)(lds + L_SH + (mb * 16 + 4 * q) * 4);
        float y[4];
#pragma unroll
        for (int e = 0; e < 4; e++) {
          const float d = fmaf(sh[e], nm[b], ah[mb][b][e]);
          y[e] = d * __builtin_amdgcn_rcpf(1.f + __builtin_amdgcn_exp2f(d * r2[b]));
        }
        pkd[mb][b][0] = pk_bf16(y[0], y[1]); pkd[mb][b][1] = pk_bf16(y[2], y[3]);
        if constexpr ((mb & 1) == 1) {
          v4u v = {pkd[mb - 1][b][0], pkd[mb - 1][b][1], pkd[mb][b][0], pkd[mb][b][1]};
          st16(rs, (unsigned)WS_Z + zf + (unsigned)(mb >> 1) * 4096u + (unsigned)(2 * (w & 1) + b) * 1024u, v);
        }
      };
      static_for(std::make_integer_sequence<int, 56>{}, [&](auto NC) __attribute__((always_inline)) NOPK {
        constexpr int n = decltype(NC)::value, T = hv_t(n), mb = hv_m(n), sl = n % 7;
        if constexpr (n == 9) {
          wait_vm<0>();
          cnt_arrive(lds, C_W);
          cnt_wait(lds, C_W, 4);
        }
        if constexpr (n < 32 && (n & 3) == 0) {
          hs0 = lds_tr(lp(hq0) + (n / 4) * 8192, lp(hq0) + (n / 4) * 8192 + 1024);
          hs1 = lds_tr(lp(hq1) + (n / 4) * 8192, lp(hq1) + (n / 4) * 8192 + 1024);
        }
        if constexpr (n == 32) {
          if (i16 == 0) {
            *(LAS f32x4*)(lds + L_SH + (w * 16 + 4 * q) * 4) = sh0;
            if (w < 3) *(LAS f32x4*)(lds + L_SH + (m1 * 16 + 4 * q) * 4) = sh1;
          }
          cnt_arrive(lds, C_SH);
          cnt_wait(lds, C_SH, 4);
          const f32x2 s0 = *(LAS f32x2*)(lds + L_STAT + (w * 32 + i16) * 8), s1 = *(LAS f32x2*)(lds + L_STAT + (w * 32 + 16 + i16) * 8);
          nm[0] = -s0[0]; nm[1] = -s1[0]; r2[0] = s0[1] * -1.44269504088896f; r2[1] = s1[1] * -1.44269504088896f;
        }
        ah[mb][0] = mfma16(wd[sl], xf[0][T], ah[mb][0]);
        ah[mb][1] = mfma16(wd[sl], xf[1][T], ah[mb][1]);
        if constexpr (n < 32 && (n & 3) == 3) { sh0 = mfma16(hs0, ones, sh0); sh1 = mfma16(hs1, ones, sh1); }
        if constexpr (n + 7 < 56) {
          constexpr int T2 = hv_t(n + 7), m2 = hv_m(n + 7);
          wd[sl] = lds_tr(lp(pt[m2]) + (T2 & 7) * 8192, lp(pt[m2]) + (T2 & 7) * 8192 + 1024);
        }
        if constexpr (n >= 32 && (n - 32) % 3 == 1) {
          constexpr int u = (n - 32) / 3;
          epi(std::integral_constant<int, (u >> 2) * 2 + (u & 1)>{}, std::integral_constant<int, (u >> 1) & 1>{});
        }
        SB0();
      });
      cnt_arrive(lds, C_B2);
      epi(std::integral_constant<int, 4>{}, std::integral_constant<int, 0>{});
      epi(std::integral_constant<int, 5>{}, std::integral_constant<int, 0>{});
      epi(std::integral_constant<int, 4>{}, std::integral_constant<int, 1>{});
      epi(std::integral_constant<int, 5>{}, std::integral_constant<int, 1>{});
      epi(std::integral_constant<int, 6>{}, std::integral_constant<int, 0>{});
      epi(std::integral_constant<int, 6>{}, std::integral_constant<int, 1>{});
      v4u v = {pkd[6][0][0], pkd[6][0][1], pkd[6][1][0], pkd[6][1][1]};
      st16(rs, (unsigned)WS_Z + zf + 12288u + (unsigned)(w & 1) * 1024u, v);
    }
    wait_vm<0>();
    if (cnt_arrive(lds, C_FS) == 3 && l < 16)
      __hip_atomic_store((GAS u64*)(p.ws + WS_FLAG) + (256 + j * 16 + s) * 16 + l, epoch, RLX_AGENT);
    cnt_wait(lds, C_B2, 4);
    cnt_wait(lds, C_HS, 4);
#pragma unroll
    for (int n = 0; n < 14; n++) WP(0, n);
    EPI_LOADS()
    poll_flags(p.ws + WS_FLAG + (size_t)(256 + j * 16 + 4 * w) * 128, l, epoch);
    asm volatile("" : "+v"(xe[0]), "+v"(xe[1]), "+v"(dk));
    f32x4 acc[4][4];
#pragma unroll
    for (int a = 0; a < 4; a++)
#pragma unroll
      for (int b = 0; b < 4; b++) acc[a][b] = f32x4{0, 0, 0, 0};
    v4u af[4][4];
    const char* afb = ab + (size_t)(4 * w) * 14336;
    unsigned ftr[4];
#pragma unroll
    for (int nb = 0; nb < 4; nb++) {
      const int row = 4 * q + ta;
      ftr[nb] = opaque(ring + (unsigned)(row * 128 + ((nb ^ ((row >> 1) & 3)) * 32) + tp * 8));
    }
#define ALD(U, i) gld16_sc1<(i) * 1024>(af[(U) & 3][i], avoff, afb + (size_t)((((U) >> 2) + rot2) & 3) * 14336 + ((U) & 3) * 4096)
#define ALDS(U, V, t)                             \
  {                                                                                                 \
    if ((V) < 16) {                                                                                 \
      if (0 * usz(U) / usz(V) == (t)) ALD(V, 0);                                                    \
      if (1 * usz(U) / usz(V) == (t)) ALD(V, 1);                                                    \
      if (usz(V) == 4 && 2 * usz(U) / usz(V) == (t)) ALD(V, 2);                                     \
      if (usz(V) == 4 && 3 * usz(U) / usz(V) == (t)) ALD(V, 3);                                     \
    }                                                                                               \
  }
#define WRD(U)                                           \
  {                                                                                                 \
    _Pragma("unroll") for (int nb = 0; nb < 4; nb++) {                                              \
      LAS char* b_ = lp(ftr[nb]) + (((U) >> 2) & 1) * 14336 + ((U) & 3) * 4096;                     \
      if (((U) & 3) == 3) { v2u t_ = lds_tr1(b_); wf[(U) & 1][nb].x = t_.x; wf[(U) & 1][nb].y = t_.y; } \
      else wf[(U) & 1][nb] = lds_tr(b_, b_ + 2048);                                                 \
    }                                                                                               \
  }
#define FSTEP(U, t)                                                                                 \
  {                                                                                                 \
    if (((U) & 3) < 3) {                                                                            \
      _Pragma("unroll") for (int nb = 0; nb < 4; nb++)                                              \
          acc[nb][t] = mfma16(wf[(U) & 1][nb], af[(U) & 3][t], acc[nb][t]);                         \
    } else {                                                                                        \
      _Pragma("unroll") for (int nb = 0; nb < 4; nb++) {                                            \
        const v2u wh_ = {wf[(U) & 1][nb].x, wf[(U) & 1][nb].y};                                     \
        acc[nb][(2 * (t)) & 3] = mfma16h(wh_, v2u{af[3][t].x, af[3][t].y}, acc[nb][(2 * (t)) & 3]); \
        acc[nb][(2 * (t) + 1) & 3] = mfma16h(wh_, v2u{af[3][t].z, af[3][t].w}, acc[nb][(2 * (t) + 1) & 3]); \
      }                                                                                             \
    }                                                                                               \
    SB0();                                                                                          \
    ALDS(U, (U) + FD, t)                                                                            \
    if ((U) < 12) WP(((U) >> 2) + 1, 4 * ((U) & 3) + (t));                                          \
    if ((t) == usz(U) - 2 && (U) < 15) {                                                            \
      if ((U) + 1 >= 4) wait_vm<f_cnt(false, (U) + 1, U, (t) + 1)>();                               \
      WRD((U) + 1)                                                                                  \
    }                                                                                               \
    SB0();                                                                                          \
  }
#define FU(U)                                                                                       \
  {                                                                                                 \
    if (((U) & 3) < 3) {                                                                            \
      wait_vm4<f_cnt(true, U, U, 0)>(af[(U) & 3][0], af[(U) & 3][1], af[(U) & 3][2], af[(U) & 3][3]); \
      FSTEP(U, 0) FSTEP(U, 1) FSTEP(U, 2) FSTEP(U, 3)                                               \
    } else {                                                                                        \
      asm volatile("s_waitcnt vmcnt(%2)" : "+v"(af[3][0]), "+v"(af[3][1]) : "i"(f_cnt(true, U, U, 0)) : "memory"); \
      FSTEP(U, 0) FSTEP(U, 1)                                                                       \
    }                                                                                               \
  }
    v4u wf[2][4];
    ALD(0, 0); ALD(0, 1); ALD(0, 2); ALD(0, 3);
    ALD(1, 0); ALD(1, 1); ALD(1, 2); ALD(1, 3);
    ALD(2, 0); ALD(2, 1); ALD(2, 2); ALD(2, 3);
    static_assert(FD == 3, "window");
    WRD(0)
    FU(0) FU(1) FU(2) FU(3) FU(4) FU(5) FU(6) FU(7) FU(8) FU(9) FU(10) FU(11) FU(12) FU(13) FU(14) FU(15)
#undef FU
#undef FSTEP
#undef WRD
#undef ALD
#undef ALDS
#undef WP
    {
      LAS char* dst = lp(ring);
#pragma unroll
      for (int tb = 0; tb < 4; tb++) {
        const int tk = 32 * (tb >> 1) + 8 * (i16 >> 2) + 4 * (tb & 1) + (i16 & 3);
#pragma unroll
        for (int nb = 0; nb < 4; nb++)
          *(LAS f32x4*)(dst + tk * 256 + (((4 * nb + q) ^ i16) & 15) * 16) = acc[nb][tb];
      }
    }
  }
  {
    bar();
    if (j == 15 && s == 0 && tid == 0)
      __hip_atomic_store((GAS u64*)(p.ws + WS_EPOCH), epoch, RLX_AGENT);
    const __amdgpu_buffer_rsrc_t ro = __builtin_amdgcn_make_buffer_rsrc((void*)p.out, 0, 2048 * 512 * 4, 0x00020000);
    const float dkf[4] = {bf2f(dk.x & 0xffff), bf2f(dk.x >> 16), bf2f(dk.y & 0xffff), bf2f(dk.y >> 16)};
#pragma unroll
    for (int n = 0; n < 2; n++) {
      const int tk = et + 32 * n;
      const int key = (tk & 3) | (((tk >> 3) & 3) << 2);
      const int o = tk * 256 + ((ec ^ key) & 15) * 16;
      LAS char* fb = lds + L_RING + o;
      const f32x4 f = (*(LAS f32x4*)(fb) + *(LAS f32x4*)(fb + RING)) + (*(LAS f32x4*)(fb + 2 * RING) + *(LAS f32x4*)(fb + 3 * RING));
      const f32x4 y = *(LAS f32x4*)(lds + L_YB0 + o) + *(LAS f32x4*)(lds + L_YB1 + o);
      const int tkp = 64 * (hp2 & 1) + tk;
      const f32x2 se = *(LAS f32x2*)(lds + L_STAT + ((tkp >> 5) * 32 + ((tkp >> 2) & 1) * 16 + key) * 8);
      const float xv[4] = {bf2f(xe[n].x & 0xffff), bf2f(xe[n].x >> 16), bf2f(xe[n].y & 0xffff), bf2f(xe[n].y >> 16)};
      f32x4 o4;
#pragma unroll
      for (int e = 0; e < 4; e++) {
        const float xn = (xv[e] - se[0]) * se[1];
        const float sv = fmaf(xn, dkf[e], y[e]);
        const float fe = f[e] * se[1];
        const float g = fe * __builtin_amdgcn_rcpf(1.f + __builtin_amdgcn_exp2f(fe * -1.44269504088896f));
        o4[e] = fmaf(sv, g, xv[e]);
      }
      __builtin_amdgcn_raw_buffer_store_b128(__builtin_bit_cast(v4u, o4), ro, (int)(((64 * hp2 + tk) * 512 + 64 * ct + 4 * ec) * 4), 0, 16);
    }
  }
}

extern "C" void kernel_launch(void* const* d_in, const int* in_sizes, int n_in, void* d_out, int out_size, void* d_ws,
                              size_t ws_size, hipStream_t stream) {
  Params p;
  p.x = (const char*)d_in[0]; p.lre = (const char*)d_in[1]; p.lim = (const char*)d_in[2];
  p.bre = (const char*)d_in[3]; p.bim = (const char*)d_in[4]; p.cre = (const char*)d_in[5]; p.cim = (const char*)d_in[6];
  p.logdt = (const char*)d_in[7]; p.dskip = (const char*)d_in[8]; p.w1 = (const char*)d_in[10]; p.w2 = (const char*)d_in[11];
  p.out = (float*)d_out; p.ws = (char*)d_ws;
  (void)hipFuncSetAttribute((const void*)k_fused, hipFuncAttributeMaxDynamicSharedMemorySize, LDS_BYTES);
  k_fused<<<256, 512, LDS_BYTES, stream>>>(p);
}
```

```cpp
#include <hip/hip_runtime.h>
#include <hip/hip_bf16.h>
#include <type_traits>
#include <utility>

typedef unsigned v4u __attribute__((ext_vector_type(4)));
typedef unsigned v2u __attribute__((ext_vector_type(2)));
typedef float f32x4 __attribute__((ext_vector_type(4)));
typedef float f32x2 __attribute__((ext_vector_type(2)));
typedef short bf16x8 __attribute__((ext_vector_type(8)));
typedef short s16x4 __attribute__((ext_vector_type(4)));
typedef __bf16 bf16x2_t __attribute__((ext_vector_type(2)));
typedef unsigned long long u64;
#define LAS __attribute__((address_space(3)))
#define GAS __attribute__((address_space(1)))
#if defined(__HIP_DEVICE_COMPILE__)
#define NOPK __attribute__((target("no-packed-fp32-ops")))
#else
#define NOPK
#endif
#define DI __device__ __forceinline__ NOPK
#define RLX_AGENT __ATOMIC_RELAXED, __HIP_MEMORY_SCOPE_AGENT
#define RLX_WG __ATOMIC_RELAXED, __HIP_MEMORY_SCOPE_WORKGROUP

static constexpr unsigned HP_BYTES = 16 * 14336 + 32 * 4096;
static constexpr unsigned Y_OFF = 16 * 14336;
static constexpr size_t WS_Z = 0;
static constexpr size_t WS_Z_BYTES = (size_t)32 * HP_BYTES;
static constexpr size_t WS_GRAN = WS_Z + WS_Z_BYTES;
static constexpr size_t WS_SG = WS_GRAN + 16 * 16 * 512;
static constexpr size_t WS_FLAG = WS_SG + 2048 * 16;
static constexpr size_t WS_EPOCH = WS_FLAG + 2 * 16 * 16 * 128;
static constexpr size_t WS_END = WS_EPOCH + 128;

static constexpr int LDS_BYTES = 160 * 1024;
static constexpr int STG = 24576;
static constexpr int L_W1A = 96 * 1024;
static constexpr int L_CST = 0;
static constexpr int L_YB1 = L_CST;
static constexpr int L_HT = 16 * 1024;
static constexpr int L_E = 24 * 1024;
static constexpr int L_KEEP = 28 * 1024;
static constexpr int L_YB0 = 32 * 1024;
static constexpr int L_RING = 48 * 1024;
static constexpr int L_STAT = L_KEEP;
static constexpr int L_SU = L_KEEP + 1024;
static constexpr int L_SH = L_KEEP + 1280;
static constexpr int L_CNT = L_KEEP + 1792;
static constexpr int RING = 28672;
static constexpr int L_HIN = L_KEEP + 2048;
enum { C_W = 9, C_SH = 10, C_B2 = 11, C_E = 0, C_CX = 1, C_HIN = 2, C_HS = 4, C_FS = 5, C_Y1 = 7 };
#define ROT 1
#define CPACE 2
#define CODEPF 32
#define APRIO 2
#define MWARM 1
#define ROT2 1

DI bool wall(bool x) { return __builtin_amdgcn_ballot_w64(!x) == 0ull; }
DI f32x4 mfma16(v4u a, v4u b, f32x4 c) {
  return __builtin_amdgcn_mfma_f32_16x16x32_bf16(__builtin_bit_cast(bf16x8, a), __builtin_bit_cast(bf16x8, b), c, 0, 0, 0);
}
DI unsigned pk_bf16(float lo, float hi) {
  f32x2 v = {lo, hi};
  return __builtin_bit_cast(unsigned, __builtin_convertvector(v, bf16x2_t));
}
DI float bf2f(unsigned u) { return __builtin_bit_cast(float, u << 16); }
DI unsigned opaque(unsigned v) { asm volatile("" : "+v"(v)); return v; }
DI LAS char* lp(unsigned a) { return (LAS char*)(size_t)a; }

template <int IMM> DI void gld16(v4u& d, unsigned voff, const void* sb) {
  asm volatile("global_load_dwordx4 %0, %1, %2 offset:%3" : "=v"(d) : "v"(voff), "s"(sb), "i"(IMM) : "memory");
}
template <int IMM> DI void gld16_sc1(v4u& d, unsigned voff, const void* sb) {
  asm volatile("global_load_dwordx4 %0, %1, %2 offset:%3 sc1" : "=v"(d) : "v"(voff), "s"(sb), "i"(IMM) : "memory");
}
template <int IMM> DI void gld_u16(unsigned& d, unsigned voff, const void* sb) {
  asm volatile("global_load_ushort %0, %1, %2 offset:%3" : "=v"(d) : "v"(voff), "s"(sb), "i"(IMM) : "memory");
}
DI void gld8_sc1(v2u& d, unsigned voff, const void* sb) {
  asm volatile("global_load_dwordx2 %0, %1, %2 sc1" : "=v"(d) : "v"(voff), "s"(sb) : "memory");
}
template <int N> DI void wait_vm() { asm volatile("s_waitcnt vmcnt(%0)" :: "i"(N) : "memory"); }
template <int N> DI void wait_vm4(v4u& a, v4u& b, v4u& c, v4u& d) {
  asm volatile("s_waitcnt vmcnt(%4)" : "+v"(a), "+v"(b), "+v"(c), "+v"(d) : "i"(N) : "memory");
}
DI void wait_lgkm() { asm volatile("s_waitcnt lgkmcnt(0)" ::: "memory"); }
DI void bar() { wait_lgkm(); __builtin_amdgcn_s_barrier(); asm volatile("" ::: "memory"); }

DI v4u lds_rd16(LAS char* p) { return *(LAS v4u*)p; }
DI v4u lds_tr(LAS char* p0, LAS char* p1) {
  s16x4 a = __builtin_amdgcn_ds_read_tr16_b64_v4i16((LAS s16x4*)p0);
  s16x4 b = __builtin_amdgcn_ds_read_tr16_b64_v4i16((LAS s16x4*)p1);
  bf16x8 f = __builtin_shufflevector(a, b, 0, 1, 2, 3, 4, 5, 6, 7);
  return __builtin_bit_cast(v4u, f);
}
DI unsigned cnt_arrive(LAS char* lds, int c) {
  wait_lgkm();
  unsigned old = 0;
  if ((threadIdx.x & 63) == 0) old = __hip_atomic_fetch_add((LAS unsigned*)(lds + L_CNT) + c, 1u, RLX_WG);
  return __builtin_amdgcn_readfirstlane(old);
}
DI void cnt_wait(LAS char* lds, int c, unsigned target) {
  unsigned spins = 0;
  while ((unsigned)__builtin_amdgcn_readfirstlane(__hip_atomic_load((LAS unsigned*)(lds + L_CNT) + c, RLX_WG)) < target) {
    if (++spins > (1u << 24)) break;
    __builtin_amdgcn_s_sleep(1);
  }
  asm volatile("" ::: "memory");
}
struct cpx { float r, i; };
DI cpx cmul(cpx a, cpx b) { return {a.r * b.r - a.i * b.i, a.r * b.i + a.i * b.r}; }
DI cpx cfma(cpx a, cpx h, cpx u) {
  return {fmaf(a.r, h.r, fmaf(-a.i, h.i, u.r)), fmaf(a.r, h.i, fmaf(a.i, h.r, u.i))};
}
DI cpx csel(bool c, cpx a, cpx b) { return {c ? a.r : b.r, c ? a.i : b.i}; }

struct Params {
  const char *x, *lre, *lim, *bre, *bim, *cre, *cim, *logdt, *dskip, *w1, *w2;
  float* out;
  char* ws;
};

DI void st16(__amdgpu_buffer_rsrc_t rs, unsigned off, v4u v) { __builtin_amdgcn_raw_buffer_store_b128(v, rs, (int)off, 0, 16); }

DI void poll_flags(const char* fl, int l, u64 epoch) {
  GAS u64* fp = (GAS u64*)fl + (l & 3) * 16;
  unsigned spins = 0;
  while (!wall(__hip_atomic_load(fp, RLX_AGENT) == epoch)) {
    if (++spins > (1u << 20)) break;
    __builtin_amdgcn_s_sleep(4);
  }
  asm volatile("" ::: "memory");
}

DI f32x4 mfma16h(v2u a, v2u b, f32x4 c) {
  return __builtin_amdgcn_mfma_f32_16x16x16bf16_1k(__builtin_bit_cast(s16x4, a), __builtin_bit_cast(s16x4, b), c, 0, 0, 0);
}
DI v2u lds_tr1(LAS char* p0) { return __builtin_bit_cast(v2u, __builtin_amdgcn_ds_read_tr16_b64_v4i16((LAS s16x4*)p0)); }
template <int CTRL> DI float dpp_add(float v) {
  return v + __builtin_bit_cast(float, __builtin_amdgcn_update_dpp(0, __builtin_bit_cast(int, v), CTRL, 0xf, 0xf, true));
}
DI float wave_sum(float v) {
  v = dpp_add<0x111>(v); v = dpp_add<0x112>(v); v = dpp_add<0x114>(v); v = dpp_add<0x118>(v);
  v = v + __builtin_bit_cast(float, __builtin_amdgcn_update_dpp(0, __builtin_bit_cast(int, v), 0x142, 0xa, 0xf, false));
  v = v + __builtin_bit_cast(float, __builtin_amdgcn_update_dpp(0, __builtin_bit_cast(int, v), 0x143, 0xc, 0xf, false));
  return __builtin_bit_cast(float, __builtin_amdgcn_readlane(__builtin_bit_cast(int, v), 63));
}
DI u64 mk_gran(u64 epoch, float v) {
  unsigned vb = __builtin_bit_cast(unsigned, v);
  vb = (vb + 0x80u) >> 8;
  return (epoch << 24) | (u64)(vb & 0xffffffu);
}
DI void dma1(unsigned lds, const void* sb, unsigned v) {
  asm volatile("s_mov_b32 m0, %0\n\ts_nop 0\n\tglobal_load_lds_dwordx4 %2, %1" :: "s"(lds), "s"(sb), "v"(v) : "memory");
}
#define FD 3
constexpr int usz(int u) { return (u & 3) == 3 ? 2 : 4; }
constexpr int f_cnt(bool kindA, int u, int at, int st) {
  int n = 0; bool seen = false;
  for (int a = 0; a < FD; a++) { if (seen) n += usz(a); if (kindA && a == u) seen = true; }
  for (int v = 0; v <= at; v++) {
    const int steps = v < at ? usz(v) : st;
    for (int t = 0; t < steps; t++) {
      if (v + FD < 16) {
        const int np = usz(v + FD), ns = usz(v);
        for (int i = 0; i < np; i++)
          if (i * ns / np == t) { if (seen) n++; if (kindA && v + FD == u && i == np - 1) seen = true; }
      }
      if (v + 4 < 16) { if (seen) n++; if (!kindA && v + 4 == u && t == usz(v) - 1) seen = true; }
    }
  }
  return n;
}
constexpr int hv_t(int n) { return n < 32 ? 8 + n / 4 : 8 + (n - 32) / 3; }
constexpr int hv_m(int n) { return n < 32 ? n % 4 : 4 + (n - 32) % 3; }
template <int... I, class F> DI void static_for(std::integer_sequence<int, I...>, F f) { (f(std::integral_constant<int, I>{}), ...); }
#define SB0() __builtin_amdgcn_sched_barrier(0)

__global__ __launch_bounds__(512, 2) NOPK void k_fused(Params p) {
  extern __shared__ __attribute__((aligned(16))) char smem[];
  LAS char* lds = (LAS char*)smem;
  const int tid = threadIdx.x, l = tid & 63;
  const int w8 = __builtin_amdgcn_readfirstlane(tid >> 6);
  const int role = w8 >> 2, w = w8 & 3;
  const int bid = blockIdx.x, xcd = bid & 7, r = bid >> 3;
  const int j = 4 * (xcd >> 1) + (r >> 3);
  const int s = 8 * (xcd & 1) + (r & 7);
  const int hp2 = 8 * (xcd >> 1) + (r >> 2);
  const int ct = 4 * (xcd & 1) + (r & 3);
  const int odd = s & 1;
  const int i16 = l & 15, q = l >> 4;
  const int ta = i16 >> 2, tp = i16 & 3;
  const unsigned ldsb = (unsigned)(size_t)lds;
  const __amdgpu_buffer_rsrc_t rs = __builtin_amdgcn_make_buffer_rsrc((void*)p.ws, 0, (int)WS_END, 0x00020000);
  const v4u ones = {0x3F803F80u, 0x3F803F80u, 0x3F803F80u, 0x3F803F80u};

  const int rot = ROT ? ((2 * (r >> 3) + (r & 7)) & 7) : 0;
  u64 ep0;
  asm volatile("s_load_dwordx2 %0, %1, 0x0" : "=s"(ep0) : "s"(p.ws + WS_EPOCH) : "memory");
  unsigned plr[2], pli[2], pdt[2];
  v4u xr[2];
  unsigned cdum = 0;
  if (role == 0) {
    const char* rb = p.x + (size_t)(128 * j + 8 * s + 2 * w) * 1024;
    gld16<0>(xr[0], (unsigned)l * 16, rb); gld16<1024>(xr[1], (unsigned)l * 16, rb);
    const unsigned co = (unsigned)(32 * s + i16) * 2;
    gld_u16<0>(plr[0], co, p.lre); gld_u16<32>(plr[1], co, p.lre);
    gld_u16<0>(pli[0], co, p.lim); gld_u16<32>(pli[1], co, p.lim);
    gld_u16<0>(pdt[0], co, p.logdt); gld_u16<32>(pdt[1], co, p.logdt);
    {
      u64 pc;
      asm volatile("s_getpc_b64 %0" : "=s"(pc));
      const int ci = r * 4 + w;
      const char* cb = (const char*)(pc & ~255ull) + (ci < CODEPF ? ci : 0) * 1024;
      asm volatile("global_load_dword %0, %1, %2" : "=v"(cdum) : "v"((unsigned)l * 16), "s"(cb) : "memory");
    }
#pragma unroll
    for (int k = 0; k < 3; k++) {
      int row = bid * 11 + w + 4 * k;
      if (w + 4 * k >= 11) row = bid * 11;
      const char* src = row < 1792 ? p.w2 + (size_t)row * 1024 : (row < 2304 ? p.cre + (size_t)(row - 1792) * 1024 : p.cim + (size_t)(row - 2304) * 1024);
      asm volatile("global_load_dword %0, %1, %2" : "+v"(cdum) : "v"((unsigned)l * 16), "s"(src) : "memory");
    }
  }
  unsigned vx0, vx1, vu, vwA, vwB;
  {
    const int r8 = l >> 3, c8 = l & 7;
    const int tok0 = 8 * (r8 >> 2) + (r8 & 3);
    vx0 = (unsigned)(tok0 * 1024 + ((c8 ^ (r8 >> 1)) & 7) * 16);
    vx1 = (unsigned)((tok0 + 16) * 1024 + ((c8 ^ (4 + (r8 >> 1))) & 7) * 16);
    const int ch = 8 * w + r8, iu = 8 * (w & 1) + r8;
    vu = (unsigned)(ch * 1024 + ((c8 ^ (iu >> 1)) & 7) * 16);
    const int winoff = 448 * (s >> 1) + (odd ? 192 : 0);
    vwA = (unsigned)((16 * w + q) * 3584 + winoff + ((((i16 >> 1) ^ q) << 1) | (i16 & 1)) * 16);
    vwB = (unsigned)((16 * w + 8 + q) * 3584 + winoff + ((((i16 >> 1) ^ (q | 4)) << 1) | (i16 & 1)) * 16);
  }
  const char* xb = p.x + (size_t)(128 * j + 32 * (w >> 1) + 4 * (w & 1)) * 1024;
  const char* ub0 = p.bre + (size_t)(32 * s) * 1024;
  const char* ub1 = p.bim + (size_t)(32 * s) * 1024;
#define PIECE(N, I)                                                                                  \
  {                                                                                                  \
    constexpr int h_ = (N) / 3, n_ = (N) % 3;                                                        \
    const int kq_ = ((I) + rot) & 7;                                                                 \
    const unsigned d_ = ldsb + w * 3072 + ((I) & 3) * STG + h_ * 12288 + n_ * 1024;                  \
    if (n_ == 0) dma1(d_, xb + h_ * 65536 + kq_ * 128, vx0);                                         \
    if (n_ == 1) dma1(d_, xb + h_ * 65536 + kq_ * 128, vx1);                                         \
    if (n_ == 2) dma1(d_, (h_ ? ub1 : ub0) + kq_ * 128, vu);                                         \
  }
#define PALL(I) PIECE(0, I) PIECE(1, I) PIECE(2, I) PIECE(3, I) PIECE(4, I) PIECE(5, I)
#define W1P(C, n) dma1(ldsb + L_W1A + ((C) & 3) * 16384 + (16 * w + 4 * (n)) * 256,          \
                       p.w1 + (size_t)((((C) + rot) & 7) * 64 + ((n) & 1) * 4) * 3584, ((n) & 2) ? vwB : vwA)
  if (role == 1) {
    PALL(0)
    __builtin_amdgcn_s_barrier();
    PALL(1)
  } else {
    __builtin_amdgcn_s_barrier();
  }
  asm volatile("s_waitcnt lgkmcnt(0)" : "+s"(ep0) :: "memory");
  const u64 epoch = ep0 + 1ull;
  const unsigned tlo = (unsigned)(epoch & 0xffu), thi = (unsigned)(epoch >> 8);

  const unsigned sw0 = (unsigned)((i16 & 7) * 128 + (((0 + q) ^ (i16 >> 1)) & 7) * 16);
  const unsigned sw1 = (unsigned)((i16 & 7) * 128 + (((4 + q) ^ (i16 >> 1)) & 7) * 16);
  const unsigned xo = (unsigned)(2 * w * 3072 + (i16 >> 3) * 1024);
  const unsigned tro = (unsigned)((8 * q + ta) * 256 + 8 * tp);
  const int trkey = ta | ((q & 1) << 2);
  const unsigned zb = (unsigned)(2 * j + (w >> 1)) * HP_BYTES + (unsigned)l * 16u;
  const char* ab = p.ws + WS_Z + (size_t)hp2 * HP_BYTES;
  const unsigned avoff = (unsigned)l * 16;
  const int rot2 = ROT2 == 1 ? (hp2 & 3) : ROT2 == 2 ? (ct & 3) : 0;

  const int ec = tid & 15, et = tid >> 4;
  v2u xe[2], dk;
#define EPI_LOADS()                                                                                  \
  {                                                                                                  \
    xe[0] = *(const GAS v2u*)(p.x + (size_t)(64 * hp2 + et) * 1024 + (64 * ct + 4 * ec) * 2);        \
    xe[1] = *(const GAS v2u*)(p.x + (size_t)(64 * hp2 + et + 32) * 1024 + (64 * ct + 4 * ec) * 2);   \
    dk = *(const GAS v2u*)(p.dskip + (64 * ct + 4 * ec) * 2);                                        \
  }
  if (role == 0) {
    if (APRIO) __builtin_amdgcn_s_setprio(APRIO);
    asm volatile("s_waitcnt vmcnt(%8)" : "+v"(plr[0]), "+v"(plr[1]), "+v"(pli[0]), "+v"(pli[1]), "+v"(pdt[0]), "+v"(pdt[1]), "+v"(xr[0]), "+v"(xr[1]) : "i"((CODEPF ? 1 : 0) + (MWARM ? 3 : 0)) : "memory");
#pragma unroll
    for (int n = 0; n < 2; n++) {
      float v[8];
#pragma unroll
      for (int e = 0; e < 4; e++) { v[2 * e] = bf2f(xr[n][e] & 0xffffu); v[2 * e + 1] = __builtin_bit_cast(float, xr[n][e] & 0xffff0000u); }
      const float mean_f = wave_sum(((v[0] + v[1]) + (v[2] + v[3])) + ((v[4] + v[5]) + (v[6] + v[7]))) * (1.f / 512.f);
      float sq = 0.f;
#pragma unroll
      for (int e = 0; e < 8; e++) { const float d = v[e] - mean_f; sq = fmaf(d, d, sq); }
      const float mean = bf2f(pk_bf16(mean_f, 0.f) & 0xffffu);
      const float var = bf2f(pk_bf16(wave_sum(sq) * (1.f / 512.f), 0.f) & 0xffffu);
      const float ve = bf2f(pk_bf16(var + 1e-5f, 0.f) & 0xffffu);
      const float rstd = __builtin_amdgcn_rcpf(bf2f(pk_bf16(__builtin_amdgcn_sqrtf(ve), 0.f) & 0xffffu));
      if (l == 0) *(LAS f32x2*)(lds + LDS_BYTES - 64 + (2 * w + n) * 8) = f32x2{mean, rstd};
    }
    wait_lgkm();
    cpx a1[2], cc[2], a8[2], pw[2], a128[2];
#pragma unroll
    for (int nb = 0; nb < 2; nb++) {
      const float lr = bf2f(plr[nb]), li = bf2f(pli[nb]);
      const float dtf = expf(bf2f(pdt[nb]));
      const float dt = bf2f(pk_bf16(dtf, 0.f) & 0xffffu);
      const float e = expf(dt * lr);
      float sn, cs;
      sincosf(dt * li, &sn, &cs);
      cpx a = {e * cs, e * sn};
      a1[nb] = a;
      const float nr = a.r - 1.f, ni = a.i, den = lr * lr + li * li;
      cc[nb] = {2.f * (nr * lr + ni * li) / den, 2.f * (ni * lr - nr * li) / den};
      cpx t = cmul(a, a); t = cmul(t, t); t = cmul(t, t);
      a8[nb] = t;
      cpx a16 = cmul(t, t), a32 = cmul(a16, a16), a64 = cmul(a32, a32);
      a128[nb] = cmul(a64, a64);
      cpx one = {1.f, 0.f};
      cpx pq = cmul(csel(q & 1, t, one), csel(q & 2, a16, one));
      cpx pwv = cmul(csel(w & 1, a32, one), csel(w & 2, a64, one));
      pw[nb] = cmul(pq, pwv);
    }
#pragma unroll
    for (int nb = 0; nb < 2; nb++)
      asm volatile("" : "+v"(a1[nb].r), "+v"(a1[nb].i), "+v"(cc[nb].r), "+v"(cc[nb].i), "+v"(a8[nb].r), "+v"(a8[nb].i), "+v"(pw[nb].r),
                   "+v"(pw[nb].i), "+v"(a128[nb].r), "+v"(a128[nb].i));
    v4u cf[8][4];
    const unsigned cvoff = (unsigned)((64 * ct + (l >> 2)) * 1024 + (l & 3) * 16);
#define CLOAD(K0)                                                                         \
  _Pragma("unroll") for (int k = K0; k < K0 + 4; k++) {                                   \
    const int kr = (k + 2 * rot2) & 7;                                                    \
    const int sidx = 4 * w + 2 * (kr >> 2) + (kr & 1);                                    \
    const char* cb = (((kr >> 1) & 1) ? p.cim : p.cre) + sidx * 64;                       \
    _Pragma("unroll") for (int n = 0; n < 4; n++) gld16<0>(cf[k][n], cvoff, cb + n * 16384); \
    if (CPACE) __builtin_amdgcn_s_sleep(CPACE);                                           \
  }
#define CWAIT(K0, N)                                                                      \
  asm volatile("s_waitcnt vmcnt(%16)" : "+v"(cf[K0][0]), "+v"(cf[K0][1]), "+v"(cf[K0][2]), "+v"(cf[K0][3]), "+v"(cf[K0 + 1][0]),   \
               "+v"(cf[K0 + 1][1]), "+v"(cf[K0 + 1][2]), "+v"(cf[K0 + 1][3]), "+v"(cf[K0 + 2][0]), "+v"(cf[K0 + 2][1]),          \
               "+v"(cf[K0 + 2][2]), "+v"(cf[K0 + 2][3]), "+v"(cf[K0 + 3][0]), "+v"(cf[K0 + 3][1]), "+v"(cf[K0 + 3][2]),          \
               "+v"(cf[K0 + 3][3]) : "i"(N) : "memory");
#define CXPOSE(K0)                                                                        \
  _Pragma("unroll") for (int k = K0; k < K0 + 4; k++) {                                   \
    _Pragma("unroll") for (int n = 0; n < 4; n++) *(LAS v4u*)(lp(cwa) + n * 1024) = cf[k][n]; \
    _Pragma("unroll") for (int n = 0; n < 4; n++) cf[k][n] = lds_rd16(lp(cra) + n * 1024);  \
  }
    v2u sg;
    const unsigned sgoff = (unsigned)l * 8;
    const char* sgb = p.ws + WS_SG + (size_t)(128 * j + 32 * w) * 16;
    f32x4 au[2][2][2], asu;
#pragma unroll
    for (int b = 0; b < 2; b++)
#pragma unroll
      for (int nb = 0; nb < 2; nb++) { au[b][nb][0] = f32x4{0, 0, 0, 0}; au[b][nb][1] = f32x4{0, 0, 0, 0}; }
    asu = f32x4{0, 0, 0, 0};
    {
      const unsigned uo = (unsigned)((i16 >> 3) * 3072 + 2048);
      const int gs = (w & 1) * 2 + (w >> 1);
      unsigned cur = 0;
      v4u xd[2][2], wd[2][4], sd[2];
#pragma unroll
      for (int t = 0; t < 2; t++) xd[t][0] = xd[t][1] = wd[t][0] = wd[t][1] = wd[t][2] = wd[t][3] = sd[t] = v4u{0, 0, 0, 0};
#define A_STEP(RD, T, PX, PU, PS)                                                                     \
  {                                                                                                   \
    _Pragma("unroll") for (int g = 0; g < 4; g++) {                                                   \
      au[0][g & 1][g >> 1] = mfma16(xd[T][0], wd[T][g], au[0][g & 1][g >> 1]);                        \
      au[1][g & 1][g >> 1] = mfma16(xd[T][1], wd[T][g], au[1][g & 1][g >> 1]);                        \
      if (RD) wd[T][g] = lds_rd16(PU + g * 6144);                                                     \
      SB0();                                                                                          \
    }                                                                                                 \
    asu = mfma16(ones, sd[T], asu);                                                                   \
    if (RD) { xd[T][0] = lds_rd16(PX); xd[T][1] = lds_rd16(PX + 3072); sd[T] = lds_rd16(PS); }        \
    SB0();                                                                                            \
  }
#pragma unroll 1
      for (int it = 0; it < 8; it++) {
        const unsigned b0 = ldsb + cur;
        LAS char* px0 = lp(opaque(b0 + xo + sw0)); LAS char* px1 = lp(opaque(b0 + xo + sw1));
        LAS char* pu0 = lp(opaque(b0 + uo + sw0)); LAS char* pu1 = lp(opaque(b0 + uo + sw1));
        LAS char* ps0 = lp(opaque(b0 + uo + sw0 + gs * 6144)); LAS char* ps1 = lp(opaque(b0 + uo + sw1 + gs * 6144));
        cur = cur >= 3 * STG ? 0 : cur + STG;
        asm volatile("" ::: "memory");
        __builtin_amdgcn_s_barrier();
        asm volatile("" ::: "memory");
        if (it == 0 && w == 0 && l < 16)
          __hip_atomic_store((GAS u64*)(p.ws + WS_SG) + (size_t)(128 * j + 8 * s) * 2 + l,
                             mk_gran(epoch, *(LAS float*)(lds + LDS_BYTES - 64 + l * 4)), RLX_AGENT);
        SB0();
        A_STEP(true, 0, px0, pu0, ps0)
        A_STEP(true, 1, px1, pu1, ps1)
      }
      gld8_sc1(sg, sgoff, sgb);
      A_STEP(false, 0, lds, lds, lds)
      A_STEP(false, 1, lds, lds, lds)
#undef A_STEP
    }
    {
      asm volatile("s_waitcnt vmcnt(0)" : "+v"(sg), "+v"(cdum) :: "memory");
      if (tid < 16) ((LAS unsigned*)(lds + L_CNT))[tid] = 0u;
      unsigned spins = 0;
      while (!wall(sg.y == thi && (sg.x >> 24) == tlo)) {
        if (++spins > (1u << 22)) break;
        __builtin_amdgcn_s_sleep(1);
        gld8_sc1(sg, sgoff, sgb);
        asm volatile("s_waitcnt vmcnt(0)" : "+v"(sg), "+v"(cdum) :: "memory");
      }
      const int tk = l >> 1;
      ((LAS unsigned*)(lds + L_STAT))[(w * 32 + ((tk >> 2) & 1) * 16 + ((tk & 3) | ((tk >> 3) << 2))) * 2 + (l & 1)] = sg.x << 8;
      if (q == 0) ((LAS float*)(lds + L_SU))[w * 16 + i16] = asu[0];
    }
    bar();
    cpx u[2][8];
    {
      f32x2 stt[2][4];
#pragma unroll
      for (int b = 0; b < 2; b++) {
        f32x4 s0 = *(LAS f32x4*)(lds + L_STAT + (w * 32 + b * 16 + 4 * q) * 8);
        f32x4 s1 = *(LAS f32x4*)(lds + L_STAT + (w * 32 + b * 16 + 4 * q) * 8 + 16);
        stt[b][0] = f32x2{s0[0], s0[1]}; stt[b][1] = f32x2{s0[2], s0[3]};
        stt[b][2] = f32x2{s1[0], s1[1]}; stt[b][3] = f32x2{s1[2], s1[3]};
      }
#pragma unroll
      for (int nb = 0; nb < 2; nb++) {
        const float sre = ((LAS float*)(lds + L_SU))[(nb * 2 + 0) * 16 + i16];
        const float sim = ((LAS float*)(lds + L_SU))[(nb * 2 + 1) * 16 + i16];
        cpx h = {0.f, 0.f};
#pragma unroll
        for (int k = 0; k < 8; k++) {
          const int b = k >> 2, rg = k & 3;
          const float m = stt[b][rg][0], rr = stt[b][rg][1];
          u[nb][k].r = fmaf(-m, sre, au[b][nb][0][rg]) * rr;
          u[nb][k].i = fmaf(-m, sim, au[b][nb][1][rg]) * rr;
          h = cfma(a1[nb], h, u[nb][k]);
        }
        *(LAS f32x2*)(lds + L_E + ((nb * 16 + i16) * 16 + 4 * w + q) * 8) = f32x2{h.r, h.i};
      }
    }
    cnt_arrive(lds, C_E);
    cnt_wait(lds, C_E, 4);
    cpx cl[2];
    {
      cpx c[2] = {{0.f, 0.f}, {0.f, 0.f}};
      LAS f32x4* e4a = (LAS f32x4*)(lds + L_E + i16 * 128);
      LAS f32x4* e4b = (LAS f32x4*)(lds + L_E + (16 + i16) * 128);
#pragma unroll 1
      for (int k = 0; k < 2 * w; k++) {
        f32x4 ea = e4a[k], eb = e4b[k];
        c[0] = cfma(a8[0], c[0], cpx{ea[0], ea[1]}); c[1] = cfma(a8[1], c[1], cpx{eb[0], eb[1]});
        c[0] = cfma(a8[0], c[0], cpx{ea[2], ea[3]}); c[1] = cfma(a8[1], c[1], cpx{eb[2], eb[3]});
      }
      cpx tot[2];
#pragma unroll
      for (int nb = 0; nb < 2; nb++) {
        LAS f32x4* e4 = nb ? e4b : e4a;
        f32x4 e0 = e4[2 * w], e1 = e4[2 * w + 1];
        cpx c1 = cfma(a8[nb], c[nb], cpx{e0[0], e0[1]});
        cpx c2 = cfma(a8[nb], c1, cpx{e0[2], e0[3]});
        cpx c3 = cfma(a8[nb], c2, cpx{e1[0], e1[1]});
        tot[nb] = cfma(a8[nb], c3, cpx{e1[2], e1[3]});
        cl[nb] = csel(q & 2, csel(q & 1, c3, c2), csel(q & 1, c1, c[nb]));
      }
      if (w == 3 && j < 15) {
        const cpx t = (q & 2) ? tot[1] : tot[0];
        const float v = (q & 1) ? t.i : t.r;
        const u64 g = mk_gran(epoch, v);
        GAS u64* gp = (GAS u64*)(p.ws + WS_GRAN + (size_t)(j * 16 + s) * 512) + (((q >> 1) * 16 + i16) * 2 + (q & 1));
        __hip_atomic_store(gp, g, RLX_AGENT);
      }
    }
    const unsigned cwa = opaque(ldsb + L_CST + w * 4096 + (l >> 2) * 64 + (((l & 3) ^ ((-(l >> 4)) & 3)) * 16));
    const unsigned cra = opaque(ldsb + L_CST + w * 4096 + i16 * 64 + ((q ^ ((-(i16 >> 2)) & 3)) * 16));
    CLOAD(0)
    CWAIT(0, 0)
    CXPOSE(0)
    if (w < 2) {
      const char* gb = p.ws + WS_GRAN + (size_t)s * 512 + w * 256;
      unsigned spins = 0;
      if (j > 0) {
        GAS u64* pp = (GAS u64*)(gb + (size_t)(l < j ? l : 0) * 8192 + 248);
        while (!wall((__hip_atomic_load(pp, RLX_AGENT) >> 24) == (epoch & 0xffffffffffull))) {
          if (++spins > (1u << 20)) break;
          __builtin_amdgcn_s_sleep(2);
        }
      }
      v4u gr[4];
      for (;;) {
#pragma unroll
        for (int m = 0; m < 4; m++) {
          const int pn = (4 * m + q) < j ? 4 * m + q : 0;
          gld16_sc1<0>(gr[m], (unsigned)(pn * 8192 + i16 * 16), gb);
        }
        wait_vm4<0>(gr[0], gr[1], gr[2], gr[3]);
        bool ok = true;
#pragma unroll
        for (int m = 0; m < 4; m++) {
          const bool need = (4 * m + q) < j;
          const bool good = (gr[m].y == thi) && ((gr[m].x >> 24) == tlo) && (gr[m].w == thi) && ((gr[m].z >> 24) == tlo);
          ok = ok && (good || !need);
        }
        if (wall(ok)) break;
        if (++spins > (1u << 20)) break;
        __builtin_amdgcn_s_sleep(2);
      }
      const cpx b128 = csel(w != 0, a128[1], a128[0]);
      const cpx a256 = cmul(b128, b128);
      const cpx a512 = cmul(a256, a256);
      cpx acc = {0.f, 0.f};
#pragma unroll
      for (int m = 0; m < 4; m++) {
        const bool need = (4 * m + q) < j;
        cpx e = {__builtin_bit_cast(float, gr[m].x << 8), __builtin_bit_cast(float, gr[m].z << 8)};
        acc = csel(need, cfma(a512, acc, e), acc);
      }
      const int dd = (j - 1 - q) & 3;
      cpx one = {1.f, 0.f};
      cpx fz = cmul(csel(dd & 1, b128, one), csel(dd & 2, a256, one));
      acc = cmul(acc, fz);
      *(LAS f32x2*)(lds + L_HIN + ((w * 16 + i16) * 4 + q) * 8) = f32x2{acc.r, acc.i};
      cnt_arrive(lds, C_HIN);
    }
    cnt_wait(lds, C_HIN, 2);
    {
      LAS char* ht = lds + L_HT + w * 2048;
      unsigned hpk[2][2][2][2];
#pragma unroll
      for (int nb = 0; nb < 2; nb++) {
        const f32x4 h01 = *(LAS f32x4*)(lds + L_HIN + (nb * 16 + i16) * 32);
        const f32x4 h23 = *(LAS f32x4*)(lds + L_HIN + (nb * 16 + i16) * 32 + 16);
        const cpx acc = {(h01[0] + h01[2]) + (h23[0] + h23[2]), (h01[1] + h01[3]) + (h23[1] + h23[3])};
        cpx h = cfma(pw[nb], acc, cl[nb]);
        float orr[8], oi[8];
#pragma unroll
        for (int k = 0; k < 8; k++) {
          h = cfma(a1[nb], h, u[nb][k]);
          orr[k] = cc[nb].r * h.r - cc[nb].i * h.i;
          oi[k] = -(cc[nb].r * h.i + cc[nb].i * h.r);
        }
#pragma unroll
        for (int b = 0; b < 2; b++) {
          hpk[nb][0][b][0] = pk_bf16(orr[4 * b], orr[4 * b + 1]); hpk[nb][0][b][1] = pk_bf16(orr[4 * b + 2], orr[4 * b + 3]);
          hpk[nb][1][b][0] = pk_bf16(oi[4 * b], oi[4 * b + 1]);   hpk[nb][1][b][1] = pk_bf16(oi[4 * b + 2], oi[4 * b + 3]);
        }
      }
      const unsigned zy = zb + Y_OFF + (unsigned)(4 * (s >> 1) + (s & 1)) * 4096u + (unsigned)(2 * (w & 1)) * 1024u;
#pragma unroll
      for (int b = 0; b < 2; b++) {
#pragma unroll
        for (int nb = 0; nb < 2; nb++)
#pragma unroll
          for (int part = 0; part < 2; part++)
            *(LAS v2u*)(ht + (part * 32 + nb * 16 + i16) * 32 + q * 8) = v2u{hpk[nb][part][b][0], hpk[nb][part][b][1]};
#pragma unroll
        for (int part = 0; part < 2; part++) {
          LAS char* rp = ht + (part * 32 + 8 * q + ta) * 32 + tp * 8;
          v4u v = lds_tr(rp, rp + 4 * 32);
          st16(rs, (unsigned)WS_Z + zy + (unsigned)part * 8192u + b * 1024u, v);
        }
      }
    }
    CLOAD(4)
    wait_vm<16>();
    if (cnt_arrive(lds, C_HS) == 3 && l < 16)
      __hip_atomic_store((GAS u64*)(p.ws + WS_FLAG) + (j * 16 + s) * 16 + l, epoch, RLX_AGENT);
    CWAIT(4, 0)
    CXPOSE(4)
    cnt_arrive(lds, C_CX);
    EPI_LOADS()
    poll_flags(p.ws + WS_FLAG + (size_t)(j * 16 + 4 * w) * 128, l, epoch);
    asm volatile("" : "+v"(xe[0]), "+v"(xe[1]), "+v"(dk));
    f32x4 acc[4][4];
#pragma unroll
    for (int a = 0; a < 4; a++)
#pragma unroll
      for (int b = 0; b < 4; b++) acc[a][b] = f32x4{0, 0, 0, 0};
    v4u af[3][4];
    const char* ay = ab + Y_OFF + (size_t)(8 * w) * 4096;
#define YL(K, i) gld16_sc1<(i) * 1024>(af[(K) % 3][i], avoff, ay + ((((K) + 2 * rot2) & 7) * 4096))
#define YST(K, tb)                                                                                  \
  {                                                                                                 \
    _Pragma("unroll") for (int nb = 0; nb < 4; nb++) acc[nb][tb] = mfma16(cf[K][nb], af[(K) % 3][tb], acc[nb][tb]); \
    SB0();                                                                                          \
    if ((K) + 2 < 8) YL((K) + 2, tb);                                                               \
    SB0();                                                                                          \
  }
#define YS(K)                                                                                       \
  {                                                                                                 \
    wait_vm4<((K) < 7 ? 4 : 0)>(af[(K) % 3][0], af[(K) % 3][1], af[(K) % 3][2], af[(K) % 3][3]);    \
    YST(K, 0) YST(K, 1) YST(K, 2) YST(K, 3)                                                         \
  }
    YL(0, 0); YL(0, 1); YL(0, 2); YL(0, 3); YL(1, 0); YL(1, 1); YL(1, 2); YL(1, 3);
    YS(0) YS(1) YS(2) YS(3) YS(4) YS(5) YS(6) YS(7)
#undef YL
#undef YS
#undef YST
    cnt_wait(lds, C_CX, 4);
    cnt_wait(lds, C_B2, 4);
    {
      LAS char* yb = lds + ((w & 1) ? L_YB1 : L_YB0);
      if (w < 2) cnt_wait(lds, C_Y1 + (w & 1), 1);
#pragma unroll
      for (int tb = 0; tb < 4; tb++) {
        const int tk = 32 * (tb >> 1) + 8 * (i16 >> 2) + 4 * (tb & 1) + (i16 & 3);
#pragma unroll
        for (int nb = 0; nb < 4; nb++) {
          LAS f32x4* d = (LAS f32x4*)(yb + tk * 256 + (((4 * nb + q) ^ i16) & 15) * 16);
          if (w < 2) *d = *d + acc[nb][tb]; else *d = acc[nb][tb];
        }
      }
      if (w >= 2) cnt_arrive(lds, C_Y1 + (w & 1));
    }
  } else {
    v4u xf[2][16];
    f32x4 ah[7][2];
#pragma unroll
    for (int mb = 0; mb < 7; mb++) { ah[mb][0] = f32x4{0, 0, 0, 0}; ah[mb][1] = f32x4{0, 0, 0, 0}; }
    unsigned pt[7];
#pragma unroll
    for (int mb = 0; mb < 7; mb++) pt[mb] = opaque(ldsb + L_W1A + tro + (unsigned)(((mb + odd) ^ trkey) * 32));
    const int m1 = w < 3 ? w + 4 : 6;
    const unsigned hq0 = opaque(ldsb + L_W1A + tro + (unsigned)(((w + odd) ^ trkey) * 32));
    const unsigned hq1 = opaque(ldsb + L_W1A + tro + (unsigned)(((m1 + odd) ^ trkey) * 32));
    f32x4 sh0 = {0, 0, 0, 0}, sh1 = {0, 0, 0, 0};
    {
      LAS char* px0 = lp(opaque(ldsb + xo + sw0)); LAS char* px1 = lp(opaque(ldsb + xo + sw1));
#define L1IT(I, NW)                                                                                   \
  {                                                                                                   \
    wait_vm<NW>();                                                                                    \
    __builtin_amdgcn_s_barrier();                                                                     \
    asm volatile("" ::: "memory");                                                                    \
    xf[0][2 * (I)] = lds_rd16(px0 + ((I) & 3) * STG); xf[1][2 * (I)] = lds_rd16(px0 + ((I) & 3) * STG + 3072); \
    xf[0][2 * (I) + 1] = lds_rd16(px1 + ((I) & 3) * STG); xf[1][2 * (I) + 1] = lds_rd16(px1 + ((I) & 3) * STG + 3072); \
    SB0();                                                                                            \
    if ((I) < 3 || (I) == 7) {                                                                        \
      if ((I) < 6) { PALL((I) + 2) }                                                                  \
      W1P(I, 0); W1P(I, 1); W1P(I, 2); W1P(I, 3);                                                     \
    } else {                                                                                          \
      constexpr int c_ = (I) - 3;                                                                     \
      v4u wa[7], wb_[7], h0[2], h1[2];                                                                \
      _Pragma("unroll") for (int mb = 0; mb < 7; mb++) wa[mb] = lds_tr(lp(pt[mb]) + c_ * 16384, lp(pt[mb]) + c_ * 16384 + 1024); \
      SB0();                                                                                          \
      if ((I) < 6) { PIECE(0, (I) + 2) PIECE(1, (I) + 2) PIECE(2, (I) + 2) }                          \
      SB0();                                                                                          \
      _Pragma("unroll") for (int mb = 0; mb < 7; mb++) {                                              \
        ah[mb][0] = mfma16(wa[mb], xf[0][2 * c_], ah[mb][0]);                                         \
        ah[mb][1] = mfma16(wa[mb], xf[1][2 * c_], ah[mb][1]);                                         \
        wb_[mb] = lds_tr(lp(pt[mb]) + c_ * 16384 + 8192, lp(pt[mb]) + c_ * 16384 + 9216);             \
        if ((I) < 6) { if (mb == 1) PIECE(3, (I) + 2) if (mb == 3) PIECE(4, (I) + 2) if (mb == 5) PIECE(5, (I) + 2) } \
        SB0();                                                                                        \
      }                                                                                               \
      _Pragma("unroll") for (int mb = 0; mb < 7; mb++) {                                              \
        ah[mb][0] = mfma16(wb_[mb], xf[0][2 * c_ + 1], ah[mb][0]);                                    \
        ah[mb][1] = mfma16(wb_[mb], xf[1][2 * c_ + 1], ah[mb][1]);                                    \
        if (mb == 0) { h0[0] = lds_tr(lp(hq0) + c_ * 16384, lp(hq0) + c_ * 16384 + 1024); h1[0] = lds_tr(lp(hq1) + c_ * 16384, lp(hq1) + c_ * 16384 + 1024); } \
        if (mb == 1) { h0[1] = lds_tr(lp(hq0) + c_ * 16384 + 8192, lp(hq0) + c_ * 16384 + 9216); h1[1] = lds_tr(lp(hq1) + c_ * 16384 + 8192, lp(hq1) + c_ * 16384 + 9216); } \
        if (mb == 0) W1P(I, 0); if (mb == 2) W1P(I, 1); if (mb == 4) W1P(I, 2); if (mb == 6) W1P(I, 3); \
        SB0();                                                                                        \
      }                                                                                               \
      sh0 = mfma16(h0[0], ones, sh0); sh1 = mfma16(h1[0], ones, sh1);                                 \
      sh0 = mfma16(h0[1], ones, sh0); sh1 = mfma16(h1[1], ones, sh1);                                 \
    }                                                                                                 \
    wait_lgkm();                                                                                      \
  }
      L1IT(0, 6) L1IT(1, 10) L1IT(2, 14) L1IT(3, 14) L1IT(4, 14) L1IT(5, 14) L1IT(6, 14) L1IT(7, 8)
#undef L1IT
    }
    wait_vm<8>();
    bar();
    const unsigned ring = ldsb + L_RING + w * RING;
    unsigned fv;
    {
      const int row = l >> 3, c16p = l & 7;
      fv = (unsigned)(row * 1024 + ct * 128 + ((((c16p >> 1) ^ ((row >> 1) & 3)) << 1) | (c16p & 1)) * 16);
    }
    const char* w2b = p.w2 + (size_t)(448 * w) * 1024;
#define WP(N, n) dma1(ring + ((N) & 1) * 14336 + (n) * 1024, w2b + (size_t)(((N) + rot2) & 3) * (112 * 1024) + (n) * 8192, fv)
    {
      v4u wd[7];
      static_for(std::make_integer_sequence<int, 7>{}, [&](auto NC) __attribute__((always_inline)) NOPK {
        constexpr int n = decltype(NC)::value, T2 = hv_t(n), m2 = hv_m(n);
        wd[n] = lds_tr(lp(pt[m2]) + (T2 & 7) * 8192, lp(pt[m2]) + (T2 & 7) * 8192 + 1024);
      });
      const unsigned zf = zb + (unsigned)s * 14336u;
      float nm[2], r2[2];
      v4u hs0, hs1;
      unsigned pkd[7][2][2];
      auto epi = [&](auto MB, auto BB) __attribute__((always_inline)) NOPK {
        constexpr int mb = decltype(MB)::value, b = decltype(BB)::value;
        const f32x4 sh = *(LAS f32x4*)(lds + L_SH + (mb * 16 + 4 * q) * 4);
        float y[4];
#pragma unroll
        for (int e = 0; e < 4; e++) {
          const float d = fmaf(sh[e], nm[b], ah[mb][b][e]);
          y[e] = d * __builtin_amdgcn_rcpf(1.f + __builtin_amdgcn_exp2f(d * r2[b]));
        }
        pkd[mb][b][0] = pk_bf16(y[0], y[1]); pkd[mb][b][1] = pk_bf16(y[2], y[3]);
        if constexpr ((mb & 1) == 1) {
          v4u v = {pkd[mb - 1][b][0], pkd[mb - 1][b][1], pkd[mb][b][0], pkd[mb][b][1]};
          st16(rs, (unsigned)WS_Z + zf + (unsigned)(mb >> 1) * 4096u + (unsigned)(2 * (w & 1) + b) * 1024u, v);
        }
      };
      static_for(std::make_integer_sequence<int, 56>{}, [&](auto NC) __attribute__((always_inline)) NOPK {
        constexpr int n = decltype(NC)::value, T = hv_t(n), mb = hv_m(n), sl = n % 7;
        if constexpr (n == 9) {
          wait_vm<0>();
          cnt_arrive(lds, C_W);
          cnt_wait(lds, C_W, 4);
        }
        if constexpr (n < 32 && (n & 3) == 0) {
          hs0 = lds_tr(lp(hq0) + (n / 4) * 8192, lp(hq0) + (n / 4) * 8192 + 1024);
          hs1 = lds_tr(lp(hq1) + (n / 4) * 8192, lp(hq1) + (n / 4) * 8192 + 1024);
        }
        if constexpr (n == 32) {
          if (i16 == 0) {
            *(LAS f32x4*)(lds + L_SH + (w * 16 + 4 * q) * 4) = sh0;
            if (w < 3) *(LAS f32x4*)(lds + L_SH + (m1 * 16 + 4 * q) * 4) = sh1;
          }
          cnt_arrive(lds, C_SH);
          cnt_wait(lds, C_SH, 4);
          const f32x2 s0 = *(LAS f32x2*)(lds + L_STAT + (w * 32 + i16) * 8), s1 = *(LAS f32x2*)(lds + L_STAT + (w * 32 + 16 + i16) * 8);
          nm[0] = -s0[0]; nm[1] = -s1[0]; r2[0] = s0[1] * -1.44269504088896f; r2[1] = s1[1] * -1.44269504088896f;
        }
        ah[mb][0] = mfma16(wd[sl], xf[0][T], ah[mb][0]);
        ah[mb][1] = mfma16(wd[sl], xf[1][T], ah[mb][1]);
        if constexpr (n < 32 && (n & 3) == 3) { sh0 = mfma16(hs0, ones, sh0); sh1 = mfma16(hs1, ones, sh1); }
        if constexpr (n + 7 < 56) {
          constexpr int T2 = hv_t(n + 7), m2 = hv_m(n + 7);
          wd[sl] = lds_tr(lp(pt[m2]) + (T2 & 7) * 8192, lp(pt[m2]) + (T2 & 7) * 8192 + 1024);
        }
        if constexpr (n >= 32 && (n - 32) % 3 == 1) {
          constexpr int u = (n - 32) / 3;
          epi(std::integral_constant<int, (u >> 2) * 2 + (u & 1)>{}, std::integral_constant<int, (u >> 1) & 1>{});
        }
        SB0();
      });
      cnt_arrive(lds, C_B2);
      epi(std::integral_constant<int, 4>{}, std::integral_constant<int, 0>{});
      epi(std::integral_constant<int, 5>{}, std::integral_constant<int, 0>{});
      epi(std::integral_constant<int, 4>{}, std::integral_constant<int, 1>{});
      epi(std::integral_constant<int, 5>{}, std::integral_constant<int, 1>{});
      epi(std::integral_constant<int, 6>{}, std::integral_constant<int, 0>{});
      epi(std::integral_constant<int, 6>{}, std::integral_constant<int, 1>{});
      v4u v = {pkd[6][0][0], pkd[6][0][1], pkd[6][1][0], pkd[6][1][1]};
      st16(rs, (unsigned)WS_Z + zf + 12288u + (unsigned)(w & 1) * 1024u, v);
    }
    wait_vm<0>();
    if (cnt_arrive(lds, C_FS) == 3 && l < 16)
      __hip_atomic_store((GAS u64*)(p.ws + WS_FLAG) + (256 + j * 16 + s) * 16 + l, epoch, RLX_AGENT);
    cnt_wait(lds, C_B2, 4);
    cnt_wait(lds, C_HS, 4);
#pragma unroll
    for (int n = 0; n < 14; n++) WP(0, n);
    EPI_LOADS()
    poll_flags(p.ws + WS_FLAG + (size_t)(256 + j * 16 + 4 * w) * 128, l, epoch);
    asm volatile("" : "+v"(xe[0]), "+v"(xe[1]), "+v"(dk));
    f32x4 acc[4][4];
#pragma unroll
    for (int a = 0; a < 4; a++)
#pragma unroll
      for (int b = 0; b < 4; b++) acc[a][b] = f32x4{0, 0, 0, 0};
    v4u af[4][4];
    const char* afb = ab + (size_t)(4 * w) * 14336;
    unsigned ftr[4];
#pragma unroll
    for (int nb = 0; nb < 4; nb++) {
      const int row = 4 * q + ta;
      ftr[nb] = opaque(ring + (unsigned)(row * 128 + ((nb ^ ((row >> 1) & 3)) * 32) + tp * 8));
    }
#define ALD(U, i) gld16_sc1<(i) * 1024>(af[(U) & 3][i], avoff, afb + (size_t)((((U) >> 2) + rot2) & 3) * 14336 + ((U) & 3) * 4096)
#define ALDS(U, V, t)                             \
  {                                                                                                 \
    if ((V) < 16) {                                                                                 \
      if (0 * usz(U) / usz(V) == (t)) ALD(V, 0);                                                    \
      if (1 * usz(U) / usz(V) == (t)) ALD(V, 1);                                                    \
      if (usz(V) == 4 && 2 * usz(U) / usz(V) == (t)) ALD(V, 2);                                     \
      if (usz(V) == 4 && 3 * usz(U) / usz(V) == (t)) ALD(V, 3);                                     \
    }                                                                                               \
  }
#define WRD(U)                                           \
  {                                                                                                 \
    _Pragma("unroll") for (int nb = 0; nb < 4; nb++) {                                              \
      LAS char* b_ = lp(ftr[nb]) + (((U) >> 2) & 1) * 14336 + ((U) & 3) * 4096;                     \
      if (((U) & 3) == 3) { v2u t_ = lds_tr1(b_); wf[(U) & 1][nb].x = t_.x; wf[(U) & 1][nb].y = t_.y; } \
      else wf[(U) & 1][nb] = lds_tr(b_, b_ + 2048);                                                 \
    }                                                                                               \
  }
#define FSTEP(U, t)                                                                                 \
  {                                                                                                 \
    if (((U) & 3) < 3) {                                                                            \
      _Pragma("unroll") for (int nb = 0; nb < 4; nb++)                                              \
          acc[nb][t] = mfma16(wf[(U) & 1][nb], af[(U) & 3][t], acc[nb][t]);                         \
    } else {                                                                                        \
      _Pragma("unroll") for (int nb = 0; nb < 4; nb++) {                                            \
        const v2u wh_ = {wf[(U) & 1][nb].x, wf[(U) & 1][nb].y};                                     \
        acc[nb][(2 * (t)) & 3] = mfma16h(wh_, v2u{af[3][t].x, af[3][t].y}, acc[nb][(2 * (t)) & 3]); \
        acc[nb][(2 * (t) + 1) & 3] = mfma16h(wh_, v2u{af[3][t].z, af[3][t].w}, acc[nb][(2 * (t) + 1) & 3]); \
      }                                                                                             \
    }                                                                                               \
    SB0();                                                                                          \
    ALDS(U, (U) + FD, t)                                                                            \
    if ((U) < 12) WP(((U) >> 2) + 1, 4 * ((U) & 3) + (t));                                          \
    if ((t) == usz(U) - 2 && (U) < 15) {                                                            \
      if ((U) + 1 >= 4) wait_vm<f_cnt(false, (U) + 1, U, (t) + 1)>();                               \
      WRD((U) + 1)                                                                                  \
    }                                                                                               \
    SB0();                                                                                          \
  }
#define FU(U)                                                                                       \
  {                                                                                                 \
    if (((U) & 3) < 3) {                                                                            \
      wait_vm4<f_cnt(true, U, U, 0)>(af[(U) & 3][0], af[(U) & 3][1], af[(U) & 3][2], af[(U) & 3][3]); \
      FSTEP(U, 0) FSTEP(U, 1) FSTEP(U, 2) FSTEP(U, 3)                                               \
    } else {                                                                                        \
      asm volatile("s_waitcnt vmcnt(%2)" : "+v"(af[3][0]), "+v"(af[3][1]) : "i"(f_cnt(true, U, U, 0)) : "memory"); \
      FSTEP(U, 0) FSTEP(U, 1)                                                                       \
    }                                                                                               \
  }
    v4u wf[2][4];
    ALD(0, 0); ALD(0, 1); ALD(0, 2); ALD(0, 3);
    ALD(1, 0); ALD(1, 1); ALD(1, 2); ALD(1, 3);
    ALD(2, 0); ALD(2, 1); ALD(2, 2); ALD(2, 3);
    static_assert(FD == 3, "window");
    WRD(0)
    FU(0) FU(1) FU(2) FU(3) FU(4) FU(5) FU(6) FU(7) FU(8) FU(9) FU(10) FU(11) FU(12) FU(13) FU(14) FU(15)
#undef FU
#undef FSTEP
#undef WRD
#undef ALD
#undef ALDS
#undef WP
    {
      LAS char* dst = lp(ring);
#pragma unroll
      for (int tb = 0; tb < 4; tb++) {
        const int tk = 32 * (tb >> 1) + 8 * (i16 >> 2) + 4 * (tb & 1) + (i16 & 3);
#pragma unroll
        for (int nb = 0; nb < 4; nb++)
          *(LAS f32x4*)(dst + tk * 256 + (((4 * nb + q) ^ i16) & 15) * 16) = acc[nb][tb];
      }
    }
  }
  {
    bar();
    if (j == 15 && s == 0 && tid == 0)
      __hip_atomic_store((GAS u64*)(p.ws + WS_EPOCH), epoch, RLX_AGENT);
    const __amdgpu_buffer_rsrc_t ro = __builtin_amdgcn_make_buffer_rsrc((void*)p.out, 0, 2048 * 512 * 4, 0x00020000);
    const float dkf[4] = {bf2f(dk.x & 0xffff), bf2f(dk.x >> 16), bf2f(dk.y & 0xffff), bf2f(dk.y >> 16)};
#pragma unroll
    for (int n = 0; n < 2; n++) {
      const int tk = et + 32 * n;
      const int key = (tk & 3) | (((tk >> 3) & 3) << 2);
      const int o = tk * 256 + ((ec ^ key) & 15) * 16;
      LAS char* fb = lds + L_RING + o;
      const f32x4 f = (*(LAS f32x4*)(fb) + *(LAS f32x4*)(fb + RING)) + (*(LAS f32x4*)(fb + 2 * RING) + *(LAS f32x4*)(fb + 3 * RING));
      const f32x4 y = *(LAS f32x4*)(lds + L_YB0 + o) + *(LAS f32x4*)(lds + L_YB1 + o);
      const int tkp = 64 * (hp2 & 1) + tk;
      const f32x2 se = *(LAS f32x2*)(lds + L_STAT + ((tkp >> 5) * 32 + ((tkp >> 2) & 1) * 16 + key) * 8);
      const float xv[4] = {bf2f(xe[n].x & 0xffff), bf2f(xe[n].x >> 16), bf2f(xe[n].y & 0xffff), bf2f(xe[n].y >> 16)};
      f32x4 o4;
#pragma unroll
      for (int e = 0; e < 4; e++) {
        const float xn = (xv[e] - se[0]) * se[1];
        const float sv = fmaf(xn, dkf[e], y[e]);
        const float fe = f[e] * se[1];
        const float g = fe * __builtin_amdgcn_rcpf(1.f + __builtin_amdgcn_exp2f(fe * -1.44269504088896f));
        o4[e] = fmaf(sv, g, xv[e]);
      }
      __builtin_amdgcn_raw_buffer_store_b128(__builtin_bit_cast(v4u, o4), ro, (int)(((64 * hp2 + tk) * 512 + 64 * ct + 4 * ec) * 4), 0, 16);
    }
  }
}

extern "C" void kernel_launch(void* const* d_in, const int* in_sizes, int n_in, void* d_out, int out_size, void* d_ws,
                              size_t ws_size, hipStream_t stream) {
  Params p;
  p.x = (const char*)d_in[0]; p.lre = (const char*)d_in[1]; p.lim = (const char*)d_in[2];
  p.bre = (const char*)d_in[3]; p.bim = (const char*)d_in[4]; p.cre = (const char*)d_in[5]; p.cim = (const char*)d_in[6];
  p.logdt = (const char*)d_in[7]; p.dskip = (const char*)d_in[8]; p.w1 = (const char*)d_in[10]; p.w2 = (const char*)d_in[11];
  p.out = (float*)d_out; p.ws = (char*)d_ws;
  (void)hipFuncSetAttribute((const void*)k_fused, hipFuncAttributeMaxDynamicSharedMemorySize, LDS_BYTES);
  k_fused<<<256, 512, LDS_BYTES, stream>>>(p);
}
```
